```python
import torch

def _softmax(x, axis=-1):
    return torch.softmax(x, dim=axis)
def _gelu(x):
    return torch.nn.functional.gelu(x, approximate="tanh")
def _silu(x):
    return torch.nn.functional.silu(x)
def _sigmoid(x):
    return torch.sigmoid(x)
def _layernorm(x, axis=-1, eps=1e-5):
    m = x.mean(dim=axis, keepdim=True)
    v = x.var(dim=axis, keepdim=True, unbiased=False)
    return (x - m) / torch.sqrt(v + eps)
def _scatter_add(tgt, idx, src, axis=0):
    return tgt.clone().index_add_(axis, idx, src)
def _segment_sum(src, seg, n):
    out = torch.zeros((n,) + src.shape[1:], dtype=src.dtype)
    return out.index_add_(0, seg, src)
def _pad(x, pw):
    flat = tuple(v for pair in reversed(pw) for v in pair)
    return torch.nn.functional.pad(x, flat)

kernel_name = 'sequential_preLN__sliding_local_dilated__dense_activation_gelu__prompt'

def setup_inputs():

    _w = (lambda a: (lambda t: t.to(torch.bfloat16) if t.is_floating_point() else t)(torch.as_tensor(a)))
    def _A_setup():
        B, S, D = 1, 2048, 448
        return {
            "x": _w((lambda *s: torch.randn(*s, dtype=torch.bfloat16) * 0.05)(B, S, D)),
            "wqkv": _w((lambda *s: torch.randn(*s, dtype=torch.bfloat16) * 0.05)(D, 3 * D)),
            "wo": _w((lambda *s: torch.randn(*s, dtype=torch.bfloat16) * 0.05)(D, D)),
        }
    _A_in = _A_setup()

    def _B_setup():
        B, S, D, F = 1, 2048, 448, 1792
        return {
            "x": _w((lambda *s: torch.randn(*s, dtype=torch.bfloat16) * 0.05)(B, S, D)),
            "w1": _w((lambda *s: torch.randn(*s, dtype=torch.bfloat16) * 0.8)(D, F) * (D ** -0.5)),
            "b1": _w((lambda *s: torch.randn(*s, dtype=torch.bfloat16) * 0.8)(F) * 0.01),
            "w2": _w((lambda *s: torch.randn(*s, dtype=torch.bfloat16) * 0.8)(F, D) * (F ** -0.5)),
            "b2": _w((lambda *s: torch.randn(*s, dtype=torch.bfloat16) * 0.8)(D) * 0.01),
        }
    _B_in = _B_setup()

    return {
        "A_x": _A_in["x"],
        "A_wqkv": _A_in["wqkv"],
        "A_wo": _A_in["wo"],
        "B_x": _B_in["x"],
        "B_w1": _B_in["w1"],
        "B_b1": _B_in["b1"],
        "B_w2": _B_in["w2"],
        "B_b2": _B_in["b2"],
    }

def reference(A_x, A_wqkv, A_wo, B_x, B_w1, B_b1, B_w2, B_b2):
    def _A_ref(x, wqkv, wo):
        B, S, D = x.shape
        H = 16; Dh = D // H
        W, dil = 8, 2
        qkv = (lambda x, *d: x.permute(*d))((x @ wqkv).reshape(B, S, 3, H, Dh), 2, 0, 3, 1, 4)
        q, k, v = qkv[0], qkv[1], qkv[2]
        scale = Dh ** -0.5
        pos = torch.arange(S)
        off = torch.arange(W) * dil
        idx = pos[:, None] - off[None, :]
        mask = idx >= 0
        idx_c = torch.where(mask, idx, 0).reshape(-1)
        kg = k[:, :, idx_c, :].reshape(B, H, S, W, Dh)
        vg = v[:, :, idx_c, :].reshape(B, H, S, W, Dh)
        logits = torch.einsum("bhsd,bhswd->bhsw", q, kg) * scale
        logits = torch.where(mask[None, None, :, :], logits, -1e9)
        attn = _softmax(logits, axis=-1)
        o = torch.einsum("bhsw,bhswd->bhsd", attn, vg)
        return (lambda x, *d: x.permute(*d))(o, 0, 2, 1, 3).reshape(B, S, D) @ wo

    def _B_ref(x, w1, b1, w2, b2):
        h = torch.einsum("bsd,df->bsf", x, w1) + b1
        h = _gelu(h)
        return torch.einsum("bsf,fd->bsd", h, w2) + b2

    x = A_x
    h = x + _A_ref(_layernorm(x, axis=-1), A_wqkv, A_wo)
    h2 = h + _B_ref(_layernorm(h, axis=-1), B_w1, B_b1, B_w2, B_b2)
    return h2
```

```cpp
#include <hip/hip_runtime.h>
#include <stdint.h>

typedef __bf16 bf16x8 __attribute__((ext_vector_type(8)));
typedef __bf16 bf16x4 __attribute__((ext_vector_type(4)));
typedef __bf16 bf16x2 __attribute__((ext_vector_type(2)));
typedef float f32x4 __attribute__((ext_vector_type(4)));
typedef float f32x2 __attribute__((ext_vector_type(2)));
typedef unsigned u32x4 __attribute__((ext_vector_type(4)));
typedef unsigned u32x2 __attribute__((ext_vector_type(2)));
#define LAS __attribute__((address_space(3)))
#define DI __device__ __forceinline__
typedef __amdgpu_buffer_rsrc_t rsrc_t;

constexpr int S_ = 2048, D_ = 448, F_ = 1792;
constexpr unsigned O_OFF = 1u << 20, H_OFF = 4u << 20, G_OFF = 6u << 20;
constexpr unsigned O_BYTES = 128u * 16 * 1024, H_BYTES = 128u * 14 * 1024, G_BYTES = 128u * 56 * 1024;
constexpr int LDS_BYTES = 163840;

DI int rfl(int v) { return __builtin_amdgcn_readfirstlane(v); }
DI rsrc_t mk_rsrc(const void* p, unsigned bytes) { return __builtin_amdgcn_make_buffer_rsrc((void*)p, (short)0, (int)bytes, 0x00020000); }
DI void dma16(unsigned ldsaddr, unsigned voff, rsrc_t srd, unsigned soff) {
    asm volatile("s_mov_b32 m0, %0\n\ts_nop 0\n\tbuffer_load_dwordx4 %1, %2, %3 offen lds" ::"s"(ldsaddr), "v"(voff), "s"(srd), "s"(soff) : "memory");
}
template <int N> DI void wait_vm() { asm volatile("s_waitcnt vmcnt(%0)" ::"i"(N) : "memory"); }
DI void barrier_lds() { asm volatile("s_waitcnt lgkmcnt(0)" ::: "memory"); __builtin_amdgcn_s_barrier(); asm volatile("" ::: "memory"); }
DI u32x4 ld16(rsrc_t r, unsigned voff, unsigned soff) { return __builtin_amdgcn_raw_buffer_load_b128(r, voff, soff, 0); }
DI u32x2 ld8(rsrc_t r, unsigned voff, unsigned soff) { return __builtin_amdgcn_raw_buffer_load_b64(r, voff, soff, 0); }
DI void st16(u32x4 v, rsrc_t r, unsigned voff, unsigned soff) { __builtin_amdgcn_raw_buffer_store_b128(v, r, voff, soff, 0); }
DI void st8(u32x2 v, rsrc_t r, unsigned voff, unsigned soff) { __builtin_amdgcn_raw_buffer_store_b64(v, r, voff, soff, 0); }
DI bf16x8 as_frag(u32x4 v) { return __builtin_bit_cast(bf16x8, v); }
DI bf16x8 lds_frag(LAS char* p) { return *(LAS bf16x8*)p; }
DI bf16x8 lds_tr(LAS char* p0, LAS char* p1) {
    bf16x4 a = __builtin_amdgcn_ds_read_tr16_b64_v4bf16((LAS bf16x4*)p0);
    bf16x4 b = __builtin_amdgcn_ds_read_tr16_b64_v4bf16((LAS bf16x4*)p1);
    return __builtin_shufflevector(a, b, 0, 1, 2, 3, 4, 5, 6, 7);
}
DI f32x4 mfma(bf16x8 a, bf16x8 b, f32x4 c) { return __builtin_amdgcn_mfma_f32_16x16x32_bf16(a, b, c, 0, 0, 0); }
DI unsigned pk(float a, float b) { f32x2 v = {a, b}; bf16x2 r = __builtin_convertvector(v, bf16x2); return __builtin_bit_cast(unsigned, r); }
DI float bf_lo(unsigned v) { return __builtin_bit_cast(float, v << 16); }
DI float bf_hi(unsigned v) { return __builtin_bit_cast(float, v & 0xffff0000u); }
DI float dot2(unsigned a, unsigned b, float c) { return __builtin_amdgcn_fdot2_f32_bf16(__builtin_bit_cast(bf16x2, a), __builtin_bit_cast(bf16x2, b), c, false); }
DI unsigned f2u(float v) { return __builtin_bit_cast(unsigned, v); }
DI float u2f(unsigned v) { return __builtin_bit_cast(float, v); }
DI float row4_sum(float v) {
    auto a = __builtin_amdgcn_permlane16_swap(f2u(v), f2u(v), false, false); unsigned a0 = a[0], a1 = a[1];
    float s = u2f(a0) + u2f(a1);
    auto b = __builtin_amdgcn_permlane32_swap(f2u(s), f2u(s), false, false); unsigned b0 = b[0], b1 = b[1];
    return u2f(b0) + u2f(b1);
}
DI float row4_max(float v) {
    auto a = __builtin_amdgcn_permlane16_swap(f2u(v), f2u(v), false, false); unsigned a0 = a[0], a1 = a[1];
    float s = fmaxf(u2f(a0), u2f(a1));
    auto b = __builtin_amdgcn_permlane32_swap(f2u(s), f2u(s), false, false); unsigned b0 = b[0], b1 = b[1];
    return fmaxf(u2f(b0), u2f(b1));
}
DI void stats_acc(bf16x8 f, float& s1, float& s2) {
    u32x4 u = __builtin_bit_cast(u32x4, f);
    unsigned a = u.x, b = u.y, c = u.z, d = u.w;
    s1 = dot2(a, 0x3F803F80u, s1); s2 = dot2(a, a, s2);
    s1 = dot2(b, 0x3F803F80u, s1); s2 = dot2(b, b, s2);
    s1 = dot2(c, 0x3F803F80u, s1); s2 = dot2(c, c, s2);
    s1 = dot2(d, 0x3F803F80u, s1); s2 = dot2(d, d, s2);
}
DI void stats_fin(float s1, float s2, float& mu, float& rstd) {
    s1 = row4_sum(s1); s2 = row4_sum(s2);
    mu = s1 * (1.0f / D_);
    float var = fmaxf(s2 * (1.0f / D_) - mu * mu, 0.0f);
    rstd = __builtin_amdgcn_rsqf(var + 1e-5f);
}

constexpr int P1_XIMG = 0, P1_RING = 71680, P1_SLOT = 22528, P1_CS = P1_RING + 4 * P1_SLOT;
__global__ __launch_bounds__(512, 2) void k_p1(const char* __restrict__ x, const char* __restrict__ wqkv, char* __restrict__ ws) {
    extern __shared__ __attribute__((aligned(16))) char smem_[];
    LAS char* smem = (LAS char*)smem_;
    const unsigned lbase = (unsigned)(size_t)smem;
    const int tid = threadIdx.x, lane = tid & 63, w = rfl(tid >> 6);
    const int b = blockIdx.x, slot = b >> 3, T64 = (b & 7) * 4 + (slot >> 3), hp = slot & 7;
    const int t0 = T64 * 64;
    const rsrc_t rX = mk_rsrc(x, S_ * D_ * 2), rW = mk_rsrc(wqkv, D_ * 3 * D_ * 2), rO = mk_rsrc(ws + O_OFF, O_BYTES);
    {
        const int i = lane >> 3, j = lane & 7;
#pragma unroll
        for (int m = 0; m < 9; ++m) {
            const int u = w + 8 * m;
            if (u < 70) {
                const int rb = u / 7, cb = u - rb * 7;
                const int rho = 8 * rb + i;
                int tok = t0 - 16 + rho; tok = tok < 0 ? 0 : tok;
                const unsigned voff = tok * 896 + (8 * cb + (j ^ ((rho >> 1) & 7))) * 16;
                if (rho != 0) dma16(lbase + P1_XIMG + u * 1024, voff, rX, 0);
            }
        }
        if (w == 0 && lane < 56) { u32x4 ones = {0x3F803F80u, 0x3F803F80u, 0x3F803F80u, 0x3F803F80u}; *(LAS u32x4*)(smem + P1_XIMG + (lane >> 3) * 1024 + (lane & 7) * 16) = ones; }
    }
    unsigned wv[3]; unsigned wl[3];
#pragma unroll
    for (int m = 0; m < 3; ++m) {
        int u = w + 8 * m; u = u > 21 ? 21 : u;
        const int c = 64 * u + lane, rs = c / 22; int cc = c - rs * 22; cc = cc > 20 ? 20 : cc;
        const int q = rs & 31, bb = q >> 3, ii = q & 7;
        const int krow = (rs & 32) + (bb & 1) * 16 + (bb >> 1) * 4 + (ii & 3) + 8 * (ii >> 2);
        const int sec = cc / 7;
        wv[m] = krow * 2688 + (sec == 2 ? 0 : (sec + 1) * 896) + hp * 112 + (cc - sec * 7) * 16;
        wl[m] = lbase + P1_RING + u * 1024;
    }
#pragma unroll
    for (int st = 0; st < 4; ++st)
#pragma unroll
        for (int m = 0; m < 3; ++m) dma16(wl[m] + st * P1_SLOT, wv[m], rW, st * 64 * 2688);

    const int ng = w & 3, tg = w >> 2;
    const int t = lane & 15, g = lane >> 4;
    const int sg = (t >> 1) & 7;
    LAS char* xb0 = smem + P1_XIMG + (t >> 3) * 7168 + (t & 7) * 128 + ((g ^ sg) * 16) + tg * 3 * 14336;
    LAS char* xb1 = smem + P1_XIMG + (t >> 3) * 7168 + (t & 7) * 128 + (((4 + g) ^ sg) * 16) + tg * 3 * 14336;
    LAS char* wb = smem + P1_RING + (8 * (g >> 1) + 4 * (g & 1) + ((lane & 15) >> 2)) * 352 + 8 * (lane & 3) + ng * 96;
    f32x4 acc[3][3];
    float s1[3], s2[3];
#pragma unroll
    for (int a = 0; a < 3; ++a) { s1[a] = 0.f; s2[a] = 0.f;
#pragma unroll
        for (int i = 0; i < 3; ++i) acc[a][i] = (f32x4){0.f, 0.f, 0.f, 0.f}; }

    for (int st = 0; st < 7; ++st) {
        if (st == 0) wait_vm<9>(); else if (st <= 4) wait_vm<6>(); else if (st == 5) wait_vm<3>(); else wait_vm<0>();
        barrier_lds();
        if (st >= 1 && st <= 3) {
#pragma unroll
            for (int m = 0; m < 3; ++m) dma16(wl[m] + ((st + 3) & 3) * P1_SLOT, wv[m], rW, (st + 3) * 64 * 2688);
        }
        LAS char* ws_ = wb + (st & 3) * P1_SLOT;
#pragma unroll
        for (int ks = 0; ks < 2; ++ks) {
            LAS char* xb = (ks ? xb1 : xb0) + st * 1024;
            bf16x8 wf[3];
#pragma unroll
            for (int i = 0; i < 3; ++i) wf[i] = lds_tr(ws_ + ks * 11264 + 32 * i, ws_ + ks * 11264 + 5632 + 32 * i);
            bf16x8 xf0 = lds_frag(xb), xf1 = lds_frag(xb + 14336);
#pragma unroll
            for (int i = 0; i < 3; ++i) { acc[0][i] = mfma(wf[i], xf0, acc[0][i]); acc[1][i] = mfma(wf[i], xf1, acc[1][i]); }
            stats_acc(xf0, s1[0], s2[0]); stats_acc(xf1, s1[1], s2[1]);
            if (tg == 0) {
                bf16x8 xf2 = lds_frag(xb + 2 * 14336);
#pragma unroll
                for (int i = 0; i < 3; ++i) acc[2][i] = mfma(wf[i], xf2, acc[2][i]);
                stats_acc(xf2, s1[2], s2[2]);
            }
        }
    }
    if (tg == 0 && t == 0) {
#pragma unroll
        for (int i = 0; i < 3; ++i) if (ng * 3 + i < 11) *(LAS f32x4*)(smem + P1_CS + ((ng * 3 + i) * 16 + 4 * g) * 4) = acc[0][i];
    }
    float mu[3], rs[3];
#pragma unroll
    for (int a = 0; a < 3; ++a) stats_fin(s1[a], s2[a], mu[a], rs[a]);
    barrier_lds();
    constexpr int STG = P1_XIMG;
    if (tid < 480) {
        const int blk = tid >> 4, tt = tid & 15, arr = blk / 10;
        const int off = (arr == 1) ? tt * 64 + ((1 ^ ((tt >> 2) & 1)) * 32) + 24 : 768 + tt * 16 + 8;
        *(LAS u32x2*)(smem + STG + blk * 1024 + off) = (u32x2){0u, 0u};
    }
#pragma unroll
    for (int i = 0; i < 3; ++i) {
        const int n0 = (ng * 3 + i) * 16 + 4 * g;
        if (n0 < 168) {
            const f32x4 cs = *(LAS f32x4*)(smem + P1_CS + n0 * 4);
            const int arr = n0 / 56, rem = n0 - arr * 56, hh = rem >= 28 ? 1 : 0, dim = rem - 28 * hh;
            const int off = (arr == 1) ? t * 64 + (((dim >> 4) ^ ((t >> 2) & 1)) * 32) + (dim & 15) * 2 : (dim >> 3) * 256 + t * 16 + ((dim >> 2) & 1) * 8;
            LAS char* dst = smem + STG + ((arr * 2 + hh) * 5 + tg * 3) * 1024 + off;
#pragma unroll
            for (int a = 0; a < 3; ++a) {
                if (a < 2 || tg == 0) {
                    const float m = mu[a], r = rs[a];
                    const float v0 = (acc[a][i][0] - m * cs[0]) * r, v1 = (acc[a][i][1] - m * cs[1]) * r;
                    const float v2 = (acc[a][i][2] - m * cs[2]) * r, v3 = (acc[a][i][3] - m * cs[3]) * r;
                    *(LAS u32x2*)(dst + a * 1024) = (u32x2){pk(v0, v1), pk(v2, v3)};
                }
            }
        }
    }
    barrier_lds();
    {
        const int hh = w & 1, tq = 1 + (w >> 1);
        LAS char* kb = smem + STG + ((0 * 2 + hh) * 5 + tq - 1) * 1024;
        LAS char* vb = smem + STG + ((1 * 2 + hh) * 5 + tq - 1) * 1024;
        LAS char* qb = smem + STG + ((2 * 2 + hh) * 5 + tq) * 1024;
        const bf16x8 qf = lds_frag(qb + lane * 16);
        const bf16x8 kp = lds_frag(kb + lane * 16), kc = lds_frag(kb + 1024 + lane * 16);
        const f32x4 z = {0.f, 0.f, 0.f, 0.f};
        const f32x4 sp = mfma(kp, qf, z), sc = mfma(kc, qf, z);
        const float scl = 0.18898223650461362f * 1.4426950408889634f;
        const int kabs0 = t0 + 16 * (tq - 1) - 16;
        float zz[8];
        float mx = -3.0e38f;
#pragma unroll
        for (int e = 0; e < 8; ++e) {
            const int kap = 16 * (e >> 2) + 4 * g + (e & 3);
            const int del = 16 + t - kap;
            const bool ok = del >= 0 && del <= 14 && ((del & 1) == 0) && (kabs0 + kap >= 0);
            const float v = (e < 4 ? sp[e & 3] : sc[e & 3]) * scl;
            zz[e] = ok ? v : -3.0e38f;
            mx = fmaxf(mx, zz[e]);
        }
        mx = row4_max(mx);
        float sum = 0.f;
#pragma unroll
        for (int e = 0; e < 8; ++e) { zz[e] = __builtin_amdgcn_exp2f(zz[e] - mx); sum += zz[e]; }
        sum = row4_sum(sum);
        const float inv = __builtin_amdgcn_rcpf(sum);
        const u32x4 pu = {pk(zz[0] * inv, zz[1] * inv), pk(zz[2] * inv, zz[3] * inv), pk(zz[4] * inv, zz[5] * inv), pk(zz[6] * inv, zz[7] * inv)};
        const bf16x8 pf = as_frag(pu);
        const int row = 4 * g + ((lane & 15) >> 2);
        f32x4 od[2];
#pragma unroll
        for (int dt = 0; dt < 2; ++dt) {
            LAS char* va = vb + row * 64 + ((dt ^ (g & 1)) * 32) + 8 * (lane & 3);
            const bf16x8 vf = lds_tr(va, va + 1024);
            od[dt] = mfma(vf, pf, z);
        }
        const u32x4 ou = {pk(od[0][0], od[0][1]), pk(od[0][2], od[0][3]), pk(od[1][0], od[1][1]), pk(od[1][2], od[1][3])};
        const int T16 = 4 * T64 + tq - 1, head = 2 * hp + hh;
        st16(ou, rO, (T16 * 16 + head) * 1024 + lane * 16, 0);
    }
}

DI unsigned wimg128_lane_off(int lane) {
    const int i8 = lane >> 3, p = lane & 7;
    const int i = (p >> 1) ^ ((i8 >> 1) & 3);
    return (2 * i + (p & 1)) * 16;
}
DI unsigned wimg128_read_base(int lane) {
    const int g = lane >> 4, q = (lane & 15) >> 2, p = lane & 3;
    const int i8 = 4 * (g & 1) + q;
    return (g >> 1) * 1024 + i8 * 128 + (((i8 >> 1) & 3) * 32) + 8 * p;
}

constexpr int P2_W = 0, P2_RED = 65536;
__global__ __launch_bounds__(512, 2) void k_p2(const char* __restrict__ x, const char* __restrict__ wo, char* __restrict__ ws) {
    extern __shared__ __attribute__((aligned(16))) char smem_[];
    LAS char* smem = (LAS char*)smem_;
    const unsigned lbase = (unsigned)(size_t)smem;
    const int tid = threadIdx.x, lane = tid & 63, w = rfl(tid >> 6);
    const int b = blockIdx.x, slot = b >> 3, T64 = (b & 7) * 4 + (slot >> 3), j = slot & 7;
    if (j == 7) return;
    const rsrc_t rX = mk_rsrc(x, S_ * D_ * 2), rW = mk_rsrc(wo, D_ * D_ * 2), rO = mk_rsrc(ws + O_OFF, O_BYTES), rH = mk_rsrc(ws + H_OFF, H_BYTES);
    const int tt = w & 3, kh = w >> 2, t = lane & 15, g = lane >> 4;
    const int T16 = 4 * T64 + tt;
    {
        const unsigned lo = wimg128_lane_off(lane) + j * 128;
#pragma unroll
        for (int m = 0; m < 8; ++m) {
            const int u = w + 8 * m;
            int row = 28 * (u >> 2) + 8 * (u & 3) + (lane >> 3); row = row > D_ - 1 ? D_ - 1 : row;
            dma16(lbase + P2_W + u * 1024, row * 896 + lo, rW, 0);
        }
    }
    u32x4 of[8];
#pragma unroll
    for (int s = 0; s < 8; ++s) of[s] = ld16(rO, lane * 16, (T16 * 16 + 8 * kh + s) * 1024);
    const unsigned xo = (T16 * 16 + t) * 896 + (64 * j + 32 * kh + 4 * g) * 2;
    const u32x2 x0 = ld8(rX, xo, 0), x1 = ld8(rX, xo + 32, 0);
    __builtin_amdgcn_sched_barrier(0);
    wait_vm<10>();
    barrier_lds();
    f32x4 acc[4];
#pragma unroll
    for (int i = 0; i < 4; ++i) acc[i] = (f32x4){0.f, 0.f, 0.f, 0.f};
    const unsigned rb = wimg128_read_base(lane);
#pragma unroll
    for (int s = 0; s < 8; ++s) {
#pragma unroll
        for (int i = 0; i < 4; ++i) {
            LAS char* a = smem + P2_W + (rb ^ (i << 5)) + (8 * kh + s) * 4096;
            acc[i] = mfma(lds_tr(a, a + 2048), as_frag(of[s]), acc[i]);
        }
    }
    LAS char* red = smem + P2_RED;
    const f32x4 k0 = kh ? acc[2] : acc[0], k1 = kh ? acc[3] : acc[1];
    const f32x4 o0 = kh ? acc[0] : acc[2], o1 = kh ? acc[1] : acc[3];
    *(LAS f32x4*)(red + (w * 2 + 0) * 1024 + lane * 16) = o0;
    *(LAS f32x4*)(red + (w * 2 + 1) * 1024 + lane * 16) = o1;
    barrier_lds();
    const f32x4 r0 = *(LAS f32x4*)(red + ((w ^ 4) * 2 + 0) * 1024 + lane * 16), r1 = *(LAS f32x4*)(red + ((w ^ 4) * 2 + 1) * 1024 + lane * 16);
    const unsigned xa = x0.x, xb = x0.y, xc = x1.x, xd = x1.y;
    const u32x4 hu = {pk(bf_lo(xa) + k0[0] + r0[0], bf_hi(xa) + k0[1] + r0[1]), pk(bf_lo(xb) + k0[2] + r0[2], bf_hi(xb) + k0[3] + r0[3]),
                      pk(bf_lo(xc) + k1[0] + r1[0], bf_hi(xc) + k1[1] + r1[1]), pk(bf_lo(xd) + k1[2] + r1[2], bf_hi(xd) + k1[3] + r1[3])};
    st16(hu, rH, (T16 * 14 + 2 * j + kh) * 1024 + lane * 16, 0);
}

constexpr int P3_W = 0, P3_TAB = 114688;
__global__ __launch_bounds__(512, 2) void k_p3(const char* __restrict__ w1, const char* __restrict__ b1, char* __restrict__ ws) {
    extern __shared__ __attribute__((aligned(16))) char smem_[];
    LAS char* smem = (LAS char*)smem_;
    const unsigned lbase = (unsigned)(size_t)smem;
    const int tid = threadIdx.x, lane = tid & 63, w = rfl(tid >> 6);
    const int b = blockIdx.x, slot = b >> 3, T64 = (b & 7) * 4 + (slot >> 3), j = slot & 7;
    if (j == 7) return;
    const int js = 7 * (T64 & 1) + j, T16 = 8 * (T64 >> 1) + w;
    const rsrc_t rW = mk_rsrc(w1, D_ * F_ * 2), rB = mk_rsrc(b1, F_ * 2), rH = mk_rsrc(ws + H_OFF, H_BYTES), rG = mk_rsrc(ws + G_OFF, G_BYTES);
    const int t = lane & 15, g = lane >> 4;
    {
        const int i4 = lane >> 4, p = lane & 15;
#pragma unroll
        for (int m = 0; m < 14; ++m) {
            const int u = w + 8 * m;
            const int row = 4 * u + i4;
            const int i = (p >> 1) ^ (row & 7);
            dma16(lbase + P3_W + u * 1024, row * 3584 + js * 256 + (2 * i + (p & 1)) * 16, rW, 0);
        }
    }
    u32x4 hf[14];
#pragma unroll
    for (int s = 0; s < 14; ++s) hf[s] = ld16(rH, lane * 16, (T16 * 14 + s) * 1024);
    unsigned short bv = 0;
    if (tid < 128) bv = __builtin_amdgcn_raw_buffer_load_b16(rB, (js * 128 + tid) * 2, 0, 0);
    __builtin_amdgcn_sched_barrier(0);
    wait_vm<0>();
    if (tid < 128) *(LAS float*)(smem + P3_TAB + 512 + tid * 4) = u2f((unsigned)bv << 16);
    barrier_lds();
    const int q = (lane & 15) >> 2, p = lane & 3;
    const unsigned rb = (4 * g + q) * 256 + ((4 * (g & 1) + q) * 32) + 8 * p;
    {
        const u32x4 onesu = {0x3F803F80u, 0x3F803F80u, 0x3F803F80u, 0x3F803F80u};
        f32x4 cs = {0.f, 0.f, 0.f, 0.f};
        LAS char* a = smem + P3_W + (rb ^ (w << 5));
#pragma unroll
        for (int s = 0; s < 14; ++s) cs = mfma(lds_tr(a + s * 8192, a + s * 8192 + 4096), as_frag(onesu), cs);
        if (t == 0) *(LAS f32x4*)(smem + P3_TAB + (16 * w + 4 * g) * 4) = cs;
    }
    float s1 = 0.f, s2 = 0.f;
#pragma unroll
    for (int s = 0; s < 14; ++s) stats_acc(as_frag(hf[s]), s1, s2);
    float mu, rstd;
    stats_fin(s1, s2, mu, rstd);
    barrier_lds();
    const float nmr = -mu * rstd;
    const float c0 = -2.0f * 0.7978845608028654f * 1.4426950408889634f, c1 = c0 * 0.044715f;
#pragma unroll
    for (int pr = 0; pr < 4; ++pr) {
        unsigned gu[4];
#pragma unroll
        for (int h2 = 0; h2 < 2; ++h2) {
            const int i = 2 * pr + h2;
            LAS char* a = smem + P3_W + (rb ^ (i << 5));
            f32x4 acc = {0.f, 0.f, 0.f, 0.f};
#pragma unroll
            for (int s = 0; s < 14; ++s) acc = mfma(lds_tr(a + s * 8192, a + s * 8192 + 4096), as_frag(hf[s]), acc);
            const f32x4 cs = *(LAS f32x4*)(smem + P3_TAB + (16 * i + 4 * g) * 4);
            const f32x4 bb = *(LAS f32x4*)(smem + P3_TAB + 512 + (16 * i + 4 * g) * 4);
            float gv[4];
#pragma unroll
            for (int r = 0; r < 4; ++r) {
                const float y = fmaf(acc[r], rstd, fmaf(nmr, cs[r], bb[r]));
                const float e = __builtin_amdgcn_exp2f(y * fmaf(y * y, c1, c0));
                gv[r] = y * __builtin_amdgcn_rcpf(1.0f + e);
            }
            gu[2 * h2] = pk(gv[0], gv[1]); gu[2 * h2 + 1] = pk(gv[2], gv[3]);
        }
        st16((u32x4){gu[0], gu[1], gu[2], gu[3]}, rG, (T16 * 56 + 4 * js + pr) * 1024 + lane * 16, 0);
    }
}

constexpr int P4_RING = 0, P4_SLOT = 16384, P4_NS = 9, P4_RED = P4_NS * P4_SLOT;
__global__ __launch_bounds__(512, 2) void k_p4(const char* __restrict__ w2, const char* __restrict__ b2, char* __restrict__ ws, char* __restrict__ out) {
    extern __shared__ __attribute__((aligned(16))) char smem_[];
    LAS char* smem = (LAS char*)smem_;
    const unsigned lbase = (unsigned)(size_t)smem;
    const int tid = threadIdx.x, lane = tid & 63, w = rfl(tid >> 6);
    const int b = blockIdx.x, slot = b >> 3, T64 = (b & 7) * 4 + (slot >> 3), j = slot & 7;
    if (j == 7) return;
    const rsrc_t rW = mk_rsrc(w2, F_ * D_ * 2), rB = mk_rsrc(b2, D_ * 2), rH = mk_rsrc(ws + H_OFF, H_BYTES), rG = mk_rsrc(ws + G_OFF, G_BYTES), rOut = mk_rsrc(out, S_ * D_ * 2);
    const int tt = w & 3, kh = w >> 2, t = lane & 15, g = lane >> 4;
    const int T16 = 4 * T64 + tt;
    unsigned wv[2], wl[2];
#pragma unroll
    for (int m = 0; m < 2; ++m) {
        const int u = w + 8 * m;
        const int row = 896 * (u >> 3) + 32 * ((u >> 2) & 1) + 8 * (u & 3) + (lane >> 3);
        wv[m] = row * 896 + j * 128 + wimg128_lane_off(lane);
        wl[m] = lbase + P4_RING + u * 1024;
    }
    const u32x4 hres = ld16(rH, lane * 16, (T16 * 14 + 2 * j + kh) * 1024);
    const unsigned bo = (64 * j + 32 * kh + 4 * g) * 2;
    const u32x2 bb0 = ld8(rB, bo, 0), bb1 = ld8(rB, bo + 32, 0);
    __builtin_amdgcn_sched_barrier(0);
#pragma unroll
    for (int st = 0; st < P4_NS; ++st)
#pragma unroll
        for (int m = 0; m < 2; ++m) dma16(wl[m] + st * P4_SLOT, wv[m], rW, st * 64 * 896);
    u32x4 gf[28];
#pragma unroll
    for (int s = 0; s < 28; ++s) gf[s] = ld16(rG, lane * 16, (T16 * 56 + 28 * kh + s) * 1024);
    __builtin_amdgcn_sched_barrier(0);
    f32x4 acc[4];
#pragma unroll
    for (int i = 0; i < 4; ++i) acc[i] = (f32x4){0.f, 0.f, 0.f, 0.f};
    const unsigned rb = wimg128_read_base(lane) + kh * 8192;
#pragma unroll
    for (int st = 0; st < 14; ++st) {
        constexpr int NREF = 5;
        if (st < P4_NS) {
            const int nref = st <= 1 ? 0 : (st - 1 > NREF ? NREF : st - 1);
            const int n = (18 - 2 * (st + 1)) + 28 + 2 * nref;
            if (n > 63) wait_vm<63>();
            switch (n) {
#define C(N) case N: wait_vm<N>(); break;
                C(44) C(42) C(40) C(38) C(36) C(34) C(32) C(30) C(28) C(46) C(48) C(50) C(52) C(54)
#undef C
                default: wait_vm<0>(); break;
            }
        } else {
            const int n = 2 * (13 - st);
            switch (n) {
#define C(N) case N: wait_vm<N>(); break;
                C(8) C(6) C(4) C(2)
#undef C
                default: wait_vm<0>(); break;
            }
        }
        barrier_lds();
        if (st >= 1 && st + 8 <= 13) {
#pragma unroll
            for (int m = 0; m < 2; ++m) dma16(wl[m] + ((st + 8) % P4_NS) * P4_SLOT, wv[m], rW, (st + 8) * 64 * 896);
        }
#pragma unroll
        for (int ks = 0; ks < 2; ++ks) {
#pragma unroll
            for (int i = 0; i < 4; ++i) {
                LAS char* a = smem + P4_RING + (st % P4_NS) * P4_SLOT + ks * 4096 + (rb ^ (i << 5));
                acc[i] = mfma(lds_tr(a, a + 2048), as_frag(gf[2 * st + ks]), acc[i]);
            }
        }
    }
    LAS char* red = smem + P4_RED;
    const f32x4 k0 = kh ? acc[2] : acc[0], k1 = kh ? acc[3] : acc[1];
    const f32x4 o0 = kh ? acc[0] : acc[2], o1 = kh ? acc[1] : acc[3];
    *(LAS f32x4*)(red + (w * 2 + 0) * 1024 + lane * 16) = o0;
    *(LAS f32x4*)(red + (w * 2 + 1) * 1024 + lane * 16) = o1;
    barrier_lds();
    const f32x4 r0 = *(LAS f32x4*)(red + ((w ^ 4) * 2 + 0) * 1024 + lane * 16), r1 = *(LAS f32x4*)(red + ((w ^ 4) * 2 + 1) * 1024 + lane * 16);
    const unsigned ha = hres.x, hb = hres.y, hc = hres.z, hd = hres.w, ba = bb0.x, bc = bb0.y, bd = bb1.x, be = bb1.y;
    const u32x2 o_0 = {pk(bf_lo(ha) + (k0[0] + r0[0] + bf_lo(ba)), bf_hi(ha) + (k0[1] + r0[1] + bf_hi(ba))), pk(bf_lo(hb) + (k0[2] + r0[2] + bf_lo(bc)), bf_hi(hb) + (k0[3] + r0[3] + bf_hi(bc)))};
    const u32x2 o_1 = {pk(bf_lo(hc) + (k1[0] + r1[0] + bf_lo(bd)), bf_hi(hc) + (k1[1] + r1[1] + bf_hi(bd))), pk(bf_lo(hd) + (k1[2] + r1[2] + bf_lo(be)), bf_hi(hd) + (k1[3] + r1[3] + bf_hi(be)))};
    const unsigned oo = (T16 * 16 + t) * 896 + (64 * j + 32 * kh + 4 * g) * 2;
    st8(o_0, rOut, oo, 0); st8(o_1, rOut, oo + 32, 0);
}

extern "C" void kernel_launch(void* const* d_in, const int* in_sizes, int n_in, void* d_out, int out_size, void* d_ws, size_t ws_size, hipStream_t stream) {
    const char* x = (const char*)d_in[0]; const char* wqkv = (const char*)d_in[1]; const char* wo = (const char*)d_in[2];
    const char* w1 = (const char*)d_in[4]; const char* b1 = (const char*)d_in[5]; const char* w2 = (const char*)d_in[6]; const char* b2 = (const char*)d_in[7];
    char* ws = (char*)d_ws;
    hipFuncSetAttribute((const void*)k_p1, hipFuncAttributeMaxDynamicSharedMemorySize, LDS_BYTES);
    hipFuncSetAttribute((const void*)k_p2, hipFuncAttributeMaxDynamicSharedMemorySize, LDS_BYTES);
    hipFuncSetAttribute((const void*)k_p3, hipFuncAttributeMaxDynamicSharedMemorySize, LDS_BYTES);
    hipFuncSetAttribute((const void*)k_p4, hipFuncAttributeMaxDynamicSharedMemorySize, LDS_BYTES);
    k_p1<<<256, 512, LDS_BYTES, stream>>>(x, wqkv, ws);
    k_p2<<<256, 512, 81920 + 1024, stream>>>(x, wo, ws);
    k_p3<<<256, 512, 116736, stream>>>(w1, b1, ws);
    k_p4<<<256, 512, LDS_BYTES, stream>>>(w2, b2, ws, (char*)d_out);
}
```

```cpp
#include <hip/hip_runtime.h>
#include <stdint.h>

typedef __bf16 bf16x8 __attribute__((ext_vector_type(8)));
typedef __bf16 bf16x4 __attribute__((ext_vector_type(4)));
typedef __bf16 bf16x2 __attribute__((ext_vector_type(2)));
typedef float f32x4 __attribute__((ext_vector_type(4)));
typedef float f32x2 __attribute__((ext_vector_type(2)));
typedef unsigned u32x4 __attribute__((ext_vector_type(4)));
typedef unsigned u32x2 __attribute__((ext_vector_type(2)));
#define LAS __attribute__((address_space(3)))
#define DI __device__ __forceinline__
typedef __amdgpu_buffer_rsrc_t rsrc_t;

constexpr int S_ = 2048, D_ = 448, F_ = 1792;
constexpr unsigned O_OFF = 1u << 20, H_OFF = 4u << 20, G_OFF = 6u << 20;
constexpr unsigned O_BYTES = 128u * 16 * 1024, H_BYTES = 128u * 14 * 1024, G_BYTES = 128u * 56 * 1024;
constexpr int LDS_BYTES = 163840;

#define STAMP(i)
DI int rfl(int v) { return __builtin_amdgcn_readfirstlane(v); }
DI rsrc_t mk_rsrc(const void* p, unsigned bytes) { return __builtin_amdgcn_make_buffer_rsrc((void*)p, (short)0, (int)bytes, 0x00020000); }
DI void dma16(unsigned ldsaddr, unsigned voff, rsrc_t srd, unsigned soff) {
    asm volatile("s_mov_b32 m0, %0\n\ts_nop 0\n\tbuffer_load_dwordx4 %1, %2, %3 offen lds" ::"s"(ldsaddr), "v"(voff), "s"(srd), "s"(soff) : "memory");
}
template <int N> DI void wait_vm() { asm volatile("s_waitcnt vmcnt(%0)" ::"i"(N) : "memory"); }
DI void barrier_lds() { asm volatile("s_waitcnt lgkmcnt(0)" ::: "memory"); __builtin_amdgcn_s_barrier(); asm volatile("" ::: "memory"); }
DI u32x4 ld16(rsrc_t r, unsigned voff, unsigned soff) { return __builtin_amdgcn_raw_buffer_load_b128(r, voff, soff, 0); }
DI u32x2 ld8(rsrc_t r, unsigned voff, unsigned soff) { return __builtin_amdgcn_raw_buffer_load_b64(r, voff, soff, 0); }
DI void st16(u32x4 v, rsrc_t r, unsigned voff, unsigned soff) { __builtin_amdgcn_raw_buffer_store_b128(v, r, voff, soff, 0); }
DI void st8(u32x2 v, rsrc_t r, unsigned voff, unsigned soff) { __builtin_amdgcn_raw_buffer_store_b64(v, r, voff, soff, 0); }
DI bf16x8 as_frag(u32x4 v) { return __builtin_bit_cast(bf16x8, v); }
DI bf16x8 lds_frag(LAS char* p) { return *(LAS bf16x8*)p; }
DI bf16x8 lds_tr(LAS char* p0, LAS char* p1) {
    bf16x4 a = __builtin_amdgcn_ds_read_tr16_b64_v4bf16((LAS bf16x4*)p0);
    bf16x4 b = __builtin_amdgcn_ds_read_tr16_b64_v4bf16((LAS bf16x4*)p1);
    return __builtin_shufflevector(a, b, 0, 1, 2, 3, 4, 5, 6, 7);
}
DI f32x4 mfma(bf16x8 a, bf16x8 b, f32x4 c) { return __builtin_amdgcn_mfma_f32_16x16x32_bf16(a, b, c, 0, 0, 0); }
DI unsigned pk(float a, float b) { f32x2 v = {a, b}; bf16x2 r = __builtin_convertvector(v, bf16x2); return __builtin_bit_cast(unsigned, r); }
DI float bf_lo(unsigned v) { return __builtin_bit_cast(float, v << 16); }
DI float bf_hi(unsigned v) { return __builtin_bit_cast(float, v & 0xffff0000u); }
DI float dot2(unsigned a, unsigned b, float c) { return __builtin_amdgcn_fdot2_f32_bf16(__builtin_bit_cast(bf16x2, a), __builtin_bit_cast(bf16x2, b), c, false); }
DI unsigned f2u(float v) { return __builtin_bit_cast(unsigned, v); }
DI float u2f(unsigned v) { return __builtin_bit_cast(float, v); }
DI float row4_sum(float v) {
    auto a = __builtin_amdgcn_permlane16_swap(f2u(v), f2u(v), false, false); unsigned a0 = a[0], a1 = a[1];
    float s = u2f(a0) + u2f(a1);
    auto b = __builtin_amdgcn_permlane32_swap(f2u(s), f2u(s), false, false); unsigned b0 = b[0], b1 = b[1];
    return u2f(b0) + u2f(b1);
}
DI float row4_max(float v) {
    auto a = __builtin_amdgcn_permlane16_swap(f2u(v), f2u(v), false, false); unsigned a0 = a[0], a1 = a[1];
    float s = fmaxf(u2f(a0), u2f(a1));
    auto b = __builtin_amdgcn_permlane32_swap(f2u(s), f2u(s), false, false); unsigned b0 = b[0], b1 = b[1];
    return fmaxf(u2f(b0), u2f(b1));
}
DI void stats_acc(bf16x8 f, float& s1, float& s2) {
    u32x4 u = __builtin_bit_cast(u32x4, f);
    unsigned a = u.x, b = u.y, c = u.z, d = u.w;
    s1 = dot2(a, 0x3F803F80u, s1); s2 = dot2(a, a, s2);
    s1 = dot2(b, 0x3F803F80u, s1); s2 = dot2(b, b, s2);
    s1 = dot2(c, 0x3F803F80u, s1); s2 = dot2(c, c, s2);
    s1 = dot2(d, 0x3F803F80u, s1); s2 = dot2(d, d, s2);
}
DI void stats_fin(float s1, float s2, float& mu, float& rstd) {
    s1 = row4_sum(s1); s2 = row4_sum(s2);
    mu = s1 * (1.0f / D_);
    float var = fmaxf(s2 * (1.0f / D_) - mu * mu, 0.0f);
    rstd = __builtin_amdgcn_rsqf(var + 1e-5f);
}

template <int N, int PD, class LD, class MM> DI void frag_pipe(LD ld, MM mm) {
    bf16x8 buf[N];
#pragma unroll
    for (int i = 0; i < PD; ++i) buf[i] = ld(i);
    __builtin_amdgcn_sched_barrier(0);
#pragma unroll
    for (int i = 0; i < N; ++i) {
        if (i + PD < N) buf[i + PD] = ld(i + PD);
        mm(i, buf[i]);
        __builtin_amdgcn_sched_barrier(0);
    }
}
constexpr int P1_XIMG = 0, P1_RING = 71680, P1_SLOT = 22528, P1_CS = P1_RING + 4 * P1_SLOT;
__global__ __launch_bounds__(512, 2) void k_p1(const char* __restrict__ x, const char* __restrict__ wqkv, char* __restrict__ ws) {
    extern __shared__ __attribute__((aligned(16))) char smem_[];
    LAS char* smem = (LAS char*)smem_;
    const unsigned lbase = (unsigned)(size_t)smem;
    const int tid = threadIdx.x, lane = tid & 63, w = rfl(tid >> 6);
    const int b = blockIdx.x, slot = b >> 3, T64 = (b & 7) * 4 + (slot >> 3), hp = slot & 7;
    const int t0 = T64 * 64;
    STAMP(0);
    const rsrc_t rX = mk_rsrc(x, S_ * D_ * 2), rW = mk_rsrc(wqkv, D_ * 3 * D_ * 2), rO = mk_rsrc(ws + O_OFF, O_BYTES);
    {
        const int i = lane >> 3, j = lane & 7;
#pragma unroll
        for (int m = 0; m < 2; ++m) {
            const int rb = w + 8 * m;
            if (rb < 10) {
                const int rho = 8 * rb + i;
                int tok = t0 - 16 + rho; tok = tok < 0 ? 0 : tok;
                const unsigned voff = tok * 896 + (j ^ ((rho >> 1) & 7)) * 16;
                if (rho != 0) {
#pragma unroll
                    for (int cb = 0; cb < 7; ++cb) dma16(lbase + P1_XIMG + (rb * 7 + cb) * 1024, voff, rX, cb * 128);
                }
            }
        }
        if (w == 1 && lane < 56) { u32x4 ones = {0x3F803F80u, 0x3F803F80u, 0x3F803F80u, 0x3F803F80u}; *(LAS u32x4*)(smem + P1_XIMG + (lane >> 3) * 1024 + (lane & 7) * 16) = ones; }
    }
    unsigned wv[3]; unsigned wl[3];
#pragma unroll
    for (int m = 0; m < 3; ++m) {
        int u = w + 8 * m; u = u > 21 ? 21 : u;
        const int c = 64 * u + lane, rs = c / 22; int cc = c - rs * 22; cc = cc > 20 ? 20 : cc;
        const int q = rs & 31, bb = q >> 3, ii = q & 7;
        const int krow = (rs & 32) + (bb & 1) * 16 + (bb >> 1) * 4 + (ii & 3) + 8 * (ii >> 2);
        const int sec = cc / 7;
        wv[m] = krow * 2688 + (sec == 2 ? 0 : (sec + 1) * 896) + hp * 112 + (cc - sec * 7) * 16;
        wl[m] = lbase + P1_RING + u * 1024;
    }
#pragma unroll
    for (int st = 0; st < 4; ++st)
#pragma unroll
        for (int m = 0; m < 3; ++m) dma16(wl[m] + st * P1_SLOT, wv[m], rW, st * 64 * 2688);

    const int ng = w & 3, tg = w >> 2;
    const int t = lane & 15, g = lane >> 4;
    const int sg = (t >> 1) & 7;
    LAS char* xb0 = smem + P1_XIMG + (t >> 3) * 7168 + (t & 7) * 128 + ((g ^ sg) * 16) + tg * 3 * 14336;
    LAS char* xb1 = smem + P1_XIMG + (t >> 3) * 7168 + (t & 7) * 128 + (((4 + g) ^ sg) * 16) + tg * 3 * 14336;
    LAS char* wb = smem + P1_RING + (8 * (g >> 1) + 4 * (g & 1) + ((lane & 15) >> 2)) * 352 + 8 * (lane & 3) + ng * 96;
    f32x4 acc[3][3];
    float s1[3], s2[3];
#pragma unroll
    for (int a = 0; a < 3; ++a) { s1[a] = 0.f; s2[a] = 0.f;
#pragma unroll
        for (int i = 0; i < 3; ++i) acc[a][i] = (f32x4){0.f, 0.f, 0.f, 0.f}; }

    for (int st = 0; st < 7; ++st) {
        if (st == 0) wait_vm<9>(); else if (st <= 4) wait_vm<6>(); else if (st == 5) wait_vm<3>(); else wait_vm<0>();
        barrier_lds();
        if (st == 0) STAMP(1);
        if (st >= 1 && st <= 3) {
#pragma unroll
            for (int m = 0; m < 3; ++m) dma16(wl[m] + ((st + 3) & 3) * P1_SLOT, wv[m], rW, (st + 3) * 64 * 2688);
        }
        LAS char* ws_ = wb + (st & 3) * P1_SLOT;
        {
            LAS char* xa = xb0 + st * 1024; LAS char* xb = xb1 + st * 1024;
            bf16x8 wf0[3], wf1[3], xf0[3], xf1[3];
#pragma unroll
            for (int i = 0; i < 3; ++i) wf0[i] = lds_tr(ws_ + 32 * i, ws_ + 5632 + 32 * i);
            xf0[0] = lds_frag(xa); xf0[1] = lds_frag(xa + 14336);
            if (tg == 0) xf0[2] = lds_frag(xa + 2 * 14336);
#pragma unroll
            for (int i = 0; i < 3; ++i) wf1[i] = lds_tr(ws_ + 11264 + 32 * i, ws_ + 11264 + 5632 + 32 * i);
            __builtin_amdgcn_sched_barrier(0);
#pragma unroll
            for (int i = 0; i < 3; ++i) { acc[0][i] = mfma(wf0[i], xf0[0], acc[0][i]); acc[1][i] = mfma(wf0[i], xf0[1], acc[1][i]); }
            xf1[0] = lds_frag(xb); xf1[1] = lds_frag(xb + 14336);
            if (tg == 0) {
                xf1[2] = lds_frag(xb + 2 * 14336);
#pragma unroll
                for (int i = 0; i < 3; ++i) acc[2][i] = mfma(wf0[i], xf0[2], acc[2][i]);
                stats_acc(xf0[2], s1[2], s2[2]);
            }
            stats_acc(xf0[0], s1[0], s2[0]); stats_acc(xf0[1], s1[1], s2[1]);
            __builtin_amdgcn_sched_barrier(0);
#pragma unroll
            for (int i = 0; i < 3; ++i) { acc[0][i] = mfma(wf1[i], xf1[0], acc[0][i]); acc[1][i] = mfma(wf1[i], xf1[1], acc[1][i]); }
            if (tg == 0) {
#pragma unroll
                for (int i = 0; i < 3; ++i) acc[2][i] = mfma(wf1[i], xf1[2], acc[2][i]);
                stats_acc(xf1[2], s1[2], s2[2]);
            }
            stats_acc(xf1[0], s1[0], s2[0]); stats_acc(xf1[1], s1[1], s2[1]);
        }
    }
    STAMP(2);
    if (tg == 0 && t == 0) {
#pragma unroll
        for (int i = 0; i < 3; ++i) if (ng * 3 + i < 11) *(LAS f32x4*)(smem + P1_CS + ((ng * 3 + i) * 16 + 4 * g) * 4) = acc[0][i];
    }
    float mu[3], rs[3];
#pragma unroll
    for (int a = 0; a < 3; ++a) stats_fin(s1[a], s2[a], mu[a], rs[a]);
    barrier_lds();
    constexpr int STG = P1_XIMG;
    if (tid < 480) {
        const int blk = tid >> 4, tt = tid & 15, arr = blk / 10;
        const int off = (arr == 1) ? tt * 64 + ((1 ^ ((tt >> 2) & 1)) * 32) + 24 : 768 + tt * 16 + 8;
        *(LAS u32x2*)(smem + STG + blk * 1024 + off) = (u32x2){0u, 0u};
    }
#pragma unroll
    for (int i = 0; i < 3; ++i) {
        const int n0 = (ng * 3 + i) * 16 + 4 * g;
        if (n0 < 168) {
            const f32x4 cs = *(LAS f32x4*)(smem + P1_CS + n0 * 4);
            const int arr = n0 / 56, rem = n0 - arr * 56, hh = rem >= 28 ? 1 : 0, dim = rem - 28 * hh;
            const int off = (arr == 1) ? t * 64 + (((dim >> 4) ^ ((t >> 2) & 1)) * 32) + (dim & 15) * 2 : (dim >> 3) * 256 + t * 16 + ((dim >> 2) & 1) * 8;
            LAS char* dst = smem + STG + ((arr * 2 + hh) * 5 + tg * 3) * 1024 + off;
#pragma unroll
            for (int a = 0; a < 3; ++a) {
                if (a < 2 || tg == 0) {
                    const float m = mu[a], r = rs[a];
                    const float v0 = (acc[a][i][0] - m * cs[0]) * r, v1 = (acc[a][i][1] - m * cs[1]) * r;
                    const float v2 = (acc[a][i][2] - m * cs[2]) * r, v3 = (acc[a][i][3] - m * cs[3]) * r;
                    *(LAS u32x2*)(dst + a * 1024) = (u32x2){pk(v0, v1), pk(v2, v3)};
                }
            }
        }
    }
    barrier_lds();
    {
        const int hh = w & 1, tq = 1 + (w >> 1);
        LAS char* kb = smem + STG + ((0 * 2 + hh) * 5 + tq - 1) * 1024;
        LAS char* vb = smem + STG + ((1 * 2 + hh) * 5 + tq - 1) * 1024;
        LAS char* qb = smem + STG + ((2 * 2 + hh) * 5 + tq) * 1024;
        const bf16x8 qf = lds_frag(qb + lane * 16);
        const bf16x8 kp = lds_frag(kb + lane * 16), kc = lds_frag(kb + 1024 + lane * 16);
        const f32x4 z = {0.f, 0.f, 0.f, 0.f};
        const f32x4 sp = mfma(kp, qf, z), sc = mfma(kc, qf, z);
        const float scl = 0.18898223650461362f * 1.4426950408889634f;
        const int kabs0 = t0 + 16 * (tq - 1) - 16;
        float zz[8];
        float mx = -3.0e38f;
#pragma unroll
        for (int e = 0; e < 8; ++e) {
            const int kap = 16 * (e >> 2) + 4 * g + (e & 3);
            const int del = 16 + t - kap;
            const bool ok = del >= 0 && del <= 14 && ((del & 1) == 0) && (kabs0 + kap >= 0);
            const float v = (e < 4 ? sp[e & 3] : sc[e & 3]) * scl;
            zz[e] = ok ? v : -3.0e38f;
            mx = fmaxf(mx, zz[e]);
        }
        mx = row4_max(mx);
        float sum = 0.f;
#pragma unroll
        for (int e = 0; e < 8; ++e) { zz[e] = __builtin_amdgcn_exp2f(zz[e] - mx); sum += zz[e]; }
        sum = row4_sum(sum);
        const float inv = __builtin_amdgcn_rcpf(sum);
        const u32x4 pu = {pk(zz[0] * inv, zz[1] * inv), pk(zz[2] * inv, zz[3] * inv), pk(zz[4] * inv, zz[5] * inv), pk(zz[6] * inv, zz[7] * inv)};
        const bf16x8 pf = as_frag(pu);
        const int row = 4 * g + ((lane & 15) >> 2);
        f32x4 od[2];
#pragma unroll
        for (int dt = 0; dt < 2; ++dt) {
            LAS char* va = vb + row * 64 + ((dt ^ (g & 1)) * 32) + 8 * (lane & 3);
            const bf16x8 vf = lds_tr(va, va + 1024);
            od[dt] = mfma(vf, pf, z);
        }
        const u32x4 ou = {pk(od[0][0], od[0][1]), pk(od[0][2], od[0][3]), pk(od[1][0], od[1][1]), pk(od[1][2], od[1][3])};
        const int T16 = 4 * T64 + tq - 1, head = 2 * hp + hh;
        st16(ou, rO, (T16 * 16 + head) * 1024 + lane * 16, 0);
        STAMP(3);
    }
}

DI unsigned wimg128_lane_off(int lane) {
    const int i8 = lane >> 3, p = lane & 7;
    const int i = (p >> 1) ^ ((i8 >> 1) & 3);
    return (2 * i + (p & 1)) * 16;
}
DI unsigned wimg128_read_base(int lane) {
    const int g = lane >> 4, q = (lane & 15) >> 2, p = lane & 3;
    const int i8 = 4 * (g & 1) + q;
    return (g >> 1) * 1024 + i8 * 128 + (((i8 >> 1) & 3) * 32) + 8 * p;
}

constexpr int P2_W = 0, P2_RED = 65536;
__global__ __launch_bounds__(512, 2) void k_p2(const char* __restrict__ x, const char* __restrict__ wo, char* __restrict__ ws) {
    extern __shared__ __attribute__((aligned(16))) char smem_[];
    LAS char* smem = (LAS char*)smem_;
    const unsigned lbase = (unsigned)(size_t)smem;
    const int tid = threadIdx.x, lane = tid & 63, w = rfl(tid >> 6);
    const int b = blockIdx.x, slot = b >> 3, T64 = (b & 7) * 4 + (slot >> 3), j = slot & 7;
    if (j == 7) return;
    const rsrc_t rX = mk_rsrc(x, S_ * D_ * 2), rW = mk_rsrc(wo, D_ * D_ * 2), rO = mk_rsrc(ws + O_OFF, O_BYTES), rH = mk_rsrc(ws + H_OFF, H_BYTES);
    const int tt = w & 3, kh = w >> 2, t = lane & 15, g = lane >> 4;
    const int T16 = 4 * T64 + tt;
    STAMP(4);
    {
        const int row0 = 28 * (w >> 2) + 8 * (w & 3) + (lane >> 3);
        const unsigned vo = row0 * 896 + wimg128_lane_off(lane) + j * 128;
#pragma unroll
        for (int m = 0; m < 8; ++m) dma16(lbase + P2_W + (w + 8 * m) * 1024, vo, rW, m * 56 * 896);
    }
    u32x4 of[8];
#pragma unroll
    for (int s = 0; s < 8; ++s) of[s] = ld16(rO, lane * 16, (T16 * 16 + 8 * kh + s) * 1024);
    const unsigned xo = (T16 * 16 + t) * 896 + (64 * j + 32 * kh + 4 * g) * 2;
    const u32x2 x0 = ld8(rX, xo, 0), x1 = ld8(rX, xo + 32, 0);
    __builtin_amdgcn_sched_barrier(0);
    wait_vm<10>();
    barrier_lds();
    STAMP(5);
    f32x4 acc[4];
#pragma unroll
    for (int i = 0; i < 4; ++i) acc[i] = (f32x4){0.f, 0.f, 0.f, 0.f};
    const unsigned rb = wimg128_read_base(lane) + kh * 8 * 4096;
    frag_pipe<32, 5>([&](int n) { LAS char* a = smem + P2_W + (rb ^ ((n & 3) << 5)) + (n >> 2) * 4096; return lds_tr(a, a + 2048); },
                     [&](int n, bf16x8 f) { acc[n & 3] = mfma(f, as_frag(of[n >> 2]), acc[n & 3]); });
    LAS char* red = smem + P2_RED;
    const f32x4 k0 = kh ? acc[2] : acc[0], k1 = kh ? acc[3] : acc[1];
    const f32x4 o0 = kh ? acc[0] : acc[2], o1 = kh ? acc[1] : acc[3];
    *(LAS f32x4*)(red + (w * 2 + 0) * 1024 + lane * 16) = o0;
    *(LAS f32x4*)(red + (w * 2 + 1) * 1024 + lane * 16) = o1;
    barrier_lds();
    const f32x4 r0 = *(LAS f32x4*)(red + ((w ^ 4) * 2 + 0) * 1024 + lane * 16), r1 = *(LAS f32x4*)(red + ((w ^ 4) * 2 + 1) * 1024 + lane * 16);
    const unsigned xa = x0.x, xb = x0.y, xc = x1.x, xd = x1.y;
    const u32x4 hu = {pk(bf_lo(xa) + k0[0] + r0[0], bf_hi(xa) + k0[1] + r0[1]), pk(bf_lo(xb) + k0[2] + r0[2], bf_hi(xb) + k0[3] + r0[3]),
                      pk(bf_lo(xc) + k1[0] + r1[0], bf_hi(xc) + k1[1] + r1[1]), pk(bf_lo(xd) + k1[2] + r1[2], bf_hi(xd) + k1[3] + r1[3])};
    st16(hu, rH, (T16 * 14 + 2 * j + kh) * 1024 + lane * 16, 0);
    STAMP(6);
}

constexpr int P3_W = 0, P3_TAB = 114688;
__global__ __launch_bounds__(512, 2) void k_p3(const char* __restrict__ w1, const char* __restrict__ b1, char* __restrict__ ws) {
    extern __shared__ __attribute__((aligned(16))) char smem_[];
    LAS char* smem = (LAS char*)smem_;
    const unsigned lbase = (unsigned)(size_t)smem;
    const int tid = threadIdx.x, lane = tid & 63, w = rfl(tid >> 6);
    const int b = blockIdx.x, slot = b >> 3, T64 = (b & 7) * 4 + (slot >> 3), j = slot & 7;
    if (j == 7) return;
    const int js = 7 * (T64 & 1) + j, T16 = 8 * (T64 >> 1) + w;
    const rsrc_t rW = mk_rsrc(w1, D_ * F_ * 2), rB = mk_rsrc(b1, F_ * 2), rH = mk_rsrc(ws + H_OFF, H_BYTES), rG = mk_rsrc(ws + G_OFF, G_BYTES);
    const int t = lane & 15, g = lane >> 4;
    STAMP(7);
    {
        const int i4 = lane >> 4, p = lane & 15;
        const int row0 = 4 * w + i4, i = (p >> 1) ^ (row0 & 7);
        const unsigned vo = row0 * 3584 + js * 256 + (2 * i + (p & 1)) * 16;
#pragma unroll
        for (int m = 0; m < 14; ++m) dma16(lbase + P3_W + (w + 8 * m) * 1024, vo, rW, m * 32 * 3584);
    }
    u32x4 hf[14];
#pragma unroll
    for (int s = 0; s < 14; ++s) hf[s] = ld16(rH, lane * 16, (T16 * 14 + s) * 1024);
    unsigned short bv = 0;
    if (tid < 128) bv = __builtin_amdgcn_raw_buffer_load_b16(rB, (js * 128 + tid) * 2, 0, 0);
    __builtin_amdgcn_sched_barrier(0);
    wait_vm<0>();
    if (tid < 128) *(LAS float*)(smem + P3_TAB + 512 + tid * 4) = u2f((unsigned)bv << 16);
    barrier_lds();
    STAMP(8);
    const int q = (lane & 15) >> 2, p = lane & 3;
    const unsigned rb = (4 * g + q) * 256 + ((4 * (g & 1) + q) * 32) + 8 * p;
    {
        const u32x4 onesu = {0x3F803F80u, 0x3F803F80u, 0x3F803F80u, 0x3F803F80u};
        f32x4 cs = {0.f, 0.f, 0.f, 0.f};
        LAS char* a = smem + P3_W + (rb ^ (w << 5));
        frag_pipe<14, 5>([&](int s) { return lds_tr(a + s * 8192, a + s * 8192 + 4096); }, [&](int s, bf16x8 f) { cs = mfma(f, as_frag(onesu), cs); });
        if (t == 0) *(LAS f32x4*)(smem + P3_TAB + (16 * w + 4 * g) * 4) = cs;
    }
    float s1 = 0.f, s2 = 0.f;
#pragma unroll
    for (int s = 0; s < 14; ++s) stats_acc(as_frag(hf[s]), s1, s2);
    float mu, rstd;
    stats_fin(s1, s2, mu, rstd);
    barrier_lds();
    STAMP(9);
    const float nmr = -mu * rstd;
    const float c0 = -2.0f * 0.7978845608028654f * 1.4426950408889634f, c1 = c0 * 0.044715f;
    f32x4 acc[8];
#pragma unroll
    for (int i = 0; i < 8; ++i) acc[i] = (f32x4){0.f, 0.f, 0.f, 0.f};
    unsigned gu[4];
    auto gelu_tile = [&](int i) {
        const f32x4 cs = *(LAS f32x4*)(smem + P3_TAB + (16 * i + 4 * g) * 4);
        const f32x4 bb = *(LAS f32x4*)(smem + P3_TAB + 512 + (16 * i + 4 * g) * 4);
        float gv[4];
#pragma unroll
        for (int r = 0; r < 4; ++r) {
            const float y = fmaf(acc[i][r], rstd, fmaf(nmr, cs[r], bb[r]));
            const float e = __builtin_amdgcn_exp2f(y * fmaf(y * y, c1, c0));
            gv[r] = y * __builtin_amdgcn_rcpf(1.0f + e);
        }
        gu[2 * (i & 1)] = pk(gv[0], gv[1]); gu[2 * (i & 1) + 1] = pk(gv[2], gv[3]);
        if (i & 1) st16((u32x4){gu[0], gu[1], gu[2], gu[3]}, rG, (T16 * 56 + 4 * js + (i >> 1)) * 1024 + lane * 16, 0);
    };
    frag_pipe<112, 5>([&](int n) { const int i = n / 14, s = n % 14; LAS char* a = smem + P3_W + (rb ^ (i << 5)) + s * 8192; return lds_tr(a, a + 4096); },
                      [&](int n, bf16x8 f) { const int i = n / 14, s = n % 14; acc[i] = mfma(f, as_frag(hf[s]), acc[i]); if (s == 3 && i > 0) gelu_tile(i - 1); });
    gelu_tile(7);
    STAMP(10);
}

constexpr int P4_RING = 0, P4_SLOT = 16384, P4_NS = 9, P4_RED = P4_NS * P4_SLOT;
__global__ __launch_bounds__(512, 2) void k_p4(const char* __restrict__ w2, const char* __restrict__ b2, char* __restrict__ ws, char* __restrict__ out) {
    extern __shared__ __attribute__((aligned(16))) char smem_[];
    LAS char* smem = (LAS char*)smem_;
    const unsigned lbase = (unsigned)(size_t)smem;
    const int tid = threadIdx.x, lane = tid & 63, w = rfl(tid >> 6);
    const int b = blockIdx.x, slot = b >> 3, T64 = (b & 7) * 4 + (slot >> 3), j = slot & 7;
    if (j == 7) return;
    const rsrc_t rW = mk_rsrc(w2, F_ * D_ * 2), rB = mk_rsrc(b2, D_ * 2), rH = mk_rsrc(ws + H_OFF, H_BYTES), rG = mk_rsrc(ws + G_OFF, G_BYTES), rOut = mk_rsrc(out, S_ * D_ * 2);
    const int tt = w & 3, kh = w >> 2, t = lane & 15, g = lane >> 4;
    const int T16 = 4 * T64 + tt;
    STAMP(11);
    unsigned wv[2], wl[2];
#pragma unroll
    for (int m = 0; m < 2; ++m) {
        const int u = w + 8 * m;
        const int row = 896 * (u >> 3) + 32 * ((u >> 2) & 1) + 8 * (u & 3) + (lane >> 3);
        wv[m] = row * 896 + j * 128 + wimg128_lane_off(lane);
        wl[m] = lbase + P4_RING + u * 1024;
    }
    const u32x4 hres = ld16(rH, lane * 16, (T16 * 14 + 2 * j + kh) * 1024);
    const unsigned bo = (64 * j + 32 * kh + 4 * g) * 2;
    const u32x2 bb0 = ld8(rB, bo, 0), bb1 = ld8(rB, bo + 32, 0);
    __builtin_amdgcn_sched_barrier(0);
    constexpr int PD = 4;
    u32x4 gf[28];
    const unsigned gso = (T16 * 56 + 28 * kh) * 1024;
#pragma unroll
    for (int st = 0; st < PD; ++st) {
#pragma unroll
        for (int m = 0; m < 2; ++m) dma16(wl[m] + st * P4_SLOT, wv[m], rW, st * 64 * 896);
        gf[2 * st] = ld16(rG, lane * 16, gso + (2 * st) * 1024); gf[2 * st + 1] = ld16(rG, lane * 16, gso + (2 * st + 1) * 1024);
        __builtin_amdgcn_sched_barrier(0);
    }
    f32x4 acc[4];
#pragma unroll
    for (int i = 0; i < 4; ++i) acc[i] = (f32x4){0.f, 0.f, 0.f, 0.f};
    const unsigned rb = wimg128_read_base(lane) + kh * 8192;
#pragma unroll
    for (int st = 0; st < 14; ++st) {
        const int later = (st + PD - 1 <= 13 ? PD - 1 : 13 - st);
        switch (2 + 4 * later) {
#define C(N) case N: wait_vm<N>(); break;
            C(14) C(10) C(6)
#undef C
            default: wait_vm<2>(); break;
        }
        barrier_lds();
        if (st == 0) STAMP(12);
        if (st + PD <= 13) {
#pragma unroll
            for (int m = 0; m < 2; ++m) dma16(wl[m] + ((st + PD) % P4_NS) * P4_SLOT, wv[m], rW, (st + PD) * 64 * 896);
            gf[2 * (st + PD)] = ld16(rG, lane * 16, gso + (2 * (st + PD)) * 1024); gf[2 * (st + PD) + 1] = ld16(rG, lane * 16, gso + (2 * (st + PD) + 1) * 1024);
        }
        __builtin_amdgcn_sched_barrier(0);
        frag_pipe<8, 4>([&](int n) { LAS char* a = smem + P4_RING + (st % P4_NS) * P4_SLOT + (n >> 2) * 4096 + (rb ^ ((n & 3) << 5)); return lds_tr(a, a + 2048); },
                        [&](int n, bf16x8 f) { acc[n & 3] = mfma(f, as_frag(gf[2 * st + (n >> 2)]), acc[n & 3]); });
    }
    STAMP(13);
    LAS char* red = smem + P4_RED;
    const f32x4 k0 = kh ? acc[2] : acc[0], k1 = kh ? acc[3] : acc[1];
    const f32x4 o0 = kh ? acc[0] : acc[2], o1 = kh ? acc[1] : acc[3];
    *(LAS f32x4*)(red + (w * 2 + 0) * 1024 + lane * 16) = o0;
    *(LAS f32x4*)(red + (w * 2 + 1) * 1024 + lane * 16) = o1;
    barrier_lds();
    const f32x4 r0 = *(LAS f32x4*)(red + ((w ^ 4) * 2 + 0) * 1024 + lane * 16), r1 = *(LAS f32x4*)(red + ((w ^ 4) * 2 + 1) * 1024 + lane * 16);
    const unsigned ha = hres.x, hb = hres.y, hc = hres.z, hd = hres.w, ba = bb0.x, bc = bb0.y, bd = bb1.x, be = bb1.y;
    const u32x2 o_0 = {pk(bf_lo(ha) + (k0[0] + r0[0] + bf_lo(ba)), bf_hi(ha) + (k0[1] + r0[1] + bf_hi(ba))), pk(bf_lo(hb) + (k0[2] + r0[2] + bf_lo(bc)), bf_hi(hb) + (k0[3] + r0[3] + bf_hi(bc)))};
    const u32x2 o_1 = {pk(bf_lo(hc) + (k1[0] + r1[0] + bf_lo(bd)), bf_hi(hc) + (k1[1] + r1[1] + bf_hi(bd))), pk(bf_lo(hd) + (k1[2] + r1[2] + bf_lo(be)), bf_hi(hd) + (k1[3] + r1[3] + bf_hi(be)))};
    const unsigned oo = (T16 * 16 + t) * 896 + (64 * j + 32 * kh + 4 * g) * 2;
    st8(o_0, rOut, oo, 0); st8(o_1, rOut, oo + 32, 0);
    STAMP(14);
}

extern "C" void kernel_launch(void* const* d_in, const int* in_sizes, int n_in, void* d_out, int out_size, void* d_ws, size_t ws_size, hipStream_t stream) {
    const char* x = (const char*)d_in[0]; const char* wqkv = (const char*)d_in[1]; const char* wo = (const char*)d_in[2];
    const char* w1 = (const char*)d_in[4]; const char* b1 = (const char*)d_in[5]; const char* w2 = (const char*)d_in[6]; const char* b2 = (const char*)d_in[7];
    char* ws = (char*)d_ws;
    hipFuncSetAttribute((const void*)k_p1, hipFuncAttributeMaxDynamicSharedMemorySize, LDS_BYTES);
    hipFuncSetAttribute((const void*)k_p2, hipFuncAttributeMaxDynamicSharedMemorySize, LDS_BYTES);
    hipFuncSetAttribute((const void*)k_p3, hipFuncAttributeMaxDynamicSharedMemorySize, LDS_BYTES);
    hipFuncSetAttribute((const void*)k_p4, hipFuncAttributeMaxDynamicSharedMemorySize, LDS_BYTES);
    k_p1<<<256, 512, LDS_BYTES, stream>>>(x, wqkv, ws);
    k_p2<<<256, 512, 81920 + 1024, stream>>>(x, wo, ws);
    k_p3<<<256, 512, 116736, stream>>>(w1, b1, ws);
    k_p4<<<256, 512, LDS_BYTES, stream>>>(w2, b2, ws, (char*)d_out);
}
```

```cpp
#include <hip/hip_runtime.h>
#include <stdint.h>

typedef __bf16 bf16x8 __attribute__((ext_vector_type(8)));
typedef __bf16 bf16x4 __attribute__((ext_vector_type(4)));
typedef __bf16 bf16x2 __attribute__((ext_vector_type(2)));
typedef float f32x16 __attribute__((ext_vector_type(16)));
typedef float f32x4 __attribute__((ext_vector_type(4)));
typedef float f32x2 __attribute__((ext_vector_type(2)));
typedef unsigned u32x4 __attribute__((ext_vector_type(4)));
typedef unsigned u32x2 __attribute__((ext_vector_type(2)));
#define LAS __attribute__((address_space(3)))
#define GAS __attribute__((address_space(1)))
#define DI __device__ __forceinline__
typedef __amdgpu_buffer_rsrc_t rsrc_t;
typedef GAS unsigned gu32;

constexpr int S_ = 2048, D_ = 448, F_ = 1792;
constexpr unsigned FL_X1 = 0, FL_X2 = 4096, FL_X3 = 6144, FL_E = 8192;
constexpr unsigned ST_OFF = 512u << 10, O_OFF = 1u << 20, H_OFF = 4u << 20, G_OFF = 6u << 20;
constexpr unsigned ST_BYTES = 64u * 14 * 256, O_BYTES = 64u * 32 * 1024, H_BYTES = 64u * 28 * 1024, G_BYTES = 64u * 112 * 1024;
constexpr int LDS_BYTES = 163840;
constexpr int SC1 = 16;
constexpr unsigned SPIN_CAP = 1u << 18;

#define STAMP(i)

DI int rfl(int v) { return __builtin_amdgcn_readfirstlane(v); }
DI rsrc_t mk_rsrc(const void* p, unsigned bytes) { return __builtin_amdgcn_make_buffer_rsrc((void*)p, (short)0, (int)bytes, 0x00020000); }
DI void dma16(unsigned ldsaddr, unsigned voff, rsrc_t srd, unsigned soff) {
    asm volatile("s_mov_b32 m0, %0\n\ts_nop 0\n\tbuffer_load_dwordx4 %1, %2, %3 offen lds" ::"s"(ldsaddr), "v"(voff), "s"(srd), "s"(soff) : "memory");
}
template <int N> DI void wait_vm() { asm volatile("s_waitcnt vmcnt(%0)" ::"i"(N) : "memory"); }
DI void bar() { asm volatile("s_waitcnt lgkmcnt(0)" ::: "memory"); __builtin_amdgcn_s_barrier(); asm volatile("" ::: "memory"); }
DI void sb() { __builtin_amdgcn_sched_barrier(0); }
template <int AUX> DI u32x4 ld16(rsrc_t r, unsigned voff, unsigned soff) { return __builtin_amdgcn_raw_buffer_load_b128(r, voff, soff, AUX); }
template <int AUX> DI u32x2 ld8(rsrc_t r, unsigned voff, unsigned soff) { return __builtin_amdgcn_raw_buffer_load_b64(r, voff, soff, AUX); }
template <int AUX> DI void st16(u32x4 v, rsrc_t r, unsigned voff, unsigned soff) { __builtin_amdgcn_raw_buffer_store_b128(v, r, voff + soff, 0, AUX); }
template <int AUX> DI void st8(u32x2 v, rsrc_t r, unsigned voff, unsigned soff) { __builtin_amdgcn_raw_buffer_store_b64(v, r, voff, soff, AUX); }
DI bf16x8 as_frag(u32x4 v) { return __builtin_bit_cast(bf16x8, v); }
DI bf16x8 lds_frag(LAS char* p) { return *(LAS bf16x8*)p; }
DI bf16x8 lds_tr(LAS char* p0, LAS char* p1) {
    bf16x4 a = __builtin_amdgcn_ds_read_tr16_b64_v4bf16((LAS bf16x4*)p0);
    bf16x4 b = __builtin_amdgcn_ds_read_tr16_b64_v4bf16((LAS bf16x4*)p1);
    return __builtin_shufflevector(a, b, 0, 1, 2, 3, 4, 5, 6, 7);
}
DI f32x4 mfma16(bf16x8 a, bf16x8 b, f32x4 c) { return __builtin_amdgcn_mfma_f32_16x16x32_bf16(a, b, c, 0, 0, 0); }
DI f32x16 mfma32(bf16x8 a, bf16x8 b, f32x16 c) { return __builtin_amdgcn_mfma_f32_32x32x16_bf16(a, b, c, 0, 0, 0); }
DI void mma16(f32x4& c, bf16x8 a, bf16x8 b) { c = mfma16(a, b, c); asm volatile("" ::"v"(c)); }
DI void mma32(f32x16& c, bf16x8 a, bf16x8 b) { c = mfma32(a, b, c); asm volatile("" ::"v"(c)); }
DI unsigned pk(float a, float b) { f32x2 v = {a, b}; bf16x2 r = __builtin_convertvector(v, bf16x2); return __builtin_bit_cast(unsigned, r); }
DI float bf_lo(unsigned v) { return __builtin_bit_cast(float, v << 16); }
DI float bf_hi(unsigned v) { return __builtin_bit_cast(float, v & 0xffff0000u); }
DI unsigned f2u(float v) { return __builtin_bit_cast(unsigned, v); }
DI float u2f(unsigned v) { return __builtin_bit_cast(float, v); }
DI float sum16(float v) { auto a = __builtin_amdgcn_permlane16_swap(f2u(v), f2u(v), false, false); unsigned a0 = a[0], a1 = a[1]; return u2f(a0) + u2f(a1); }
DI float sum32(float v) { auto a = __builtin_amdgcn_permlane32_swap(f2u(v), f2u(v), false, false); unsigned a0 = a[0], a1 = a[1]; return u2f(a0) + u2f(a1); }
DI float max16(float v) { auto a = __builtin_amdgcn_permlane16_swap(f2u(v), f2u(v), false, false); unsigned a0 = a[0], a1 = a[1]; return fmaxf(u2f(a0), u2f(a1)); }
DI float max32(float v) { auto a = __builtin_amdgcn_permlane32_swap(f2u(v), f2u(v), false, false); unsigned a0 = a[0], a1 = a[1]; return fmaxf(u2f(a0), u2f(a1)); }
DI f32x16 zero16() { f32x16 z; for (int i = 0; i < 16; ++i) z[i] = 0.f; return z; }

template <int N, int PD, class LD, class MM> DI void frag_pipe(LD ld, MM mm) {
    bf16x8 buf[N];
#pragma unroll
    for (int i = 0; i < PD; ++i) buf[i] = ld(i);
    sb();
#pragma unroll
    for (int i = 0; i < N; ++i) {
        if (i + PD < N) buf[i + PD] = ld(i + PD);
        mm(i, buf[i]);
        sb();
    }
}

DI void flag_set(char* ws, unsigned off, unsigned tag) { __hip_atomic_store((gu32*)(ws + off), tag, __ATOMIC_RELAXED, __HIP_MEMORY_SCOPE_AGENT); }
DI void flag_wait(char* ws, unsigned off, int n, unsigned tag, int lane) {
    for (unsigned spin = 0; spin < SPIN_CAP; ++spin) {
        unsigned v = tag;
        if (lane < n) v = __hip_atomic_load((gu32*)(ws + off) + lane, __ATOMIC_RELAXED, __HIP_MEMORY_SCOPE_AGENT);
        if (__all(v == tag)) break;
    }
    asm volatile("" ::: "memory");
}

DI unsigned img_src_lane(int lane) {
    const int i8 = lane >> 3, p8 = lane & 7;
    return (4 * ((p8 >> 2) ^ ((i8 >> 1) & 1)) + (p8 & 3)) * 16;
}
DI unsigned img_rd_lane(int lane) {
    const int G = lane >> 4, nh = G & 1, hh = G >> 1, q = (lane & 15) >> 2, p = lane & 3;
    return (4 * hh + q) * 128 + (q >> 1) * 64 + nh * 32 + 8 * p;
}

constexpr int L_X = 0, L_RING = 71680, RSLOT = 22528, L_TAB1 = 161792;
constexpr int L_W1A = 0, L_WO = 57344, L_STG = 122880;
constexpr int L_W1B = 57344, L_RED = 147456;
constexpr int W2SLOT = 16384;

__global__ __launch_bounds__(512, 2) void k_fused(const char* __restrict__ x, const char* __restrict__ wqkv, const char* __restrict__ wo,
                                                  const char* __restrict__ w1, const char* __restrict__ b1, const char* __restrict__ w2,
                                                  const char* __restrict__ b2, char* __restrict__ out, char* __restrict__ ws) {
    extern __shared__ __attribute__((aligned(16))) char smem_[];
    LAS char* smem = (LAS char*)smem_;
    const unsigned lbase = (unsigned)(size_t)smem;
    const int tid = threadIdx.x, lane = tid & 63, w = rfl(tid >> 6);
    const int b = blockIdx.x, slot = b >> 3, T64 = (b & 7) * 4 + (slot >> 3), mem = slot & 7;
    const int PP = T64 >> 1, js = 7 * (T64 & 1) + mem;
    const int t0 = T64 * 64;
    const bool loader = w >= 4;
    STAMP(0);

    if (loader) {
        const int lw = w - 4;
        const rsrc_t rX = mk_rsrc(x, S_ * D_ * 2), rW = mk_rsrc(wqkv, D_ * 3 * D_ * 2);
        {
            const int i = lane >> 3, j = lane & 7;
#pragma unroll
            for (int m = 0; m < 3; ++m) {
                const int rb = lw + 4 * m;
                if (rb < 10) {
                    const int rho = 8 * rb + i;
                    int tok = t0 - 16 + rho; tok = tok < 0 ? 0 : tok;
                    const unsigned voff = tok * 896 + (j ^ ((rho >> 1) & 7)) * 16;
                    if (rho != 0) {
#pragma unroll
                        for (int cb = 0; cb < 7; ++cb) dma16(lbase + L_X + (rb * 7 + cb) * 1024, voff, rX, cb * 128);
                    }
                }
            }
        }
        unsigned wv[6], wl[6]; bool wok[6];
#pragma unroll
        for (int m = 0; m < 6; ++m) {
            int u = lw + 4 * m; u = u > 21 ? 21 : u;
            const int c = 64 * u + lane, rs = c / 22, cc = c - rs * 22;
            const int q = rs & 31, bb = q >> 3, ii = q & 7;
            const int krow = (rs & 32) + (bb & 1) * 16 + (bb >> 1) * 4 + (ii & 3) + 8 * (ii >> 2);
            const int sec = cc / 7;
            wv[m] = krow * 2688 + (sec == 2 ? 0 : (sec + 1) * 896) + mem * 112 + (cc - sec * 7) * 16;
            wl[m] = lbase + L_RING + u * 1024;
            wok[m] = cc != 21;
        }
        auto w_stage = [&](int st) {
#pragma unroll
            for (int m = 0; m < 6; ++m) if (wok[m]) dma16(wl[m] + (st & 3) * RSLOT, wv[m], rW, st * 64 * 2688);
        };
        w_stage(0); w_stage(1); w_stage(2); w_stage(3);
        for (int st = 0; st < 7; ++st) {
            if (st == 0) wait_vm<18>(); else if (st <= 4) wait_vm<12>(); else if (st == 5) wait_vm<6>(); else wait_vm<0>();
            bar();
            if (st >= 1 && st <= 3) w_stage(st + 3);
        }
        bar();
        if (mem == 7) { bar(); bar(); return; }
        const unsigned isl = img_src_lane(lane);
        const int i8 = lane >> 3;
        {
            const rsrc_t rWo = mk_rsrc(wo, D_ * D_ * 2);
            const unsigned vo = (8 * lw + i8) * 896 + mem * 128 + isl;
#pragma unroll
            for (int m = 0; m < 16; ++m) dma16(lbase + L_WO + (lw + 4 * m) * 1024, vo, rWo, m * 28 * 896);
        }
        const rsrc_t rW1 = mk_rsrc(w1, D_ * F_ * 2);
        const unsigned v1o = (8 * lw + i8) * 3584 + js * 256 + isl;
#pragma unroll
        for (int m = 0; m < 14; ++m) dma16(lbase + L_W1A + (lw + 4 * m) * 1024, v1o, rW1, m * 32 * 3584);
        bar();
        bar();
        wait_vm<14>();
        bar();
        bar();
#pragma unroll
        for (int m = 0; m < 14; ++m) dma16(lbase + L_W1B + (lw + 4 * m) * 1024, v1o + 128, rW1, m * 32 * 3584);
        const rsrc_t rW2 = mk_rsrc(w2, F_ * D_ * 2);
        const unsigned v2o = (8 * lw + i8) * 896 + mem * 128 + isl;
        auto w2_stage = [&](int st) {
#pragma unroll
            for (int m = 0; m < 4; ++m)
                dma16(lbase + ((st + 7) % 9) * W2SLOT + (m >> 1) * 8192 + ((m & 1) * 4 + lw) * 1024, v2o, rW2, ((m >> 1) * 896 + 64 * st + 32 * (m & 1)) * 896);
        };
        w2_stage(0); w2_stage(1);
        bar();
        wait_vm<8>();
        bar();
        u32x4 wr[20];
#pragma unroll
        for (int m = 0; m < 20; ++m) wr[m] = ld16<0>(rW2, v2o, (((m & 3) >> 1) * 896 + 64 * (9 + (m >> 2)) + 32 * (m & 1)) * 896);
        bar();
        bar();
        w2_stage(2); w2_stage(3); w2_stage(4);
        bar();
        w2_stage(5); w2_stage(6); w2_stage(7); w2_stage(8);
        wait_vm<0>();
        bar();
        bar();
#pragma unroll
        for (int m = 0; m < 20; ++m)
            *(LAS u32x4*)(smem + ((9 + (m >> 2) + 7) % 9) * W2SLOT + ((m & 3) >> 1) * 8192 + ((m & 1) * 4 + lw) * 1024 + lane * 16) = wr[m];
        bar();
        bar();
        return;
    }

    unsigned tag = 0;
    {
        const u32x4 ones = {0x3F803F80u, 0x3F803F80u, 0x3F803F80u, 0x3F803F80u};
        if (w == 0 && lane < 56) *(LAS u32x4*)(smem + L_X + (lane >> 3) * 1024 + (lane & 7) * 16) = ones;
        const u32x4 e0 = {0x00003F80u, 0u, 0u, 0u};
        *(LAS u32x4*)(smem + L_RING + w * RSLOT + lane * 352 + 336) = e0;
        if (w == 0) tag = __hip_atomic_load((gu32*)(ws + FL_E + PP * 128), __ATOMIC_RELAXED, __HIP_MEMORY_SCOPE_AGENT) + 1u;
    }
    unsigned tch;
    {
        const char* tp = w == 0 ? wqkv : (w == 1 ? w1 : (w == 2 ? w2 : wo));
        const unsigned per = w == 0 ? 4704u : (w == 3 ? 1568u : 6272u);
        const rsrc_t rT = mk_rsrc(tp, per * 256);
        unsigned o = b * per + lane * 128; o = lane * 128 < per ? o : 0x80000000u;
        tch = __builtin_amdgcn_raw_buffer_load_b32(rT, o, 0, 0);
    }
    const int t = lane & 15, g = lane >> 4;
    f32x4 acc1[5][3];
#pragma unroll
    for (int a = 0; a < 5; ++a)
#pragma unroll
        for (int i = 0; i < 3; ++i) acc1[a][i] = (f32x4){0.f, 0.f, 0.f, 0.f};
    {
        const int sg = (t >> 1) & 7;
        LAS char* xb0 = smem + L_X + (t >> 3) * 7168 + (t & 7) * 128 + ((g ^ sg) * 16);
        LAS char* xb1 = smem + L_X + (t >> 3) * 7168 + (t & 7) * 128 + (((4 + g) ^ sg) * 16);
        LAS char* wb = smem + L_RING + (8 * (g >> 1) + 4 * (g & 1) + ((lane & 15) >> 2)) * 352 + 8 * (lane & 3) + w * 96;
        for (int st = 0; st < 7; ++st) {
            bar();
            if (st == 0) STAMP(1);
            LAS char* wp = wb + (st & 3) * RSLOT;
#pragma unroll
            for (int ks = 0; ks < 2; ++ks) {
                LAS char* xp = (ks ? xb1 : xb0) + st * 1024;
                bf16x8 wf[3], xf[5];
                wf[0] = lds_tr(wp + ks * 11264, wp + ks * 11264 + 5632);
                xf[0] = lds_frag(xp);
                wf[1] = lds_tr(wp + ks * 11264 + 32, wp + ks * 11264 + 5632 + 32);
                xf[1] = lds_frag(xp + 14336);
                if (w != 3) wf[2] = lds_tr(wp + ks * 11264 + 64, wp + ks * 11264 + 5632 + 64);
#pragma unroll
                for (int a = 2; a < 5; ++a) xf[a] = lds_frag(xp + a * 14336);
                sb();
#pragma unroll
                for (int a = 0; a < 5; ++a) { mma16(acc1[a][0], wf[0], xf[a]); mma16(acc1[a][1], wf[1], xf[a]); }
                if (w != 3) {
#pragma unroll
                    for (int a = 0; a < 5; ++a) mma16(acc1[a][2], wf[2], xf[a]);
                } else {
#pragma unroll
                    for (int a = 0; a < 5; ++a) mma16(acc1[a][2], xf[a], xf[a]);
                }
                sb();
            }
        }
    }
    STAMP(2);
    LAS float* tcs = (LAS float*)(smem + L_TAB1);
    LAS float* tst = tcs + 176;
    if (t == 0) {
#pragma unroll
        for (int i = 0; i < 3; ++i) if (w * 3 + i < 11) *(LAS f32x4*)(tcs + (w * 3 + i) * 16 + 4 * g) = acc1[0][i];
    }
    if (w == 3) {
#pragma unroll
        for (int a = 0; a < 5; ++a) {
            if (g == 2) tst[(16 * a + t) * 2] = acc1[a][1][0];
            const int r = t & 3;
            const float d01 = (r & 1) ? acc1[a][2][1] : acc1[a][2][0], d23 = (r & 1) ? acc1[a][2][3] : acc1[a][2][2];
            if (g == (t >> 2)) tst[(16 * a + t) * 2 + 1] = (r & 2) ? d23 : d01;
        }
    }
    asm volatile("" ::"v"(tch));
    bar();
    if (tid < 240) {
#pragma unroll
        for (int m = 0; m < 2; ++m) {
            const int it = tid + 240 * m, blk = it >> 4, tt = it & 15, arr = blk / 10;
            const int off = (arr == 1) ? tt * 64 + ((1 ^ ((tt >> 2) & 1)) * 32) + 24 : 768 + tt * 16 + 8;
            *(LAS u32x2*)(smem + L_STG + blk * 1024 + off) = (u32x2){0u, 0u};
        }
    }
    {
        float mu[5], rs[5];
#pragma unroll
        for (int a = 0; a < 5; ++a) {
            const f32x2 s = *(LAS f32x2*)(tst + (16 * a + t) * 2);
            mu[a] = s[0] * (1.0f / D_);
            rs[a] = __builtin_amdgcn_rsqf(fmaxf(s[1] * (1.0f / D_) - mu[a] * mu[a], 0.0f) + 1e-5f);
        }
#pragma unroll
        for (int i = 0; i < 3; ++i) {
            const int n0 = (w * 3 + i) * 16 + 4 * g;
            if (n0 < 168) {
                const f32x4 cs = *(LAS f32x4*)(tcs + n0);
                const int arr = n0 / 56, rem = n0 - arr * 56, hh = rem >= 28 ? 1 : 0, dim = rem - 28 * hh;
                const int off = (arr == 1) ? t * 64 + (((dim >> 4) ^ ((t >> 2) & 1)) * 32) + (dim & 15) * 2 : (dim >> 3) * 256 + t * 16 + ((dim >> 2) & 1) * 8;
                LAS char* dst = smem + L_STG + (arr * 2 + hh) * 5 * 1024 + off;
#pragma unroll
                for (int a = 0; a < 5; ++a) {
                    const f32x2 m2 = {-mu[a], -mu[a]}, r2 = {rs[a], rs[a]};
                    const f32x2 lo = {acc1[a][i][0], acc1[a][i][1]}, hi = {acc1[a][i][2], acc1[a][i][3]};
                    const f32x2 v01 = (m2 * (f32x2){cs[0], cs[1]} + lo) * r2, v23 = (m2 * (f32x2){cs[2], cs[3]} + hi) * r2;
                    *(LAS u32x2*)(dst + a * 1024) = (u32x2){pk(v01[0], v01[1]), pk(v23[0], v23[1])};
                }
            }
        }
    }
    bar();
    const rsrc_t rO = mk_rsrc(ws + O_OFF, O_BYTES);
    {
        const int tq = 1 + w;
        const int u = t - 4 * g, r0 = t & 1;
        const bool curA = u - r0 >= 0, curB = u - r0 - 2 >= 0;
        const bool noprev = (T64 == 0 && w == 0);
        const bool okA = curA || !noprev, okB = curB || !noprev;
        const float scl = 0.18898223650461362f * 1.4426950408889634f;
        const int vrow = 4 * g + ((lane & 15) >> 2);
        const f32x4 z = {0.f, 0.f, 0.f, 0.f};
        const int T16 = 4 * T64 + w;
        const unsigned odst = ((T16 >> 1) * 32 + (g >> 1)) * 1024 + (32 * (g & 1) + 16 * (T16 & 1) + t) * 16;
#pragma unroll
        for (int hh = 0; hh < 2; ++hh) {
            LAS char* kb = smem + L_STG + ((0 * 2 + hh) * 5 + tq - 1) * 1024;
            LAS char* vb = smem + L_STG + ((1 * 2 + hh) * 5 + tq - 1) * 1024;
            LAS char* qb = smem + L_STG + ((2 * 2 + hh) * 5 + tq) * 1024;
            const bf16x8 qf = lds_frag(qb + lane * 16);
            const bf16x8 kp = lds_frag(kb + lane * 16), kc = lds_frag(kb + 1024 + lane * 16);
            bf16x8 vf[2];
#pragma unroll
            for (int dt = 0; dt < 2; ++dt) { LAS char* va = vb + vrow * 64 + ((dt ^ (g & 1)) * 32) + 8 * (lane & 3); vf[dt] = lds_tr(va, va + 1024); }
            const f32x4 sp = mfma16(kp, qf, z), sc = mfma16(kc, qf, z);
            const float ac = r0 ? sc[1] : sc[0], ap = r0 ? sp[1] : sp[0], bc = r0 ? sc[3] : sc[2], bp = r0 ? sp[3] : sp[2];
            const float za = okA ? (curA ? ac : ap) * scl : -3.0e38f, zb = okB ? (curB ? bc : bp) * scl : -3.0e38f;
            const float mx = max32(max16(fmaxf(za, zb)));
            const float ea = __builtin_amdgcn_exp2f(za - mx), eb = __builtin_amdgcn_exp2f(zb - mx);
            const float inv = __builtin_amdgcn_rcpf(sum32(sum16(ea + eb)));
            const unsigned pw = pk(ea * inv, eb * inv);
            const unsigned wa = (pw & 0xffffu) << (16 * r0), wb_ = (pw >> 16) << (16 * r0);
            const u32x4 pu = {curA ? 0u : wa, curB ? 0u : wb_, curA ? wa : 0u, curB ? wb_ : 0u};
            const f32x4 o0 = mfma16(vf[0], as_frag(pu), z), o1 = mfma16(vf[1], as_frag(pu), z);
            const u32x4 ou = {pk(o0[0], o0[1]), pk(o0[2], o0[3]), pk(o1[0], o1[1]), pk(o1[2], o1[3])};
            st16<SC1>(ou, rO, odst, (2 * (2 * mem + hh)) * 1024);
        }
    }
    STAMP(3);
    wait_vm<0>();
    bar();
    if (w == 0) { tag = rfl(tag); if (lane == 0) flag_set(ws, FL_X1 + T64 * 128 + mem * 4, tag); }
    if (mem == 7) return;
    const int th = w & 1, kh = w >> 1, t32 = lane & 31, hl = lane >> 5;
    const int T32 = 2 * T64 + th;
    const unsigned colb = (64 * mem + 32 * kh) * 2;
    const unsigned rowo = (T32 * 32 + t32) * 896 + colb + hl * 8;
    u32x2 xr[4];
    {
        const rsrc_t rX = mk_rsrc(x, S_ * D_ * 2);
#pragma unroll
        for (int q = 0; q < 4; ++q) xr[q] = ld8<0>(rX, rowo, q * 16);
    }
    if (w == 0) flag_wait(ws, FL_X1 + T64 * 128, 8, tag, lane);
    bar();
    STAMP(4);
    const unsigned ird = img_rd_lane(lane);
    const rsrc_t rH = mk_rsrc(ws + H_OFF, H_BYTES), rSt = mk_rsrc(ws + ST_OFF, ST_BYTES);
    {
        u32x4 of[16];
#pragma unroll
        for (int s = 0; s < 16; ++s) of[s] = ld16<SC1>(rO, lane * 16, (T32 * 32 + 16 * kh + s) * 1024);
        sb();
        f32x16 a2[2] = {zero16(), zero16()};
        LAS char* wbp = smem + L_WO + kh * 8 * 4096;
        frag_pipe<32, 4>([&](int n) { const int s = n >> 1; LAS char* a = wbp + (ird ^ ((n & 1) << 6)) + (s >> 1) * 4096 + (s & 1) * 1024; return lds_tr(a, a + 2048); },
                         [&](int n, bf16x8 f) { mma32(a2[n & 1], f, as_frag(of[n >> 1])); });
        const f32x16 keep = kh ? a2[1] : a2[0], give = kh ? a2[0] : a2[1];
        LAS char* red = smem + L_RED;
#pragma unroll
        for (int q = 0; q < 4; ++q) *(LAS f32x4*)(red + (w * 4 + q) * 1024 + lane * 16) = (f32x4){give[4 * q], give[4 * q + 1], give[4 * q + 2], give[4 * q + 3]};
        bar();
        float hv[16];
        float s1 = 0.f, s2 = 0.f;
        unsigned hp[8];
#pragma unroll
        for (int q = 0; q < 4; ++q) {
            const f32x4 r = *(LAS f32x4*)(red + ((w ^ 2) * 4 + q) * 1024 + lane * 16);
            const unsigned xa = xr[q].x, xb = xr[q].y;
            hv[4 * q] = bf_lo(xa) + (keep[4 * q] + r[0]); hv[4 * q + 1] = bf_hi(xa) + (keep[4 * q + 1] + r[1]);
            hv[4 * q + 2] = bf_lo(xb) + (keep[4 * q + 2] + r[2]); hv[4 * q + 3] = bf_hi(xb) + (keep[4 * q + 3] + r[3]);
            hp[2 * q] = pk(hv[4 * q], hv[4 * q + 1]); hp[2 * q + 1] = pk(hv[4 * q + 2], hv[4 * q + 3]);
        }
        st16<SC1>((u32x4){hp[0], hp[1], hp[2], hp[3]}, rH, lane * 16, (T32 * 28 + 4 * mem + 2 * kh) * 1024);
        st16<SC1>((u32x4){hp[4], hp[5], hp[6], hp[7]}, rH, lane * 16, (T32 * 28 + 4 * mem + 2 * kh + 1) * 1024);
#pragma unroll
        for (int q = 0; q < 8; ++q) { const float lo = bf_lo(hp[q]), hi = bf_hi(hp[q]); s1 += lo + hi; s2 = fmaf(lo, lo, fmaf(hi, hi, s2)); }
        s1 = sum32(s1); s2 = sum32(s2);
        if (hl == 0) st8<SC1>((u32x2){f2u(s1), f2u(s2)}, rSt, t32 * 8, (T32 * 14 + 2 * mem + kh) * 256);
    }
    STAMP(5);
    unsigned short b1v = 0;
    if (tid < 128) { const rsrc_t rB1 = mk_rsrc(b1, F_ * 2); b1v = __builtin_amdgcn_raw_buffer_load_b16(rB1, (js * 128 + tid) * 2, 0, 0); }
    wait_vm<0>();
    bar();
    if (w == 0 && lane == 0) flag_set(ws, FL_X2 + PP * 128 + js * 4, tag);
    LAS float* tab = (LAS float*)(smem + L_RED);
    if (tid < 128) tab[128 + tid] = u2f((unsigned)b1v << 16);
    bar();
    {
        const u32x4 onesu = {0x3F803F80u, 0x3F803F80u, 0x3F803F80u, 0x3F803F80u};
        f32x16 cs = zero16();
        LAS char* a0 = smem + (w >> 1) * 57344 + (ird ^ ((w & 1) << 6));
        frag_pipe<28, 4>([&](int s) { return lds_tr(a0 + s * 2048, a0 + s * 2048 + 1024); }, [&](int s, bf16x8 f) { mma32(cs, f, as_frag(onesu)); });
        if (t32 == 0) {
#pragma unroll
            for (int q = 0; q < 4; ++q) *(LAS f32x4*)(tab + 32 * w + 8 * q + 4 * hl) = (f32x4){cs[4 * q], cs[4 * q + 1], cs[4 * q + 2], cs[4 * q + 3]};
        }
    }
    if (w == 0) {
        flag_wait(ws, FL_X2 + PP * 128, 14, tag, lane);
        if (js == 0 && lane == 0) flag_set(ws, FL_E + PP * 128, tag);
    }
    bar();
    STAMP(6);
    const rsrc_t rG = mk_rsrc(ws + G_OFF, G_BYTES);
    {
        const int TP = 4 * PP + w;
        u32x2 sv[7];
#pragma unroll
        for (int q = 0; q < 7; ++q) sv[q] = ld8<SC1>(rSt, t32 * 8 + hl * 256, (TP * 14 + 2 * q) * 256);
        u32x4 hf[28];
#pragma unroll
        for (int s = 0; s < 28; ++s) hf[s] = ld16<SC1>(rH, lane * 16, (TP * 28 + s) * 1024);
        sb();
        f32x16 a3[4] = {zero16(), zero16(), zero16(), zero16()};
        LAS char* wA = smem + L_W1A; LAS char* wB = smem + L_W1B;
        frag_pipe<56, 4>([&](int n) { LAS char* a = wA + (ird ^ ((n & 1) << 6)) + (n >> 1) * 2048; return lds_tr(a, a + 1024); },
                         [&](int n, bf16x8 f) { mma32(a3[n & 1], f, as_frag(hf[n >> 1])); });
        float s1 = 0.f, s2 = 0.f;
#pragma unroll
        for (int q = 0; q < 7; ++q) { const unsigned sa = sv[q].x, sb_ = sv[q].y; s1 += u2f(sa); s2 += u2f(sb_); }
        s1 = sum32(s1); s2 = sum32(s2);
        const float mu = s1 * (1.0f / D_);
        const float rstd = __builtin_amdgcn_rsqf(fmaxf(s2 * (1.0f / D_) - mu * mu, 0.0f) + 1e-5f);
        const f32x2 rs2 = {rstd, rstd}, nm2 = {-mu * rstd, -mu * rstd};
        const f32x2 c02 = {-2.0f * 0.7978845608028654f * 1.4426950408889634f, -2.0f * 0.7978845608028654f * 1.4426950408889634f};
        const f32x2 c12 = c02 * 0.044715f, one2 = {1.0f, 1.0f};
        bar();
        auto gelu4 = [&](int i, int q, unsigned& o0, unsigned& o1) {
            const f32x4 cs = *(LAS f32x4*)(tab + 32 * i + 8 * q + 4 * hl), bb = *(LAS f32x4*)(tab + 128 + 32 * i + 8 * q + 4 * hl);
            f32x2 y0 = (f32x2){a3[i][4 * q], a3[i][4 * q + 1]} * rs2 + (nm2 * (f32x2){cs[0], cs[1]} + (f32x2){bb[0], bb[1]});
            f32x2 y1 = (f32x2){a3[i][4 * q + 2], a3[i][4 * q + 3]} * rs2 + (nm2 * (f32x2){cs[2], cs[3]} + (f32x2){bb[2], bb[3]});
            const f32x2 z0 = y0 * ((y0 * y0) * c12 + c02), z1 = y1 * ((y1 * y1) * c12 + c02);
            const f32x2 d0 = (f32x2){__builtin_amdgcn_exp2f(z0[0]), __builtin_amdgcn_exp2f(z0[1])} + one2;
            const f32x2 d1 = (f32x2){__builtin_amdgcn_exp2f(z1[0]), __builtin_amdgcn_exp2f(z1[1])} + one2;
            y0 = y0 * (f32x2){__builtin_amdgcn_rcpf(d0[0]), __builtin_amdgcn_rcpf(d0[1])};
            y1 = y1 * (f32x2){__builtin_amdgcn_rcpf(d1[0]), __builtin_amdgcn_rcpf(d1[1])};
            o0 = pk(y0[0], y0[1]); o1 = pk(y1[0], y1[1]);
        };
        unsigned gq[8];
        auto gelu_q = [&](int i, int q) {
            gelu4(i, q, gq[2 * q], gq[2 * q + 1]);
            if (q & 1) st16<SC1>((u32x4){gq[2 * q - 2], gq[2 * q - 1], gq[2 * q], gq[2 * q + 1]}, rG, lane * 16, (TP * 112 + 8 * js + 2 * i + (q >> 1)) * 1024);
        };
        frag_pipe<28, 4>([&](int s) { LAS char* a = wB + ird + s * 2048; return lds_tr(a, a + 1024); },
                         [&](int s, bf16x8 f) { mma32(a3[2], f, as_frag(hf[s])); if (s % 3 == 1 && s / 3 < 8) gelu_q(s / 12, (s / 3) & 3); });
        frag_pipe<28, 4>([&](int s) { LAS char* a = wB + (ird ^ 64) + s * 2048; return lds_tr(a, a + 1024); },
                         [&](int s, bf16x8 f) { mma32(a3[3], f, as_frag(hf[s])); if (s % 6 == 3 && s / 6 < 4) gelu_q(2, s / 6); });
#pragma unroll
        for (int q = 0; q < 4; ++q) gelu_q(3, q);
    }
    STAMP(7);
    wait_vm<0>();
    bar();
    if (w == 0 && lane == 0) flag_set(ws, FL_X3 + PP * 128 + js * 4, tag);
    u32x4 hres[2]; u32x2 b2v[4];
    {
        const rsrc_t rB2 = mk_rsrc(b2, D_ * 2);
        hres[0] = ld16<SC1>(rH, lane * 16, (T32 * 28 + 4 * mem + 2 * kh) * 1024);
        hres[1] = ld16<SC1>(rH, lane * 16, (T32 * 28 + 4 * mem + 2 * kh + 1) * 1024);
#pragma unroll
        for (int q = 0; q < 4; ++q) b2v[q] = ld8<0>(rB2, colb + hl * 8, q * 16);
    }
    if (w == 0) flag_wait(ws, FL_X3 + PP * 128, 14, tag, lane);
    bar();
    STAMP(8);
    {
        constexpr int PD = 16;
        u32x4 gf[56];
        const unsigned gso = (T32 * 112 + 56 * kh) * 1024;
#pragma unroll
        for (int s = 0; s < PD; ++s) gf[s] = ld16<SC1>(rG, lane * 16, gso + s * 1024);
        sb();
        f32x16 a4[2] = {zero16(), zero16()};
        LAS char* wbp = smem + kh * 8192;
        auto run = [&](auto S0, auto NS) {
            constexpr int s0 = decltype(S0)::v, ns = decltype(NS)::v;
            frag_pipe<2 * ns, 4>(
                [&](int n) { const int s = s0 + (n >> 1); LAS char* a = wbp + (((s >> 2) + 7) % 9) * W2SLOT + (s & 3) * 2048 + (ird ^ ((n & 1) << 6)); return lds_tr(a, a + 1024); },
                [&](int n, bf16x8 f) { const int s = s0 + (n >> 1); if ((n & 1) == 0 && s + PD < 56) gf[s + PD] = ld16<SC1>(rG, lane * 16, gso + (s + PD) * 1024); mma32(a4[n & 1], f, as_frag(gf[s])); });
        };
        struct I0 { enum { v = 0 }; }; struct I20 { enum { v = 20 }; }; struct I16 { enum { v = 16 }; }; struct I36 { enum { v = 36 }; };
        run(I0{}, I20{});
        bar();
        run(I20{}, I16{});
        bar();
        run(I36{}, I20{});
        STAMP(9);
        const f32x16 keep = kh ? a4[1] : a4[0], give = kh ? a4[0] : a4[1];
        LAS char* red = smem + L_RED;
#pragma unroll
        for (int q = 0; q < 4; ++q) *(LAS f32x4*)(red + (w * 4 + q) * 1024 + lane * 16) = (f32x4){give[4 * q], give[4 * q + 1], give[4 * q + 2], give[4 * q + 3]};
        bar();
        const rsrc_t rOut = mk_rsrc(out, S_ * D_ * 2);
#pragma unroll
        for (int q = 0; q < 4; ++q) {
            const f32x4 r = *(LAS f32x4*)(red + ((w ^ 2) * 4 + q) * 1024 + lane * 16);
            const unsigned ha = hres[q >> 1][2 * (q & 1)], hb = hres[q >> 1][2 * (q & 1) + 1], ba = b2v[q].x, bb = b2v[q].y;
            const u32x2 ov = {pk(bf_lo(ha) + (keep[4 * q] + r[0] + bf_lo(ba)), bf_hi(ha) + (keep[4 * q + 1] + r[1] + bf_hi(ba))),
                              pk(bf_lo(hb) + (keep[4 * q + 2] + r[2] + bf_lo(bb)), bf_hi(hb) + (keep[4 * q + 3] + r[3] + bf_hi(bb)))};
            st8<0>(ov, rOut, rowo, q * 16);
        }
    }
    STAMP(10);
}

extern "C" void kernel_launch(void* const* d_in, const int* in_sizes, int n_in, void* d_out, int out_size, void* d_ws, size_t ws_size, hipStream_t stream) {
    (void)in_sizes; (void)n_in; (void)out_size; (void)ws_size;
    (void)hipFuncSetAttribute((const void*)k_fused, hipFuncAttributeMaxDynamicSharedMemorySize, LDS_BYTES);
    k_fused<<<256, 512, LDS_BYTES, stream>>>((const char*)d_in[0], (const char*)d_in[1], (const char*)d_in[2], (const char*)d_in[4], (const char*)d_in[5],
                                            (const char*)d_in[6], (const char*)d_in[7], (char*)d_out, (char*)d_ws);
}
```

```cpp
#include <hip/hip_runtime.h>
#include <stdint.h>

typedef __bf16 bf16x8 __attribute__((ext_vector_type(8)));
typedef __bf16 bf16x4 __attribute__((ext_vector_type(4)));
typedef __bf16 bf16x2 __attribute__((ext_vector_type(2)));
typedef float f32x16 __attribute__((ext_vector_type(16)));
typedef float f32x4 __attribute__((ext_vector_type(4)));
typedef float f32x2 __attribute__((ext_vector_type(2)));
typedef unsigned u32x4 __attribute__((ext_vector_type(4)));
typedef unsigned u32x2 __attribute__((ext_vector_type(2)));
#define LAS __attribute__((address_space(3)))
#define GAS __attribute__((address_space(1)))
#define DI __device__ __forceinline__
typedef __amdgpu_buffer_rsrc_t rsrc_t;
typedef GAS unsigned gu32;

constexpr int S_ = 2048, D_ = 448, F_ = 1792;
constexpr unsigned FL_X1 = 0, FL_X2 = 4096, FL_X3 = 6144, FL_E = 8192;
constexpr unsigned ST_OFF = 512u << 10, O_OFF = 1u << 20, H_OFF = 4u << 20, G_OFF = 6u << 20;
constexpr unsigned ST_BYTES = 64u * 14 * 256, O_BYTES = 64u * 32 * 1024, H_BYTES = 64u * 28 * 1024, G_BYTES = 64u * 112 * 1024;
constexpr int LDS_BYTES = 163840;
constexpr int SC1 = 16;
constexpr unsigned SPIN_CAP = 1u << 18;
#define POLL_SLEEP 5
#define PD3V 10
#define PD4V 16
#define LEAD1 1
#define PRIO 0
#define LPRIO 0

#define STAMP(i)
#define CSTAMP(i)
#define LSTAMP(i)

DI int rfl(int v) { return __builtin_amdgcn_readfirstlane(v); }
DI rsrc_t mk_rsrc(const void* p, unsigned bytes) { return __builtin_amdgcn_make_buffer_rsrc((void*)p, (short)0, (int)bytes, 0x00020000); }
DI void dma16(unsigned ldsaddr, unsigned voff, rsrc_t srd, unsigned soff) {
    asm volatile("s_mov_b32 m0, %0\n\ts_nop 0\n\tbuffer_load_dwordx4 %1, %2, %3 offen lds" ::"s"(ldsaddr), "v"(voff), "s"(srd), "s"(soff) : "memory");
}
DI void dma16_masked(unsigned ldsaddr, unsigned voff, rsrc_t srd, unsigned soff, unsigned long long off_mask) {
    unsigned long long keep;
    asm volatile("s_mov_b64 %0, exec\n\ts_andn2_b64 exec, exec, %5\n\ts_mov_b32 m0, %1\n\ts_nop 0\n\tbuffer_load_dwordx4 %2, %3, %4 offen lds\n\ts_mov_b64 exec, %0"
                 : "=&s"(keep) : "s"(ldsaddr), "v"(voff), "s"(srd), "s"(soff), "s"(off_mask) : "memory", "scc");
}
template <int N> DI void wait_vm() { asm volatile("s_waitcnt vmcnt(%0)" ::"i"(N) : "memory"); }
DI void bar() { asm volatile("s_waitcnt lgkmcnt(0)" ::: "memory"); __builtin_amdgcn_s_barrier(); asm volatile("" ::: "memory"); }
DI void sb() { __builtin_amdgcn_sched_barrier(0); }
template <int AUX> DI u32x4 ld16(rsrc_t r, unsigned voff, unsigned soff) { return __builtin_amdgcn_raw_buffer_load_b128(r, voff, soff, AUX); }
template <int AUX> DI u32x2 ld8(rsrc_t r, unsigned voff, unsigned soff) { return __builtin_amdgcn_raw_buffer_load_b64(r, voff, soff, AUX); }
template <int AUX> DI void st16(u32x4 v, rsrc_t r, unsigned voff, unsigned soff) { __builtin_amdgcn_raw_buffer_store_b128(v, r, voff + soff, 0, AUX); }
template <int AUX> DI void st8(u32x2 v, rsrc_t r, unsigned voff, unsigned soff) { __builtin_amdgcn_raw_buffer_store_b64(v, r, voff, soff, AUX); }
DI bf16x8 as_frag(u32x4 v) { return __builtin_bit_cast(bf16x8, v); }
DI bf16x8 lds_frag(LAS char* p) { return *(LAS bf16x8*)p; }
DI bf16x8 lds_tr(LAS char* p0, LAS char* p1) {
    bf16x4 a = __builtin_amdgcn_ds_read_tr16_b64_v4bf16((LAS bf16x4*)p0);
    bf16x4 b = __builtin_amdgcn_ds_read_tr16_b64_v4bf16((LAS bf16x4*)p1);
    return __builtin_shufflevector(a, b, 0, 1, 2, 3, 4, 5, 6, 7);
}
DI f32x4 mfma16(bf16x8 a, bf16x8 b, f32x4 c) { return __builtin_amdgcn_mfma_f32_16x16x32_bf16(a, b, c, 0, 0, 0); }
DI f32x16 mfma32(bf16x8 a, bf16x8 b, f32x16 c) { return __builtin_amdgcn_mfma_f32_32x32x16_bf16(a, b, c, 0, 0, 0); }
DI void mma16(f32x4& c, bf16x8 a, bf16x8 b) { c = mfma16(a, b, c); asm volatile("" ::"v"(c)); }
DI void mma32(f32x16& c, bf16x8 a, bf16x8 b) { c = mfma32(a, b, c); asm volatile("" ::"v"(c)); }
DI unsigned pk(float a, float b) { f32x2 v = {a, b}; bf16x2 r = __builtin_convertvector(v, bf16x2); return __builtin_bit_cast(unsigned, r); }
DI float bf_lo(unsigned v) { return __builtin_bit_cast(float, v << 16); }
DI float bf_hi(unsigned v) { return __builtin_bit_cast(float, v & 0xffff0000u); }
DI float dot2(unsigned a, unsigned b, float c) { return __builtin_amdgcn_fdot2_f32_bf16(__builtin_bit_cast(bf16x2, a), __builtin_bit_cast(bf16x2, b), c, false); }
DI unsigned f2u(float v) { return __builtin_bit_cast(unsigned, v); }
DI float u2f(unsigned v) { return __builtin_bit_cast(float, v); }
DI float sum16(float v) { auto a = __builtin_amdgcn_permlane16_swap(f2u(v), f2u(v), false, false); unsigned a0 = a[0], a1 = a[1]; return u2f(a0) + u2f(a1); }
DI float sum32(float v) { auto a = __builtin_amdgcn_permlane32_swap(f2u(v), f2u(v), false, false); unsigned a0 = a[0], a1 = a[1]; return u2f(a0) + u2f(a1); }
DI float max16(float v) { auto a = __builtin_amdgcn_permlane16_swap(f2u(v), f2u(v), false, false); unsigned a0 = a[0], a1 = a[1]; return fmaxf(u2f(a0), u2f(a1)); }
DI float max32(float v) { auto a = __builtin_amdgcn_permlane32_swap(f2u(v), f2u(v), false, false); unsigned a0 = a[0], a1 = a[1]; return fmaxf(u2f(a0), u2f(a1)); }
DI f32x16 zero16() { f32x16 z; for (int i = 0; i < 16; ++i) z[i] = 0.f; return z; }

template <int N, int PD, class LD, class MM> DI void frag_pipe(LD ld, MM mm) {
    bf16x8 buf[N];
#pragma unroll
    for (int i = 0; i < PD; ++i) buf[i] = ld(i);
    sb();
#pragma unroll
    for (int i = 0; i < N; ++i) {
        if (i + PD < N) buf[i + PD] = ld(i + PD);
        mm(i, buf[i]);
        sb();
    }
}

DI void flag_set(char* ws, unsigned off, unsigned tag) { __hip_atomic_store((gu32*)(ws + off), tag, __ATOMIC_RELAXED, __HIP_MEMORY_SCOPE_AGENT); }
DI void flag_wait(char* ws, unsigned off, int n, unsigned tag, int lane) {
    gu32* p = (gu32*)(ws + off) + (lane < n ? lane : 0);
    unsigned v0 = __hip_atomic_load(p, __ATOMIC_RELAXED, __HIP_MEMORY_SCOPE_AGENT);
    for (unsigned spin = 0; spin < SPIN_CAP; ++spin) {
        __builtin_amdgcn_s_sleep(POLL_SLEEP);
        const unsigned v1 = __hip_atomic_load(p, __ATOMIC_RELAXED, __HIP_MEMORY_SCOPE_AGENT);
        if (__all(v0 == tag)) break;
        __builtin_amdgcn_s_sleep(POLL_SLEEP);
        v0 = __hip_atomic_load(p, __ATOMIC_RELAXED, __HIP_MEMORY_SCOPE_AGENT);
        if (__all(v1 == tag)) break;
    }
    asm volatile("" ::: "memory");
}

DI unsigned img_src_lane(int lane) {
    const int i8 = lane >> 3, p8 = lane & 7;
    return (4 * ((p8 >> 2) ^ ((i8 >> 1) & 1)) + (p8 & 3)) * 16;
}
DI unsigned img_rd_lane(int lane) {
    const int G = lane >> 4, nh = G & 1, hh = G >> 1, q = (lane & 15) >> 2, p = lane & 3;
    return (4 * hh + q) * 128 + (q >> 1) * 64 + nh * 32 + 8 * p;
}

constexpr int SL1 = 32768, SL1W = 10240, L_TAB1 = 161792;
constexpr int L_STG = 122880;
DI constexpr int wo_pos(int m) { return m < 14 ? m * 4096 : 114688 + (m - 14) * 4096; }
constexpr int L_W1A = 0, L_W1B = 57344, L_RED = 147456;
constexpr int W2SLOT = 16384;

__global__ __launch_bounds__(512, 2) void k_fused(const char* __restrict__ x, const char* __restrict__ wqkv, const char* __restrict__ wo,
                                                  const char* __restrict__ w1, const char* __restrict__ b1, const char* __restrict__ w2,
                                                  const char* __restrict__ b2, char* __restrict__ out, char* __restrict__ ws) {
    extern __shared__ __attribute__((aligned(16))) char smem_[];
    LAS char* smem = (LAS char*)smem_;
    const unsigned lbase = (unsigned)(size_t)smem;
    const int tid = threadIdx.x, lane = tid & 63, w = rfl(tid >> 6);
    const int b = blockIdx.x, slot = b >> 3, T64 = (b & 7) * 4 + (slot >> 3), mem = slot & 7;
    const int PP = T64 >> 1, js = 7 * (T64 & 1) + mem;
    const int t0 = T64 * 64;
    const bool loader = w >= 4;
    STAMP(0);

    if (loader) {
        __builtin_amdgcn_s_setprio(LPRIO);
        const int lw = w - 4, i8 = lane >> 3;
        const rsrc_t rX = mk_rsrc(x, S_ * D_ * 2), rW = mk_rsrc(wqkv, D_ * 3 * D_ * 2);
        unsigned xv[3], xl[3];
#pragma unroll
        for (int m = 0; m < 3; ++m) {
            int rb = lw + 4 * m; rb = rb > 9 ? 9 : rb;
            const int rho = 8 * rb + i8;
            int tok = t0 - 16 + rho; tok = tok < 0 ? 0 : tok;
            xv[m] = tok * 896 + ((lane & 7) ^ ((rho >> 1) & 7)) * 16;
            xl[m] = lbase + rb * 1024;
        }
        const unsigned long long x0off = lw == 0 ? 0xFFull : 0ull;
        unsigned wv[6], wu[6]; unsigned long long woff[6];
#pragma unroll
        for (int m = 0; m < 6; ++m) {
            int u = lw + 4 * m; u = u > 21 ? 21 : u;
            const int c = 64 * u + lane, rs = c / 22, cc = c - rs * 22;
            const int q = rs & 31, bb = q >> 3, ii = q & 7;
            const int krow = (rs & 32) + (bb & 1) * 16 + (bb >> 1) * 4 + (ii & 3) + 8 * (ii >> 2);
            const int sec = cc / 7;
            wv[m] = krow * 2688 + (sec == 2 ? 0 : (sec + 1) * 896) + mem * 112 + (cc - sec * 7) * 16;
            wu[m] = lbase + SL1W + u * 1024;
            woff[m] = __ballot(cc == 21);
        }
        const bool lo2 = lw < 2;
        auto stage1 = [&](int st) {
            const unsigned sl = ((st + 1) & 3) * SL1;
            dma16_masked(xl[0] + sl, xv[0], rX, st * 128, x0off);
            dma16(xl[1] + sl, xv[1], rX, st * 128);
            if (lo2) dma16(xl[2] + sl, xv[2], rX, st * 128);
#pragma unroll
            for (int m = 0; m < 5; ++m) dma16_masked(wu[m] + sl, wv[m], rW, st * 64 * 2688, woff[m]);
            if (lo2) dma16_masked(wu[5] + sl, wv[5], rW, st * 64 * 2688, woff[5]);
        };
        stage1(0); stage1(1);
        for (int st = 0; st < 7; ++st) {
            if (st == 6) wait_vm<0>(); else if (lo2) wait_vm<9>(); else wait_vm<7>();
            bar();
            if (st <= 4) stage1(st + 2);
        }
        if (mem == 7) {
            bar(); bar(); bar();
            const int pi = (T64 & 3) * 4 + lw;
            unsigned acc = 0;
#pragma unroll 1
            for (int mtx = 0; mtx < 2; ++mtx) {
                const rsrc_t r = mk_rsrc(mtx ? w2 : w1, D_ * F_ * 2);
                unsigned v[13];
#pragma unroll
                for (int m = 0; m < 13; ++m) { const int l = (m * 16 + pi) * 64 + lane; v[m] = __builtin_amdgcn_raw_buffer_load_b32(r, l < 12544 ? l * 128 : 0x80000000u, 0, 0); }
#pragma unroll
                for (int m = 0; m < 13; ++m) acc ^= v[m];
            }
            asm volatile("" ::"v"(acc));
            return;
        }
        const unsigned isl = img_src_lane(lane);
        const rsrc_t rWo = mk_rsrc(wo, D_ * D_ * 2);
        const unsigned vo = (8 * lw + i8) * 896 + mem * 128 + isl;
        auto wo_unit = [&](int m) { dma16(lbase + wo_pos(m) + lw * 1024, vo, rWo, (m) * 28 * 896); };
#pragma unroll
        for (int m = 0; m < 12; ++m) wo_unit(m);
        LSTAMP(0);
        bar();
#pragma unroll
        for (int m = 12; m < 16; ++m) wo_unit(m);
        const rsrc_t rW1 = mk_rsrc(w1, D_ * F_ * 2);
        const unsigned v1o = i8 * 3584 + js * 256 + isl;
        auto w1_so = [&](int m) { return (unsigned)((lw + 4 * m) * 8 * 3584); };
#pragma unroll
        for (int m = 0; m < 14; ++m) dma16(lbase + L_W1B + (lw + 4 * m) * 1024, v1o + 128, rW1, w1_so(m));
        LSTAMP(1);
        bar();
        u32x4 w1r[14];
#pragma unroll
        for (int m = 0; m < 14; ++m) w1r[m] = ld16<0>(rW1, v1o, w1_so(m));
        LSTAMP(2);
        bar();
        LSTAMP(3);
        wait_vm<28>();
        LSTAMP(4);
        bar();
        wait_vm<0>();
        bar();
#pragma unroll
        for (int m = 0; m < 14; ++m) *(LAS u32x4*)(smem + L_W1A + (lw + 4 * m) * 1024 + lane * 16) = w1r[m];
        LSTAMP(5);
        bar();
        const rsrc_t rW2 = mk_rsrc(w2, F_ * D_ * 2);
        const unsigned v2o = (8 * lw + i8) * 896 + mem * 128 + isl;
        auto kst = [&](int st) { return st; };
        auto w2_stage = [&](int st) {
#pragma unroll
            for (int m = 0; m < 4; ++m)
                dma16(lbase + ((st + 7) % 9) * W2SLOT + (m >> 1) * 8192 + ((m & 1) * 4 + lw) * 1024, v2o, rW2, ((m >> 1) * 896 + 64 * kst(st) + 32 * (m & 1)) * 896);
        };
        w2_stage(0); w2_stage(1);
        bar();
        bar();
        w2_stage(2); w2_stage(3); w2_stage(4);
        u32x4 wr[20];
#pragma unroll
        for (int m = 0; m < 20; ++m) wr[m] = ld16<0>(rW2, v2o, (((m & 3) >> 1) * 896 + 64 * kst(9 + (m >> 2)) + 32 * (m & 1)) * 896);
        LSTAMP(6);
        bar();
        w2_stage(5); w2_stage(6); w2_stage(7); w2_stage(8);
        LSTAMP(7);
        wait_vm<36>();
        LSTAMP(8);
        bar();
        wait_vm<0>();
        bar();
#pragma unroll
        for (int m = 0; m < 20; ++m)
            *(LAS u32x4*)(smem + ((9 + (m >> 2) + 7) % 9) * W2SLOT + ((m & 3) >> 1) * 8192 + ((m & 1) * 4 + lw) * 1024 + lane * 16) = wr[m];
        bar();
        bar();
        return;
    }

    __builtin_amdgcn_s_setprio(PRIO);
    unsigned tag = 0;
    {
        const u32x4 ones = {0x3F803F80u, 0x3F803F80u, 0x3F803F80u, 0x3F803F80u};
        if (lane < 8) *(LAS u32x4*)(smem + w * SL1 + lane * 16) = ones;
        const u32x4 e0 = {0x00003F80u, 0u, 0u, 0u};
        *(LAS u32x4*)(smem + w * SL1 + SL1W + lane * 352 + 336) = e0;
        if (w == 0) tag = __hip_atomic_load((gu32*)(ws + FL_E + PP * 128), __ATOMIC_RELAXED, __HIP_MEMORY_SCOPE_AGENT) + 1u;
    }
    unsigned tch;
    {
        const char* tp = w == 0 ? wqkv : (w == 1 ? w1 : (w == 2 ? w2 : wo));
        const unsigned per = w == 0 ? 4704u : (w == 3 ? 1568u : 6272u);
        const rsrc_t rT = mk_rsrc(tp, per * 256);
        unsigned o = b * per + lane * 128; o = lane * 128 < per ? o : 0x80000000u;
        tch = __builtin_amdgcn_raw_buffer_load_b32(rT, o, 0, 0);
    }
    const int t = lane & 15, g = lane >> 4;
    f32x4 acc1[5][3];
#pragma unroll
    for (int a = 0; a < 5; ++a)
#pragma unroll
        for (int i = 0; i < 3; ++i) acc1[a][i] = (f32x4){0.f, 0.f, 0.f, 0.f};
    {
        const int sg = (t >> 1) & 7;
        LAS char* xa[2]; LAS char* wa;
        xa[0] = smem + (SL1 + (t >> 3) * 1024 + (t & 7) * 128 + ((g ^ sg) * 16)); xa[1] = smem + (SL1 + (t >> 3) * 1024 + (t & 7) * 128 + (((4 + g) ^ sg) * 16));
        wa = smem + (SL1 + SL1W + (8 * (g >> 1) + 4 * (g & 1) + ((lane & 15) >> 2)) * 352 + 8 * (lane & 3) + w * 96);
        auto trd = [&](LAS char* p) { return __builtin_amdgcn_ds_read_tr16_b64_v4bf16((LAS bf16x4*)p); };
#pragma unroll
        for (int a = 0; a < 5; ++a)
#pragma unroll
            for (int i = 0; i < 3; ++i) asm volatile("" : "+v"(acc1[a][i]));
        auto gemm1 = [&](auto W3) {
            constexpr bool w3 = decltype(W3)::v;
            bf16x4 wl[2][3], wh[2][3]; bf16x8 xf[2][5];
            auto rd = [&](int ks, int j) {
                if (j == 0) wl[ks][0] = trd(wa + ks * 11264);
                else if (j == 1) wh[ks][0] = trd(wa + ks * 11264 + 5632);
                else if (j <= 6) xf[ks][j - 2] = lds_frag(xa[ks] + (j - 2) * 2048);
                else if (w3 && j >= 9) { }
                else if (j & 1) wl[ks][(j - 5) >> 1] = trd(wa + ks * 11264 + ((j - 5) >> 1) * 32);
                else wh[ks][(j - 6) >> 1] = trd(wa + ks * 11264 + 5632 + ((j - 6) >> 1) * 32);
            };
#pragma unroll 1
            for (int st = 0; st < 7; ++st) {
                bar();
                if (st == 0) STAMP(1);
#pragma unroll
                for (int j = 0; j < 11; ++j) rd(0, j);
                sb();
#pragma unroll
                for (int ks = 0; ks < 2; ++ks) {
#pragma unroll
                    for (int n = 0; n < 15; ++n) {
                        const int i = n / 5, a = n % 5;
                        if (w3 && i == 2) mma16(acc1[a][2], xf[ks][a], xf[ks][a]);
                        else mma16(acc1[a][i], __builtin_shufflevector(wl[ks][i], wh[ks][i], 0, 1, 2, 3, 4, 5, 6, 7), xf[ks][a]);
                        if (ks == 0 && n < 11) rd(1, n);
                        sb();
                    }
                }
                const int dd = ((st + 2) & 3) == 0 ? -3 * SL1 : SL1;
                xa[0] += dd; xa[1] += dd; wa += dd;
            }
        };
        struct BT { enum { v = 1 }; }; struct BF { enum { v = 0 }; };
        if (w == 3) gemm1(BT{}); else gemm1(BF{});
    }
    STAMP(2);
    LAS float* tst = (LAS float*)(smem + L_TAB1);
    if (w == 3) {
        const int r = t & 3;
        float d[5];
#pragma unroll
        for (int a = 0; a < 5; ++a) {
            const float da = (r & 1) ? acc1[a][2][1] : acc1[a][2][0], db = (r & 1) ? acc1[a][2][3] : acc1[a][2][2];
            d[a] = (r & 2) ? db : da;
        }
        LAS float* tp = tst + 2 * t;
        if (g == 2) {
#pragma unroll
            for (int a = 0; a < 5; ++a) tp[32 * a] = acc1[a][1][0];
        }
        if (g == (t >> 2)) {
#pragma unroll
            for (int a = 0; a < 5; ++a) tp[32 * a + 1] = d[a];
        }
    }
    asm volatile("" ::"v"(tch));
    bar();
    STAMP(11);
    {
#pragma unroll
        for (int m = 0; m < 2; ++m) {
            const int it = tid + 256 * m, blk = it >> 4, tt = it & 15;
            if (it < 480) {
                const int sv_ = ((tt >> 1) & 1) | (((tt >> 3) & 1) << 1) | (((tt >> 2) & 1) << 2);
                const int off = (blk >= 10 && blk < 20) ? tt * 64 + ((7 ^ sv_) * 8) : 768 + tt * 16 + 8;
                *(LAS u32x2*)(smem + L_STG + blk * 1024 + off) = (u32x2){0u, 0u};
            }
        }
        f32x2 nmu[5], rs[5];
#pragma unroll
        for (int a = 0; a < 5; ++a) {
            const f32x2 s = *(LAS f32x2*)(tst + (16 * a + t) * 2);
            const float mu = s[0] * (1.0f / D_);
            const float r = __builtin_amdgcn_rsqf(fmaxf(s[1] * (1.0f / D_) - mu * mu, 0.0f) + 1e-5f);
            nmu[a] = (f32x2){-mu, -mu}; rs[a] = (f32x2){r, r};
        }
        const int svt = ((t >> 1) & 1) | (((t >> 3) & 1) << 1) | (((t >> 2) & 1) << 2);
#pragma unroll
        for (int i = 0; i < 3; ++i) {
            const int n0 = (w * 3 + i) * 16 + 4 * g;
            f32x4 cs;
#pragma unroll
            for (int r = 0; r < 4; ++r) { const float c = acc1[0][i][r]; cs[r] = u2f(__builtin_amdgcn_update_dpp(0, (int)f2u(c), 0x150, 0xf, 0xf, true)); }
            u32x2 pv[5];
#pragma unroll
            for (int a = 0; a < 5; ++a) {
                const f32x2 lo = {acc1[a][i][0], acc1[a][i][1]}, hi = {acc1[a][i][2], acc1[a][i][3]};
                const f32x2 v01 = (nmu[a] * (f32x2){cs[0], cs[1]} + lo) * rs[a], v23 = (nmu[a] * (f32x2){cs[2], cs[3]} + hi) * rs[a];
                pv[a] = (u32x2){pk(v01[0], v01[1]), pk(v23[0], v23[1])};
            }
            const int arr = (n0 >= 56 ? 1 : 0) + (n0 >= 112 ? 1 : 0), rem = n0 - arr * 56, hh = rem >= 28 ? 1 : 0, dim = rem - 28 * hh;
            const int off = (arr == 1) ? t * 64 + (((dim >> 2) ^ svt) * 8) : (dim >> 3) * 256 + t * 16 + ((dim >> 2) & 1) * 8;
            LAS char* dst = smem + L_STG + (arr * 2 + hh) * 5 * 1024 + off;
            if (n0 < 168) {
#pragma unroll
                for (int a = 0; a < 5; ++a) *(LAS u32x2*)(dst + a * 1024) = pv[a];
            }
        }
    }
    STAMP(12);
    bar();
    STAMP(13);
    const rsrc_t rO = mk_rsrc(ws + O_OFF, O_BYTES);
    {
        const int tq = 1 + w;
        const int uu = t - 4 * g, r0 = t & 1;
        const bool curA = uu - r0 >= 0, curB = uu - r0 - 2 >= 0;
        const bool noprev = (T64 == 0 && w == 0);
        const bool okA = curA || !noprev, okB = curB || !noprev;
        const int vrow = 4 * g + ((lane & 15) >> 2);
        const int svr = ((vrow >> 1) & 1) | (((vrow >> 3) & 1) << 1) | (((vrow >> 2) & 1) << 2);
        const int T16 = 4 * T64 + w;
        const unsigned odst = ((T16 >> 1) * 32 + (g >> 1)) * 1024 + (32 * (g & 1) + 16 * (T16 & 1) + t) * 16;
        const float scl = 0.18898223650461362f * 1.4426950408889634f;
        const f32x4 z = {0.f, 0.f, 0.f, 0.f};
#pragma unroll
        for (int hh = 0; hh < 2; ++hh) {
            LAS char* kb = smem + L_STG + ((0 * 2 + hh) * 5 + tq - 1) * 1024;
            LAS char* vb = smem + L_STG + ((1 * 2 + hh) * 5 + tq - 1) * 1024;
            LAS char* qb = smem + L_STG + ((2 * 2 + hh) * 5 + tq) * 1024;
            const bf16x8 qf = lds_frag(qb + lane * 16);
            const bf16x8 kp = lds_frag(kb + lane * 16), kc = lds_frag(kb + 1024 + lane * 16);
            bf16x8 vf[2];
#pragma unroll
            for (int dt = 0; dt < 2; ++dt) { LAS char* va = vb + vrow * 64 + (((4 * dt + (lane & 3)) ^ svr) * 8); vf[dt] = lds_tr(va, va + 1024); }
            const f32x4 sp = mfma16(kp, qf, z), sc = mfma16(kc, qf, z);
            const float ac = r0 ? sc[1] : sc[0], ap = r0 ? sp[1] : sp[0], bc = r0 ? sc[3] : sc[2], bp = r0 ? sp[3] : sp[2];
            const float za = okA ? (curA ? ac : ap) * scl : -3.0e38f, zb = okB ? (curB ? bc : bp) * scl : -3.0e38f;
            const float mx = max32(max16(fmaxf(za, zb)));
            const float ea = __builtin_amdgcn_exp2f(za - mx), eb = __builtin_amdgcn_exp2f(zb - mx);
            const float inv = __builtin_amdgcn_rcpf(sum32(sum16(ea + eb)));
            const unsigned pw = pk(ea * inv, eb * inv);
            const unsigned wa_ = (pw & 0xffffu) << (16 * r0), wb_ = (pw >> 16) << (16 * r0);
            const u32x4 pu = {curA ? 0u : wa_, curB ? 0u : wb_, curA ? wa_ : 0u, curB ? wb_ : 0u};
            const f32x4 o0 = mfma16(vf[0], as_frag(pu), z), o1 = mfma16(vf[1], as_frag(pu), z);
            const u32x4 ou = {pk(o0[0], o0[1]), pk(o0[2], o0[3]), pk(o1[0], o1[1]), pk(o1[2], o1[3])};
            st16<SC1>(ou, rO, odst, (2 * (2 * mem + hh)) * 1024);
        }
    }
    STAMP(3);
    wait_vm<0>();
    STAMP(14);
    bar();
    STAMP(15);
    if (w == 0) { tag = rfl(tag); if (lane == 0) flag_set(ws, FL_X1 + T64 * 128 + mem * 4, tag); }
    if (mem == 7) return;
    const int th = w & 1, kh = w >> 1, t32 = lane & 31, hl = lane >> 5;
    const int T32 = 2 * T64 + th;
    const unsigned colb = (64 * mem + 32 * kh) * 2;
    const unsigned rowo = (T32 * 32 + t32) * 896 + colb + hl * 8;
    u32x2 xr[4];
    {
        const rsrc_t rX = mk_rsrc(x, S_ * D_ * 2);
#pragma unroll
        for (int q = 0; q < 4; ++q) xr[q] = ld8<0>(rX, rowo, q * 16);
    }
    if (w == 0) flag_wait(ws, FL_X1 + T64 * 128, 8, tag, lane);
    bar();
    STAMP(4);
    const unsigned ird = img_rd_lane(lane);
    const rsrc_t rH = mk_rsrc(ws + H_OFF, H_BYTES), rSt = mk_rsrc(ws + ST_OFF, ST_BYTES);
    {
        constexpr int PD2 = 8;
        u32x4 of[16];
        const unsigned oso = (T32 * 32 + 16 * kh) * 1024;
        auto o_so = [&](int s) { return (unsigned)(s * 1024); };
#pragma unroll
        for (int s = 0; s < PD2; ++s) of[s] = ld16<SC1>(rO, lane * 16, oso + o_so(s));
        sb();
        f32x16 a2[2] = {zero16(), zero16()};
        const unsigned wo_hi = kh ? 114688 - 14 * 4096 : 0;
        frag_pipe<32, 4>([&](int n) { const int s = n >> 1, hp_ = s >> 1; LAS char* a = smem + (ird ^ ((n & 1) << 6)) + kh * 8 * 4096 + hp_ * 4096 + (hp_ >= 6 ? wo_hi : 0) + (s & 1) * 1024; return lds_tr(a, a + 2048); },
                         [&](int n, bf16x8 f) { const int s = n >> 1; if ((n & 1) == 0 && s + PD2 < 16) of[s + PD2] = ld16<SC1>(rO, lane * 16, oso + o_so(s + PD2)); mma32(a2[n & 1], f, as_frag(of[s])); });
        const f32x16 keep = kh ? a2[1] : a2[0], give = kh ? a2[0] : a2[1];
        LAS char* red = smem + L_RED;
#pragma unroll
        for (int q = 0; q < 4; ++q) *(LAS f32x4*)(red + (w * 4 + q) * 1024 + lane * 16) = (f32x4){give[4 * q], give[4 * q + 1], give[4 * q + 2], give[4 * q + 3]};
        bar();
        float hv[16];
        float s1 = 0.f, s2 = 0.f;
        unsigned hp[8];
#pragma unroll
        for (int q = 0; q < 4; ++q) {
            const f32x4 r = *(LAS f32x4*)(red + ((w ^ 2) * 4 + q) * 1024 + lane * 16);
            const unsigned xa = xr[q].x, xb = xr[q].y;
            hv[4 * q] = bf_lo(xa) + (keep[4 * q] + r[0]); hv[4 * q + 1] = bf_hi(xa) + (keep[4 * q + 1] + r[1]);
            hv[4 * q + 2] = bf_lo(xb) + (keep[4 * q + 2] + r[2]); hv[4 * q + 3] = bf_hi(xb) + (keep[4 * q + 3] + r[3]);
            hp[2 * q] = pk(hv[4 * q], hv[4 * q + 1]); hp[2 * q + 1] = pk(hv[4 * q + 2], hv[4 * q + 3]);
        }
        st16<SC1>((u32x4){hp[0], hp[1], hp[2], hp[3]}, rH, lane * 16, (T32 * 28 + 4 * mem + 2 * kh) * 1024);
        st16<SC1>((u32x4){hp[4], hp[5], hp[6], hp[7]}, rH, lane * 16, (T32 * 28 + 4 * mem + 2 * kh + 1) * 1024);
#pragma unroll
        for (int q = 0; q < 8; ++q) { const float lo = bf_lo(hp[q]), hi = bf_hi(hp[q]); s1 += lo + hi; s2 = fmaf(lo, lo, fmaf(hi, hi, s2)); }
        s1 = sum32(s1); s2 = sum32(s2);
        if (hl == 0) st8<SC1>((u32x2){f2u(s1), f2u(s2)}, rSt, t32 * 8, (T32 * 14 + 2 * mem + kh) * 256);
    }
    STAMP(5);
    unsigned short b1v = 0;
    if (tid < 128) { const rsrc_t rB1 = mk_rsrc(b1, F_ * 2); b1v = __builtin_amdgcn_raw_buffer_load_b16(rB1, (js * 128 + tid) * 2, 0, 0); }
    wait_vm<0>();
    STAMP(16);
    bar();
    STAMP(17);
    if (w == 0 && lane == 0) flag_set(ws, FL_X2 + PP * 128 + js * 4, tag);
    LAS float* tab = (LAS float*)(smem + L_RED);
    if (tid < 128) tab[128 + tid] = u2f((unsigned)b1v << 16);
    {
        const u32x4 onesu = {0x3F803F80u, 0x3F803F80u, 0x3F803F80u, 0x3F803F80u};
        f32x16 cs = zero16();
        LAS char* a0 = smem + (w >> 1) * 57344 + (ird ^ ((w & 1) << 6));
        frag_pipe<28, 4>([&](int s) { return lds_tr(a0 + s * 2048, a0 + s * 2048 + 1024); }, [&](int s, bf16x8 f) { mma32(cs, f, as_frag(onesu)); });
        if (t32 == 0) {
#pragma unroll
            for (int q = 0; q < 4; ++q) *(LAS f32x4*)(tab + 32 * w + 8 * q + 4 * hl) = (f32x4){cs[4 * q], cs[4 * q + 1], cs[4 * q + 2], cs[4 * q + 3]};
        }
    }
    if (w == 0) {
        flag_wait(ws, FL_X2 + PP * 128, 14, tag, lane);
        if (js == 0 && lane == 0) flag_set(ws, FL_E + PP * 128, tag);
    }
    bar();
    STAMP(6);
    const rsrc_t rG = mk_rsrc(ws + G_OFF, G_BYTES);
    {
        const int TP = 4 * PP + w;
        u32x2 sv[7];
#pragma unroll
        for (int q = 0; q < 7; ++q) sv[q] = ld8<SC1>(rSt, t32 * 8 + hl * 256, (TP * 14 + 2 * q) * 256);
        constexpr int PD3 = PD3V;
        u32x4 hf[28];
        const unsigned hso = TP * 28 * 1024;
        auto h_so = [&](int s) { return (unsigned)(s * 1024); };
#pragma unroll
        for (int s = 0; s < PD3; ++s) hf[s] = ld16<SC1>(rH, lane * 16, hso + h_so(s));
        sb();
        f32x16 a3[4] = {zero16(), zero16(), zero16(), zero16()};
        LAS char* wA = smem + L_W1A; LAS char* wB = smem + L_W1B;
        frag_pipe<56, 4>([&](int n) { LAS char* a = wA + (ird ^ ((n & 1) << 6)) + (n >> 1) * 2048; return lds_tr(a, a + 1024); },
                         [&](int n, bf16x8 f) { const int s = n >> 1; if ((n & 1) == 0 && s + PD3 < 28) hf[s + PD3] = ld16<SC1>(rH, lane * 16, hso + h_so(s + PD3)); mma32(a3[n & 1], f, as_frag(hf[s])); });
        float s1 = 0.f, s2 = 0.f;
#pragma unroll
        for (int q = 0; q < 7; ++q) { const unsigned sa = sv[q].x, sb_ = sv[q].y; s1 += u2f(sa); s2 += u2f(sb_); }
        s1 = sum32(s1); s2 = sum32(s2);
        const float mu = s1 * (1.0f / D_);
        const float rstd = __builtin_amdgcn_rsqf(fmaxf(s2 * (1.0f / D_) - mu * mu, 0.0f) + 1e-5f);
        const f32x2 rs2 = {rstd, rstd}, nm2 = {-mu * rstd, -mu * rstd};
        const f32x2 c02 = {-2.0f * 0.7978845608028654f * 1.4426950408889634f, -2.0f * 0.7978845608028654f * 1.4426950408889634f};
        const f32x2 c12 = c02 * 0.044715f, one2 = {1.0f, 1.0f};
        STAMP(18);
        bar();
        f32x4 tcs_[16], tbb_[16];
        unsigned gq[8];
        LAS float* tabl = tab + 4 * hl;
        asm volatile("" : "+v"(tabl));
        auto gelu_ld = [&](int m) { const int i = m >> 2, q = m & 3; tcs_[m] = *(LAS f32x4*)(tabl + 32 * i + 8 * q); tbb_[m] = *(LAS f32x4*)(tabl + 128 + 32 * i + 8 * q); };
        auto gelu_do = [&](int m) {
            const int i = m >> 2, q = m & 3;
            const f32x4 cs = tcs_[m], bb = tbb_[m];
            f32x2 y0 = (f32x2){a3[i][4 * q], a3[i][4 * q + 1]} * rs2 + (nm2 * (f32x2){cs[0], cs[1]} + (f32x2){bb[0], bb[1]});
            f32x2 y1 = (f32x2){a3[i][4 * q + 2], a3[i][4 * q + 3]} * rs2 + (nm2 * (f32x2){cs[2], cs[3]} + (f32x2){bb[2], bb[3]});
            const f32x2 z0 = y0 * ((y0 * y0) * c12 + c02), z1 = y1 * ((y1 * y1) * c12 + c02);
            const f32x2 d0 = (f32x2){__builtin_amdgcn_exp2f(z0[0]), __builtin_amdgcn_exp2f(z0[1])} + one2;
            const f32x2 d1 = (f32x2){__builtin_amdgcn_exp2f(z1[0]), __builtin_amdgcn_exp2f(z1[1])} + one2;
            y0 = y0 * (f32x2){__builtin_amdgcn_rcpf(d0[0]), __builtin_amdgcn_rcpf(d0[1])};
            y1 = y1 * (f32x2){__builtin_amdgcn_rcpf(d1[0]), __builtin_amdgcn_rcpf(d1[1])};
            gq[2 * q] = pk(y0[0], y0[1]); gq[2 * q + 1] = pk(y1[0], y1[1]);
            if (q & 1) st16<SC1>((u32x4){gq[2 * q - 2], gq[2 * q - 1], gq[2 * q], gq[2 * q + 1]}, rG, lane * 16, (TP * 112 + 8 * js + 2 * i + (q >> 1)) * 1024);
        };
        frag_pipe<28, 4>([&](int s) { LAS char* a = wB + ird + s * 2048; return lds_tr(a, a + 1024); },
                         [&](int s, bf16x8 f) { if (s % 3 == 0 && s / 3 < 8) gelu_ld(s / 3); mma32(a3[2], f, as_frag(hf[s])); if (s % 3 == 2 && s / 3 < 8) gelu_do(s / 3); });
        frag_pipe<28, 4>([&](int s) { LAS char* a = wB + (ird ^ 64) + s * 2048; return lds_tr(a, a + 1024); },
                         [&](int s, bf16x8 f) { if (s % 6 == 1 && s / 6 < 4) gelu_ld(8 + s / 6); if (s >= 24) gelu_ld(12 + s - 24); mma32(a3[3], f, as_frag(hf[s])); if (s % 6 == 4 && s / 6 < 4) gelu_do(8 + s / 6); });
#pragma unroll
        for (int q = 0; q < 4; ++q) gelu_do(12 + q);
    }
    STAMP(7);
    wait_vm<0>();
    STAMP(19);
    bar();
    if (w == 0 && lane == 0) flag_set(ws, FL_X3 + PP * 128 + js * 4, tag);
    u32x4 hres[2]; u32x2 b2v[4];
    {
        const rsrc_t rB2 = mk_rsrc(b2, D_ * 2);
        hres[0] = ld16<SC1>(rH, lane * 16, (T32 * 28 + 4 * mem + 2 * kh) * 1024);
        hres[1] = ld16<SC1>(rH, lane * 16, (T32 * 28 + 4 * mem + 2 * kh + 1) * 1024);
#pragma unroll
        for (int q = 0; q < 4; ++q) b2v[q] = ld8<0>(rB2, colb + hl * 8, q * 16);
    }
    if (w == 0) flag_wait(ws, FL_X3 + PP * 128, 14, tag, lane);
    bar();
    STAMP(8);
    {
        constexpr int PD = PD4V;
        u32x4 gf[56];
        const unsigned gso = (T32 * 112 + 56 * kh) * 1024;
        auto g_so = [&](int s) { return (unsigned)(s * 1024); };
#pragma unroll
        for (int s = 0; s < PD; ++s) gf[s] = ld16<SC1>(rG, lane * 16, gso + g_so(s));
        sb();
        f32x16 a4[2] = {zero16(), zero16()};
        LAS char* wbp = smem + kh * 8192;
        auto run = [&](auto S0, auto NS) {
            constexpr int s0 = decltype(S0)::v, ns = decltype(NS)::v;
            frag_pipe<2 * ns, 4>(
                [&](int n) { const int s = s0 + (n >> 1); LAS char* a = wbp + (((s >> 2) + 7) % 9) * W2SLOT + (s & 3) * 2048 + (ird ^ ((n & 1) << 6)); return lds_tr(a, a + 1024); },
                [&](int n, bf16x8 f) { const int s = s0 + (n >> 1); if ((n & 1) == 0 && s + PD < 56) gf[s + PD] = ld16<SC1>(rG, lane * 16, gso + g_so(s + PD)); mma32(a4[n & 1], f, as_frag(gf[s])); });
        };
        struct I0 { enum { v = 0 }; }; struct I20 { enum { v = 20 }; }; struct I16 { enum { v = 16 }; }; struct I36 { enum { v = 36 }; };
        run(I0{}, I20{});
        bar();
        run(I20{}, I16{});
        bar();
        run(I36{}, I20{});
        STAMP(9);
        const f32x16 keep = kh ? a4[1] : a4[0], give = kh ? a4[0] : a4[1];
        LAS char* red = smem + L_RED;
#pragma unroll
        for (int q = 0; q < 4; ++q) *(LAS f32x4*)(red + (w * 4 + q) * 1024 + lane * 16) = (f32x4){give[4 * q], give[4 * q + 1], give[4 * q + 2], give[4 * q + 3]};
        bar();
        const rsrc_t rOut = mk_rsrc(out, S_ * D_ * 2);
#pragma unroll
        for (int q = 0; q < 4; ++q) {
            const f32x4 r = *(LAS f32x4*)(red + ((w ^ 2) * 4 + q) * 1024 + lane * 16);
            const unsigned ha = hres[q >> 1][2 * (q & 1)], hb = hres[q >> 1][2 * (q & 1) + 1], ba = b2v[q].x, bb = b2v[q].y;
            const u32x2 ov = {pk(bf_lo(ha) + (keep[4 * q] + r[0] + bf_lo(ba)), bf_hi(ha) + (keep[4 * q + 1] + r[1] + bf_hi(ba))),
                              pk(bf_lo(hb) + (keep[4 * q + 2] + r[2] + bf_lo(bb)), bf_hi(hb) + (keep[4 * q + 3] + r[3] + bf_hi(bb)))};
            st8<0>(ov, rOut, rowo, q * 16);
        }
    }
    STAMP(10);
}

extern "C" void kernel_launch(void* const* d_in, const int* in_sizes, int n_in, void* d_out, int out_size, void* d_ws, size_t ws_size, hipStream_t stream) {
    (void)in_sizes; (void)n_in; (void)out_size; (void)ws_size;
    (void)hipFuncSetAttribute((const void*)k_fused, hipFuncAttributeMaxDynamicSharedMemorySize, LDS_BYTES);
    k_fused<<<256, 512, LDS_BYTES, stream>>>((const char*)d_in[0], (const char*)d_in[1], (const char*)d_in[2], (const char*)d_in[4], (const char*)d_in[5],
                                            (const char*)d_in[6], (const char*)d_in[7], (char*)d_out, (char*)d_ws);
}
```

```cpp
#include <hip/hip_runtime.h>
#include <stdint.h>

typedef __bf16 bf16x8 __attribute__((ext_vector_type(8)));
typedef __bf16 bf16x4 __attribute__((ext_vector_type(4)));
typedef __bf16 bf16x2 __attribute__((ext_vector_type(2)));
typedef float f32x16 __attribute__((ext_vector_type(16)));
typedef float f32x4 __attribute__((ext_vector_type(4)));
typedef float f32x2 __attribute__((ext_vector_type(2)));
typedef unsigned u32x4 __attribute__((ext_vector_type(4)));
typedef unsigned u32x2 __attribute__((ext_vector_type(2)));
#define LAS __attribute__((address_space(3)))
#define GAS __attribute__((address_space(1)))
#define DI __device__ __forceinline__
typedef __amdgpu_buffer_rsrc_t rsrc_t;
typedef GAS unsigned gu32;

constexpr int S_ = 2048, D_ = 448, F_ = 1792;
constexpr unsigned FL_X1 = 0, FL_X2 = 4096, FL_X3 = 6144, FL_E = 8192;
constexpr unsigned ST_OFF = 512u << 10, O_OFF = 1u << 20, H_OFF = 4u << 20, G_OFF = 6u << 20;
constexpr unsigned ST_BYTES = 64u * 14 * 256, O_BYTES = 64u * 32 * 1024, H_BYTES = 64u * 28 * 1024, G_BYTES = 64u * 112 * 1024;
constexpr int LDS_BYTES = 163840;
constexpr int SC1 = 16;
constexpr unsigned SPIN_CAP = 1u << 18;
#define POLL_SLEEP 5
#define PD3V 10
#define PD4V 16
#define LEAD1 1
#define ROT1 2
#define XBYC 0
#define TOUCH_AT 1
#define TLBW 1
#define OUTAUX 0
#define PRIO 0
#define LPRIO 0

#define STAMP(i)
#define CSTAMP(i)
#define LSTAMP(i)

DI int rfl(int v) { return __builtin_amdgcn_readfirstlane(v); }
DI rsrc_t mk_rsrc(const void* p, unsigned bytes) { return __builtin_amdgcn_make_buffer_rsrc((void*)p, (short)0, (int)bytes, 0x00020000); }
DI void dma16(unsigned ldsaddr, unsigned voff, rsrc_t srd, unsigned soff) {
    asm volatile("s_mov_b32 m0, %0\n\ts_nop 0\n\tbuffer_load_dwordx4 %1, %2, %3 offen lds" ::"s"(ldsaddr), "v"(voff), "s"(srd), "s"(soff) : "memory");
}
DI void dma16_masked(unsigned ldsaddr, unsigned voff, rsrc_t srd, unsigned soff, unsigned long long off_mask) {
    unsigned long long keep;
    asm volatile("s_mov_b64 %0, exec\n\ts_andn2_b64 exec, exec, %5\n\ts_mov_b32 m0, %1\n\ts_nop 0\n\tbuffer_load_dwordx4 %2, %3, %4 offen lds\n\ts_mov_b64 exec, %0"
                 : "=&s"(keep) : "s"(ldsaddr), "v"(voff), "s"(srd), "s"(soff), "s"(off_mask) : "memory", "scc");
}
template <int N> DI void wait_vm() { asm volatile("s_waitcnt vmcnt(%0)" ::"i"(N) : "memory"); }
DI void bar() { asm volatile("s_waitcnt lgkmcnt(0)" ::: "memory"); __builtin_amdgcn_s_barrier(); asm volatile("" ::: "memory"); }
DI void sb() { __builtin_amdgcn_sched_barrier(0); }
template <int AUX> DI u32x4 ld16(rsrc_t r, unsigned voff, unsigned soff) { return __builtin_amdgcn_raw_buffer_load_b128(r, voff, soff, AUX); }
template <int AUX> DI u32x2 ld8(rsrc_t r, unsigned voff, unsigned soff) { return __builtin_amdgcn_raw_buffer_load_b64(r, voff, soff, AUX); }
template <int AUX> DI void st16(u32x4 v, rsrc_t r, unsigned voff, unsigned soff) { __builtin_amdgcn_raw_buffer_store_b128(v, r, voff + soff, 0, AUX); }
template <int AUX> DI void st8(u32x2 v, rsrc_t r, unsigned voff, unsigned soff) { __builtin_amdgcn_raw_buffer_store_b64(v, r, voff, soff, AUX); }
DI bf16x8 as_frag(u32x4 v) { return __builtin_bit_cast(bf16x8, v); }
DI bf16x8 lds_frag(LAS char* p) { return *(LAS bf16x8*)p; }
DI bf16x8 lds_tr(LAS char* p0, LAS char* p1) {
    bf16x4 a = __builtin_amdgcn_ds_read_tr16_b64_v4bf16((LAS bf16x4*)p0);
    bf16x4 b = __builtin_amdgcn_ds_read_tr16_b64_v4bf16((LAS bf16x4*)p1);
    return __builtin_shufflevector(a, b, 0, 1, 2, 3, 4, 5, 6, 7);
}
DI f32x4 mfma16(bf16x8 a, bf16x8 b, f32x4 c) { return __builtin_amdgcn_mfma_f32_16x16x32_bf16(a, b, c, 0, 0, 0); }
DI f32x16 mfma32(bf16x8 a, bf16x8 b, f32x16 c) { return __builtin_amdgcn_mfma_f32_32x32x16_bf16(a, b, c, 0, 0, 0); }
DI void mma16(f32x4& c, bf16x8 a, bf16x8 b) { c = mfma16(a, b, c); asm volatile("" ::"v"(c)); }
DI void mma32(f32x16& c, bf16x8 a, bf16x8 b) { c = mfma32(a, b, c); asm volatile("" ::"v"(c)); }
DI unsigned pk(float a, float b) { f32x2 v = {a, b}; bf16x2 r = __builtin_convertvector(v, bf16x2); return __builtin_bit_cast(unsigned, r); }
DI float bf_lo(unsigned v) { return __builtin_bit_cast(float, v << 16); }
DI float bf_hi(unsigned v) { return __builtin_bit_cast(float, v & 0xffff0000u); }
DI float dot2(unsigned a, unsigned b, float c) { return __builtin_amdgcn_fdot2_f32_bf16(__builtin_bit_cast(bf16x2, a), __builtin_bit_cast(bf16x2, b), c, false); }
DI unsigned f2u(float v) { return __builtin_bit_cast(unsigned, v); }
DI float u2f(unsigned v) { return __builtin_bit_cast(float, v); }
DI float sum16(float v) { auto a = __builtin_amdgcn_permlane16_swap(f2u(v), f2u(v), false, false); unsigned a0 = a[0], a1 = a[1]; return u2f(a0) + u2f(a1); }
DI float sum32(float v) { auto a = __builtin_amdgcn_permlane32_swap(f2u(v), f2u(v), false, false); unsigned a0 = a[0], a1 = a[1]; return u2f(a0) + u2f(a1); }
DI float max16(float v) { auto a = __builtin_amdgcn_permlane16_swap(f2u(v), f2u(v), false, false); unsigned a0 = a[0], a1 = a[1]; return fmaxf(u2f(a0), u2f(a1)); }
DI float max32(float v) { auto a = __builtin_amdgcn_permlane32_swap(f2u(v), f2u(v), false, false); unsigned a0 = a[0], a1 = a[1]; return fmaxf(u2f(a0), u2f(a1)); }
DI f32x16 zero16() { f32x16 z; for (int i = 0; i < 16; ++i) z[i] = 0.f; return z; }

template <int N, int PD, class LD, class MM> DI void frag_pipe(LD ld, MM mm) {
    bf16x8 buf[N];
#pragma unroll
    for (int i = 0; i < PD; ++i) buf[i] = ld(i);
    sb();
#pragma unroll
    for (int i = 0; i < N; ++i) {
        if (i + PD < N) buf[i + PD] = ld(i + PD);
        mm(i, buf[i]);
        sb();
    }
}

DI void flag_set(char* ws, unsigned off, unsigned tag) { __hip_atomic_store((gu32*)(ws + off), tag, __ATOMIC_RELAXED, __HIP_MEMORY_SCOPE_AGENT); }
DI void flag_wait(char* ws, unsigned off, int n, unsigned tag, int lane) {
    gu32* p = (gu32*)(ws + off) + (lane < n ? lane : 0);
    unsigned v0 = __hip_atomic_load(p, __ATOMIC_RELAXED, __HIP_MEMORY_SCOPE_AGENT);
    for (unsigned spin = 0; spin < SPIN_CAP; ++spin) {
        __builtin_amdgcn_s_sleep(POLL_SLEEP);
        const unsigned v1 = __hip_atomic_load(p, __ATOMIC_RELAXED, __HIP_MEMORY_SCOPE_AGENT);
        if (__all(v0 == tag)) break;
        __builtin_amdgcn_s_sleep(POLL_SLEEP);
        v0 = __hip_atomic_load(p, __ATOMIC_RELAXED, __HIP_MEMORY_SCOPE_AGENT);
        if (__all(v1 == tag)) break;
    }
    asm volatile("" ::: "memory");
}

DI unsigned img_src_lane(int lane) {
    const int i8 = lane >> 3, p8 = lane & 7;
    return (4 * ((p8 >> 2) ^ ((i8 >> 1) & 1)) + (p8 & 3)) * 16;
}
DI unsigned img_rd_lane(int lane) {
    const int G = lane >> 4, nh = G & 1, hh = G >> 1, q = (lane & 15) >> 2, p = lane & 3;
    return (4 * hh + q) * 128 + (q >> 1) * 64 + nh * 32 + 8 * p;
}

constexpr int SL1 = 32768, SL1W = 10240, L_TAB1 = 161792;
constexpr int L_STG = 131072;
DI constexpr int wo_pos(int m) { return m < 14 ? m * 4096 : 114688 + (m - 14) * 4096; }
constexpr int L_W1A = 0, L_W1B = 57344, L_RED = 147456;
constexpr int W2SLOT = 16384;

__global__ __launch_bounds__(512, 2) void k_fused(const char* __restrict__ x, const char* __restrict__ wqkv, const char* __restrict__ wo,
                                                  const char* __restrict__ w1, const char* __restrict__ b1, const char* __restrict__ w2,
                                                  const char* __restrict__ b2, char* __restrict__ out, char* __restrict__ ws) {
    extern __shared__ __attribute__((aligned(16))) char smem_[];
    LAS char* smem = (LAS char*)smem_;
    const unsigned lbase = (unsigned)(size_t)smem;
    const int tid = threadIdx.x, lane = tid & 63, w = rfl(tid >> 6);
    const int b = blockIdx.x, slot = b >> 3, T64 = (b & 7) * 4 + (slot >> 3), mem = slot & 7;
    const int PP = T64 >> 1, js = 7 * (T64 & 1) + mem;
    const int t0 = T64 * 64;
    const bool loader = w >= 4;
    const int rot1 = ROT1 == 0 ? 0 : rfl(ROT1 == 1 ? (2 * (T64 & 3) + mem) % 7 : (ROT1 == 2 ? (2 * (T64 & 3)) % 7 : mem % 7));
    STAMP(0);

    if (loader) {
        __builtin_amdgcn_s_setprio(LPRIO);
        const int lw = w - 4, i8 = lane >> 3;
        const rsrc_t rX = mk_rsrc(x, S_ * D_ * 2), rW = mk_rsrc(wqkv, D_ * 3 * D_ * 2);
        unsigned xv[3], xl[3];
#pragma unroll
        for (int m = 0; m < 3; ++m) {
            int rb = lw + 4 * m; rb = rb > 9 ? 9 : rb;
            const int rho = 8 * rb + i8;
            int tok = t0 - 16 + rho; tok = tok < 0 ? 0 : tok;
            xv[m] = tok * 896 + ((lane & 7) ^ ((rho >> 1) & 7)) * 16;
            xl[m] = lbase + rb * 1024;
        }
        const unsigned long long x0off = lw == 0 ? 0xFFull : 0ull;
        unsigned wv[6], wu[6]; unsigned long long woff[6];
#pragma unroll
        for (int m = 0; m < 6; ++m) {
            int u = lw + 4 * m; u = u > 21 ? 21 : u;
            const int c = 64 * u + lane, rs = c / 22, cc = c - rs * 22;
            const int q = rs & 31, bb = q >> 3, ii = q & 7;
            const int krow = (rs & 32) + (bb & 1) * 16 + (bb >> 1) * 4 + (ii & 3) + 8 * (ii >> 2);
            const int sec = cc / 7;
            wv[m] = krow * 2688 + (sec == 2 ? 0 : (sec + 1) * 896) + mem * 112 + (cc - sec * 7) * 16;
            wu[m] = lbase + SL1W + u * 1024;
            woff[m] = __ballot(cc == 21);
        }
        const bool lo2 = lw < 2;
        auto stage1 = [&](int st) {
            const unsigned sl = ((st + 1) & 3) * SL1;
            int kc = st + rot1; kc = kc >= 7 ? kc - 7 : kc;
            if (ROT1 != 0) { kc = rfl(kc); asm volatile("s_nop 4" : "+s"(kc)); }
            dma16_masked(xl[0] + sl, xv[0], rX, kc * 128, x0off);
            dma16(xl[1] + sl, xv[1], rX, kc * 128);
            if (lo2) dma16(xl[2] + sl, xv[2], rX, kc * 128);
#pragma unroll
            for (int m = 0; m < 5; ++m) dma16_masked(wu[m] + sl, wv[m], rW, kc * 64 * 2688, woff[m]);
            if (lo2) dma16_masked(wu[5] + sl, wv[5], rW, kc * 64 * 2688, woff[5]);
        };
        stage1(0); stage1(1); stage1(2);
        for (int st = 0; st < 7; ++st) {
            if (st >= 5) wait_vm<0>(); else if (lo2) wait_vm<9>(); else wait_vm<7>();
            bar();
            if (st <= 3) stage1(st + 3);
        }
        if (mem == 7) {
            bar(); bar(); bar();
            const int pi = (T64 & 3) * 4 + lw;
            unsigned acc = 0;
#pragma unroll 1
            for (int mtx = 0; mtx < 2; ++mtx) {
                const rsrc_t r = mk_rsrc(mtx ? w2 : w1, D_ * F_ * 2);
                unsigned v[13];
#pragma unroll
                for (int m = 0; m < 13; ++m) { const int l = (m * 16 + pi) * 64 + lane; v[m] = __builtin_amdgcn_raw_buffer_load_b32(r, l < 12544 ? l * 128 : 0x80000000u, 0, 0); }
#pragma unroll
                for (int m = 0; m < 13; ++m) acc ^= v[m];
            }
            asm volatile("" ::"v"(acc));
            return;
        }
        const unsigned isl = img_src_lane(lane);
        const rsrc_t rWo = mk_rsrc(wo, D_ * D_ * 2);
        const unsigned vo = (8 * lw + i8) * 896 + mem * 128 + isl;
        auto wo_unit = [&](int m) { dma16(lbase + wo_pos(m) + lw * 1024, vo, rWo, (m) * 28 * 896); };
#pragma unroll
        for (int m = 0; m < 12; ++m) wo_unit(m);
        LSTAMP(0);
        bar();
#pragma unroll
        for (int m = 12; m < 16; ++m) wo_unit(m);
        const rsrc_t rW1 = mk_rsrc(w1, D_ * F_ * 2);
        const unsigned v1o = i8 * 3584 + js * 256 + isl;
        auto w1_so = [&](int m) { return (unsigned)((lw + 4 * m) * 8 * 3584); };
#pragma unroll
        for (int m = 0; m < 14; ++m) dma16(lbase + L_W1B + (lw + 4 * m) * 1024, v1o + 128, rW1, w1_so(m));
        LSTAMP(1);
        bar();
        u32x4 w1r[14];
#pragma unroll
        for (int m = 0; m < 14; ++m) w1r[m] = ld16<0>(rW1, v1o, w1_so(m));
        LSTAMP(2);
        bar();
        const rsrc_t rW2 = mk_rsrc(w2, F_ * D_ * 2);
        const unsigned v2o = (8 * lw + i8) * 896 + mem * 128 + isl;
        u32x4 wr[20];
#pragma unroll
        for (int m = 0; m < 20; ++m) wr[m] = ld16<0>(rW2, v2o, (((m & 3) >> 1) * 896 + 64 * (9 + (m >> 2)) + 32 * (m & 1)) * 896);
        LSTAMP(3);
        wait_vm<48>();
        LSTAMP(4);
        bar();
        wait_vm<0>();
        bar();
#pragma unroll
        for (int m = 0; m < 14; ++m) *(LAS u32x4*)(smem + L_W1A + (lw + 4 * m) * 1024 + lane * 16) = w1r[m];
        LSTAMP(5);
        bar();
        auto kst = [&](int st) { return st; };
        auto w2_stage = [&](int st) {
#pragma unroll
            for (int m = 0; m < 4; ++m)
                dma16(lbase + ((st + 7) % 9) * W2SLOT + (m >> 1) * 8192 + ((m & 1) * 4 + lw) * 1024, v2o, rW2, ((m >> 1) * 896 + 64 * kst(st) + 32 * (m & 1)) * 896);
        };
        w2_stage(0); w2_stage(1);
        bar();
        bar();
        w2_stage(2); w2_stage(3); w2_stage(4);
        LSTAMP(6);
        bar();
        w2_stage(5); w2_stage(6); w2_stage(7); w2_stage(8);
        LSTAMP(7);
        wait_vm<16>();
        LSTAMP(8);
        bar();
        wait_vm<0>();
        bar();
#pragma unroll
        for (int m = 0; m < 20; ++m)
            *(LAS u32x4*)(smem + ((9 + (m >> 2) + 7) % 9) * W2SLOT + ((m & 3) >> 1) * 8192 + ((m & 1) * 4 + lw) * 1024 + lane * 16) = wr[m];
        bar();
        bar();
        return;
    }

    __builtin_amdgcn_s_setprio(PRIO);
    unsigned tag = 0;
    {
        const u32x4 ones = {0x3F803F80u, 0x3F803F80u, 0x3F803F80u, 0x3F803F80u};
        if (lane < 8) *(LAS u32x4*)(smem + w * SL1 + lane * 16) = ones;
        const u32x4 e0 = {0x00003F80u, 0u, 0u, 0u};
        *(LAS u32x4*)(smem + w * SL1 + SL1W + lane * 352 + 336) = e0;
        if (w == 0) tag = __hip_atomic_load((gu32*)(ws + FL_E + PP * 128), __ATOMIC_RELAXED, __HIP_MEMORY_SCOPE_AGENT) + 1u;
    }
    unsigned tch = 0;
    auto touch = [&]() {
        const char* tp = w == 0 ? wqkv : (w == 1 ? w1 : (w == 2 ? w2 : wo));
        const unsigned per = w == 0 ? 4704u : (w == 3 ? 1568u : 6272u);
        const rsrc_t rT = mk_rsrc(tp, per * 256);
        unsigned o = b * per + lane * 128; o = lane * 128 < per ? o : 0x80000000u;
        tch = __builtin_amdgcn_raw_buffer_load_b32(rT, o, 0, 0);
    };
    if (w == 0) touch();
    if (w == 2) {
        const rsrc_t rWs = mk_rsrc(ws, 16u << 20);
        unsigned o = 0x80000000u;
        if (lane < 2) o = O_OFF + ((2 * T64 + lane) * 32 + 4 * mem) * 1024;
        else if (mem < 7) {
            if (lane < 4) o = H_OFF + ((2 * T64 + lane - 2) * 28 + 4 * mem) * 1024;
            else if (lane < 6) o = ST_OFF + ((2 * T64 + lane - 4) * 14 + 2 * mem) * 256;
            else if (lane < 10) o = G_OFF + ((4 * PP + lane - 6) * 112 + 8 * js) * 1024;
        }
        __builtin_amdgcn_raw_buffer_store_b32(0u, rWs, o, 0, SC1);
    }
    if (w == 3 && mem < 7) {
        const rsrc_t rOutW = mk_rsrc(out, S_ * D_ * 2);
        __builtin_amdgcn_raw_buffer_store_b32(0u, rOutW, lane < 4 ? (t0 + 16 * lane) * 896 + mem * 128 : 0x80000000u, 0, 0);
    }
    unsigned tb = 0;
    if (w == 1) tb = __builtin_amdgcn_raw_buffer_load_b32(mk_rsrc(lane < 32 ? b1 : b1, F_ * 2), 0, 0, 0) ^ __builtin_amdgcn_raw_buffer_load_b32(mk_rsrc(b2, D_ * 2), 0, 0, 0);
    const int t = lane & 15, g = lane >> 4;
#pragma unroll
    for (int m = 0; m < 2; ++m) {
        const int it = tid + 256 * m, blk = it >> 4, tt = it & 15;
        if (it < 480) {
            const int sv_ = ((tt >> 1) & 1) | (((tt >> 3) & 1) << 1) | (((tt >> 2) & 1) << 2);
            const int off = (blk >= 10 && blk < 20) ? tt * 64 + ((7 ^ sv_) * 8) : 768 + tt * 16 + 8;
            *(LAS u32x2*)(smem + L_STG + blk * 1024 + off) = (u32x2){0u, 0u};
        }
    }
    LAS char* edst[3]; bool eok[3];
    {
        const int svt = ((t >> 1) & 1) | (((t >> 3) & 1) << 1) | (((t >> 2) & 1) << 2);
#pragma unroll
        for (int i = 0; i < 3; ++i) {
            const int n0 = (w * 3 + i) * 16 + 4 * g;
            const int arr = (n0 >= 56 ? 1 : 0) + (n0 >= 112 ? 1 : 0), rem = n0 - arr * 56, hh = rem >= 28 ? 1 : 0, dim = rem - 28 * hh;
            const int off = (arr == 1) ? t * 64 + (((dim >> 2) ^ svt) * 8) : (dim >> 3) * 256 + t * 16 + ((dim >> 2) & 1) * 8;
            edst[i] = smem + (n0 < 168 ? L_STG + (arr * 2 + hh) * 5 * 1024 + off : L_STG);
            eok[i] = n0 < 168;
            asm volatile("" : "+v"(edst[i]));
        }
    }
    const int uu = t - 4 * g, r0 = t & 1;
    const bool curA = uu - r0 >= 0, curB = uu - r0 - 2 >= 0;
    const bool noprev = (T64 == 0 && w == 0);
    const bool okA = curA || !noprev, okB = curB || !noprev;
    LAS char* aqk = smem + (L_STG + w * 1024 + lane * 16);
    LAS char* avv[2];
    unsigned odst;
    {
        const int vrow = 4 * g + ((lane & 15) >> 2);
        const int svr = ((vrow >> 1) & 1) | (((vrow >> 3) & 1) << 1) | (((vrow >> 2) & 1) << 2);
        avv[0] = smem + (L_STG + 10240 + w * 1024 + vrow * 64 + (((lane & 3) ^ svr) * 8)); avv[1] = smem + (L_STG + 10240 + w * 1024 + vrow * 64 + (((4 + (lane & 3)) ^ svr) * 8));
        const int T16 = 4 * T64 + w;
        odst = ((T16 >> 1) * 32 + (g >> 1) + 4 * mem) * 1024 + (32 * (g & 1) + 16 * (T16 & 1) + t) * 16;
        asm volatile("" : "+v"(aqk), "+v"(avv[0]), "+v"(avv[1]), "+v"(odst));
    }
    f32x4 acc1[5][3];
#pragma unroll
    for (int a = 0; a < 5; ++a)
#pragma unroll
        for (int i = 0; i < 3; ++i) acc1[a][i] = (f32x4){0.f, 0.f, 0.f, 0.f};
    {
        const int sg = (t >> 1) & 7;
        LAS char* xa[2]; LAS char* wa[2];
        xa[0] = smem + (SL1 + (t >> 3) * 1024 + (t & 7) * 128 + ((g ^ sg) * 16)); xa[1] = smem + (SL1 + (t >> 3) * 1024 + (t & 7) * 128 + (((4 + g) ^ sg) * 16));
        wa[0] = smem + (SL1 + SL1W + (8 * (g >> 1) + 4 * (g & 1) + ((lane & 15) >> 2)) * 352 + 8 * (lane & 3) + w * 96); wa[1] = wa[0] + 11264;
        auto trd = [&](LAS char* p) { return __builtin_amdgcn_ds_read_tr16_b64_v4bf16((LAS bf16x4*)p); };
#pragma unroll
        for (int a = 0; a < 5; ++a)
#pragma unroll
            for (int i = 0; i < 3; ++i) asm volatile("" : "+v"(acc1[a][i]));
        auto gemm1 = [&](auto W3) {
            constexpr bool w3 = decltype(W3)::v;
            bf16x4 wl[2][3], wh[2][3]; bf16x8 xf[2][5];
            auto rd = [&](int ks, int j) {
                if (j == 0) wl[ks][0] = trd(wa[ks]);
                else if (j == 1) wh[ks][0] = trd(wa[ks] + 5632);
                else if (j <= 6) xf[ks][j - 2] = lds_frag(xa[ks] + (j - 2) * 2048);
                else if (w3 && j >= 9) { }
                else if (j & 1) wl[ks][(j - 5) >> 1] = trd(wa[ks] + ((j - 5) >> 1) * 32);
                else wh[ks][(j - 6) >> 1] = trd(wa[ks] + 5632 + ((j - 6) >> 1) * 32);
            };
            bar();
            STAMP(1);
            if (w != 0) touch();
#pragma unroll
            for (int j = 0; j < 11; ++j) rd(0, j);
            xa[0] += SL1; wa[0] += SL1;
            sb();
#pragma unroll 1
            for (int st = 0; st < 7; ++st) {
                const int dd[2] = {((st + 3) & 3) == 0 ? -3 * SL1 : SL1, ((st + 2) & 3) == 0 ? -3 * SL1 : SL1};
#pragma unroll
                for (int ks = 0; ks < 2; ++ks) {
#pragma unroll
                    for (int n = 0; n < 15; ++n) {
                        const int i = n / 5, a = n % 5;
                        if (w3 && i == 2) mma16(acc1[a][2], xf[ks][a], xf[ks][a]);
                        else mma16(acc1[a][i], __builtin_shufflevector(wl[ks][i], wh[ks][i], 0, 1, 2, 3, 4, 5, 6, 7), xf[ks][a]);
                        if (n >= 1 && n < 12) rd(ks ^ 1, n - 1);
                        if (n == 13) { xa[ks ^ 1] += dd[ks ^ 1]; wa[ks ^ 1] += dd[ks ^ 1]; asm volatile("" : "+v"(xa[ks ^ 1]), "+v"(wa[ks ^ 1])); }
                        sb();
                    }
                }
                if (st < 6) { asm volatile("" ::: "memory"); __builtin_amdgcn_s_barrier(); asm volatile("" ::: "memory"); }
            }
        };
        struct BT { enum { v = 1 }; }; struct BF { enum { v = 0 }; };
        if (w == 3) gemm1(BT{}); else gemm1(BF{});
    }
    STAMP(2);
    LAS float* tst = (LAS float*)(smem + L_TAB1);
    if (w == 3) {
        const int r = t & 3;
        float d[5];
#pragma unroll
        for (int a = 0; a < 5; ++a) {
            const float da = (r & 1) ? acc1[a][2][1] : acc1[a][2][0], db = (r & 1) ? acc1[a][2][3] : acc1[a][2][2];
            d[a] = (r & 2) ? db : da;
        }
        LAS float* tp = tst + 2 * t;
        if (g == 2) {
#pragma unroll
            for (int a = 0; a < 5; ++a) tp[32 * a] = acc1[a][1][0];
        }
        if (g == (t >> 2)) {
#pragma unroll
            for (int a = 0; a < 5; ++a) tp[32 * a + 1] = d[a];
        }
    }
    asm volatile("" ::"v"(tch));
    asm volatile("" ::"v"(tb));
    wait_vm<0>();
    bar();
    STAMP(11);
    {
        f32x2 nmu[5], rs[5];
#pragma unroll
        for (int a = 0; a < 5; ++a) {
            const f32x2 s = *(LAS f32x2*)(tst + (16 * a + t) * 2);
            const float mu = s[0] * (1.0f / D_);
            const float r = __builtin_amdgcn_rsqf(fmaxf(s[1] * (1.0f / D_) - mu * mu, 0.0f) + 1e-5f);
            nmu[a] = (f32x2){-mu, -mu}; rs[a] = (f32x2){r, r};
        }
#pragma unroll
        for (int i = 0; i < 3; ++i) {
            f32x4 cs;
#pragma unroll
            for (int r = 0; r < 4; ++r) { const float c = acc1[0][i][r]; cs[r] = u2f(__builtin_amdgcn_update_dpp(0, (int)f2u(c), 0x150, 0xf, 0xf, true)); }
            u32x2 pv[5];
#pragma unroll
            for (int a = 0; a < 5; ++a) {
                const f32x2 lo = {acc1[a][i][0], acc1[a][i][1]}, hi = {acc1[a][i][2], acc1[a][i][3]};
                const f32x2 v01 = (nmu[a] * (f32x2){cs[0], cs[1]} + lo) * rs[a], v23 = (nmu[a] * (f32x2){cs[2], cs[3]} + hi) * rs[a];
                pv[a] = (u32x2){pk(v01[0], v01[1]), pk(v23[0], v23[1])};
            }
            if (eok[i]) {
#pragma unroll
                for (int a = 0; a < 5; ++a) *(LAS u32x2*)(edst[i] + a * 1024) = pv[a];
            }
        }
    }
    STAMP(12);
    bar();
    STAMP(13);
    const rsrc_t rO = mk_rsrc(ws + O_OFF, O_BYTES);
    {
        const float scl = 0.18898223650461362f * 1.4426950408889634f;
        const f32x4 z = {0.f, 0.f, 0.f, 0.f};
        bf16x8 qf[2], kp[2], kc[2], vf[2][2];
#pragma unroll
        for (int hh = 0; hh < 2; ++hh) {
            qf[hh] = lds_frag(aqk + 20480 + 1024 + hh * 5120); kp[hh] = lds_frag(aqk + hh * 5120); kc[hh] = lds_frag(aqk + 1024 + hh * 5120);
        }
#pragma unroll
        for (int hh = 0; hh < 2; ++hh)
#pragma unroll
            for (int dt = 0; dt < 2; ++dt) vf[hh][dt] = lds_tr(avv[dt] + hh * 5120, avv[dt] + hh * 5120 + 1024);
#pragma unroll
        for (int hh = 0; hh < 2; ++hh) {
            const f32x4 sp = mfma16(kp[hh], qf[hh], z), sc = mfma16(kc[hh], qf[hh], z);
            const float ac = r0 ? sc[1] : sc[0], ap = r0 ? sp[1] : sp[0], bc = r0 ? sc[3] : sc[2], bp = r0 ? sp[3] : sp[2];
            const float za = okA ? (curA ? ac : ap) * scl : -3.0e38f, zb = okB ? (curB ? bc : bp) * scl : -3.0e38f;
            const float mx = max32(max16(fmaxf(za, zb)));
            const float ea = __builtin_amdgcn_exp2f(za - mx), eb = __builtin_amdgcn_exp2f(zb - mx);
            const float inv = __builtin_amdgcn_rcpf(sum32(sum16(ea + eb)));
            const unsigned pw = pk(ea * inv, eb * inv);
            const unsigned wa_ = (pw & 0xffffu) << (16 * r0), wb_ = (pw >> 16) << (16 * r0);
            const u32x4 pu = {curA ? 0u : wa_, curB ? 0u : wb_, curA ? wa_ : 0u, curB ? wb_ : 0u};
            const f32x4 o0 = mfma16(vf[hh][0], as_frag(pu), z), o1 = mfma16(vf[hh][1], as_frag(pu), z);
            const u32x4 ou = {pk(o0[0], o0[1]), pk(o0[2], o0[3]), pk(o1[0], o1[1]), pk(o1[2], o1[3])};
            st16<SC1>(ou, rO, odst, hh * 2048);
        }
    }
    STAMP(3);
    wait_vm<0>();
    STAMP(14);
    bar();
    STAMP(15);
    if (w == 0) { tag = rfl(tag); if (lane == 0) flag_set(ws, FL_X1 + T64 * 128 + mem * 4, tag); }
    if (mem == 7) return;
    const int th = w & 1, kh = w >> 1, t32 = lane & 31, hl = lane >> 5;
    const int T32 = 2 * T64 + th;
    const unsigned colb = (64 * mem + 32 * kh) * 2;
    const unsigned rowo = (T32 * 32 + t32) * 896 + colb + hl * 8;
    u32x2 xr[4];
    {
        const rsrc_t rX = mk_rsrc(x, S_ * D_ * 2);
#pragma unroll
        for (int q = 0; q < 4; ++q) xr[q] = ld8<0>(rX, rowo, q * 16);
    }
    if (w == 0) flag_wait(ws, FL_X1 + T64 * 128, 8, tag, lane);
    bar();
    STAMP(4);
    const unsigned ird = img_rd_lane(lane);
    const rsrc_t rH = mk_rsrc(ws + H_OFF, H_BYTES), rSt = mk_rsrc(ws + ST_OFF, ST_BYTES);
    {
        constexpr int PD2 = 8;
        u32x4 of[16];
        const unsigned oso = (T32 * 32 + 16 * kh) * 1024;
        auto o_so = [&](int s) { return (unsigned)(s * 1024); };
#pragma unroll
        for (int s = 0; s < PD2; ++s) of[s] = ld16<SC1>(rO, lane * 16, oso + o_so(s));
        sb();
        f32x16 a2[2] = {zero16(), zero16()};
        const unsigned wo_hi = kh ? 114688 - 14 * 4096 : 0;
        frag_pipe<32, 4>([&](int n) { const int s = n >> 1, hp_ = s >> 1; LAS char* a = smem + (ird ^ ((n & 1) << 6)) + kh * 8 * 4096 + hp_ * 4096 + (hp_ >= 6 ? wo_hi : 0) + (s & 1) * 1024; return lds_tr(a, a + 2048); },
                         [&](int n, bf16x8 f) { const int s = n >> 1; if ((n & 1) == 0 && s + PD2 < 16) of[s + PD2] = ld16<SC1>(rO, lane * 16, oso + o_so(s + PD2)); mma32(a2[n & 1], f, as_frag(of[s])); });
        const f32x16 keep = kh ? a2[1] : a2[0], give = kh ? a2[0] : a2[1];
        LAS char* red = smem + L_RED;
#pragma unroll
        for (int q = 0; q < 4; ++q) *(LAS f32x4*)(red + (w * 4 + q) * 1024 + lane * 16) = (f32x4){give[4 * q], give[4 * q + 1], give[4 * q + 2], give[4 * q + 3]};
        bar();
        float hv[16];
        float s1 = 0.f, s2 = 0.f;
        unsigned hp[8];
#pragma unroll
        for (int q = 0; q < 4; ++q) {
            const f32x4 r = *(LAS f32x4*)(red + ((w ^ 2) * 4 + q) * 1024 + lane * 16);
            const unsigned xa = xr[q].x, xb = xr[q].y;
            hv[4 * q] = bf_lo(xa) + (keep[4 * q] + r[0]); hv[4 * q + 1] = bf_hi(xa) + (keep[4 * q + 1] + r[1]);
            hv[4 * q + 2] = bf_lo(xb) + (keep[4 * q + 2] + r[2]); hv[4 * q + 3] = bf_hi(xb) + (keep[4 * q + 3] + r[3]);
            hp[2 * q] = pk(hv[4 * q], hv[4 * q + 1]); hp[2 * q + 1] = pk(hv[4 * q + 2], hv[4 * q + 3]);
        }
        st16<SC1>((u32x4){hp[0], hp[1], hp[2], hp[3]}, rH, lane * 16, (T32 * 28 + 4 * mem + 2 * kh) * 1024);
        st16<SC1>((u32x4){hp[4], hp[5], hp[6], hp[7]}, rH, lane * 16, (T32 * 28 + 4 * mem + 2 * kh + 1) * 1024);
#pragma unroll
        for (int q = 0; q < 8; ++q) { const float lo = bf_lo(hp[q]), hi = bf_hi(hp[q]); s1 += lo + hi; s2 = fmaf(lo, lo, fmaf(hi, hi, s2)); }
        s1 = sum32(s1); s2 = sum32(s2);
        if (hl == 0) st8<SC1>((u32x2){f2u(s1), f2u(s2)}, rSt, t32 * 8, (T32 * 14 + 2 * mem + kh) * 256);
    }
    STAMP(5);
    unsigned short b1v = 0;
    if (tid < 128) { const rsrc_t rB1 = mk_rsrc(b1, F_ * 2); b1v = __builtin_amdgcn_raw_buffer_load_b16(rB1, (js * 128 + tid) * 2, 0, 0); }
    wait_vm<0>();
    STAMP(16);
    bar();
    STAMP(17);
    if (w == 0 && lane == 0) flag_set(ws, FL_X2 + PP * 128 + js * 4, tag);
    LAS float* tab = (LAS float*)(smem + L_RED);
    if (tid < 128) tab[128 + tid] = u2f((unsigned)b1v << 16);
    {
        const u32x4 onesu = {0x3F803F80u, 0x3F803F80u, 0x3F803F80u, 0x3F803F80u};
        f32x16 cs = zero16();
        LAS char* a0 = smem + (w >> 1) * 57344 + (ird ^ ((w & 1) << 6));
        frag_pipe<28, 4>([&](int s) { return lds_tr(a0 + s * 2048, a0 + s * 2048 + 1024); }, [&](int s, bf16x8 f) { mma32(cs, f, as_frag(onesu)); });
        if (t32 == 0) {
#pragma unroll
            for (int q = 0; q < 4; ++q) *(LAS f32x4*)(tab + 32 * w + 8 * q + 4 * hl) = (f32x4){cs[4 * q], cs[4 * q + 1], cs[4 * q + 2], cs[4 * q + 3]};
        }
    }
    if (w == 0) {
        flag_wait(ws, FL_X2 + PP * 128, 14, tag, lane);
        if (js == 0 && lane == 0) flag_set(ws, FL_E + PP * 128, tag);
    }
    bar();
    STAMP(6);
    const rsrc_t rG = mk_rsrc(ws + G_OFF, G_BYTES);
    {
        const int TP = 4 * PP + w;
        u32x2 sv[7];
#pragma unroll
        for (int q = 0; q < 7; ++q) sv[q] = ld8<SC1>(rSt, t32 * 8 + hl * 256, (TP * 14 + 2 * q) * 256);
        constexpr int PD3 = PD3V;
        u32x4 hf[28];
        const unsigned hso = TP * 28 * 1024;
        auto h_so = [&](int s) { return (unsigned)(s * 1024); };
#pragma unroll
        for (int s = 0; s < PD3; ++s) hf[s] = ld16<SC1>(rH, lane * 16, hso + h_so(s));
        sb();
        f32x16 a3[4] = {zero16(), zero16(), zero16(), zero16()};
        LAS char* wA = smem + L_W1A; LAS char* wB = smem + L_W1B;
        frag_pipe<56, 4>([&](int n) { LAS char* a = wA + (ird ^ ((n & 1) << 6)) + (n >> 1) * 2048; return lds_tr(a, a + 1024); },
                         [&](int n, bf16x8 f) { const int s = n >> 1; if ((n & 1) == 0 && s + PD3 < 28) hf[s + PD3] = ld16<SC1>(rH, lane * 16, hso + h_so(s + PD3)); mma32(a3[n & 1], f, as_frag(hf[s])); });
        float s1 = 0.f, s2 = 0.f;
#pragma unroll
        for (int q = 0; q < 7; ++q) { const unsigned sa = sv[q].x, sb_ = sv[q].y; s1 += u2f(sa); s2 += u2f(sb_); }
        s1 = sum32(s1); s2 = sum32(s2);
        const float mu = s1 * (1.0f / D_);
        const float rstd = __builtin_amdgcn_rsqf(fmaxf(s2 * (1.0f / D_) - mu * mu, 0.0f) + 1e-5f);
        const f32x2 rs2 = {rstd, rstd}, nm2 = {-mu * rstd, -mu * rstd};
        const f32x2 c02 = {-2.0f * 0.7978845608028654f * 1.4426950408889634f, -2.0f * 0.7978845608028654f * 1.4426950408889634f};
        const f32x2 c12 = c02 * 0.044715f, one2 = {1.0f, 1.0f};
        STAMP(18);
        bar();
        f32x4 tcs_[16], tbb_[16];
        unsigned gq[8];
        LAS float* tabl = tab + 4 * hl;
        asm volatile("" : "+v"(tabl));
        auto gelu_ld = [&](int m) { const int i = m >> 2, q = m & 3; tcs_[m] = *(LAS f32x4*)(tabl + 32 * i + 8 * q); tbb_[m] = *(LAS f32x4*)(tabl + 128 + 32 * i + 8 * q); };
        auto gelu_do = [&](int m) {
            const int i = m >> 2, q = m & 3;
            const f32x4 cs = tcs_[m], bb = tbb_[m];
            f32x2 y0 = (f32x2){a3[i][4 * q], a3[i][4 * q + 1]} * rs2 + (nm2 * (f32x2){cs[0], cs[1]} + (f32x2){bb[0], bb[1]});
            f32x2 y1 = (f32x2){a3[i][4 * q + 2], a3[i][4 * q + 3]} * rs2 + (nm2 * (f32x2){cs[2], cs[3]} + (f32x2){bb[2], bb[3]});
            const f32x2 z0 = y0 * ((y0 * y0) * c12 + c02), z1 = y1 * ((y1 * y1) * c12 + c02);
            const f32x2 d0 = (f32x2){__builtin_amdgcn_exp2f(z0[0]), __builtin_amdgcn_exp2f(z0[1])} + one2;
            const f32x2 d1 = (f32x2){__builtin_amdgcn_exp2f(z1[0]), __builtin_amdgcn_exp2f(z1[1])} + one2;
            y0 = y0 * (f32x2){__builtin_amdgcn_rcpf(d0[0]), __builtin_amdgcn_rcpf(d0[1])};
            y1 = y1 * (f32x2){__builtin_amdgcn_rcpf(d1[0]), __builtin_amdgcn_rcpf(d1[1])};
            gq[2 * q] = pk(y0[0], y0[1]); gq[2 * q + 1] = pk(y1[0], y1[1]);
            if (q & 1) st16<SC1>((u32x4){gq[2 * q - 2], gq[2 * q - 1], gq[2 * q], gq[2 * q + 1]}, rG, lane * 16, (TP * 112 + 8 * js + 2 * i + (q >> 1)) * 1024);
        };
        frag_pipe<28, 4>([&](int s) { LAS char* a = wB + ird + s * 2048; return lds_tr(a, a + 1024); },
                         [&](int s, bf16x8 f) { if (s % 3 == 0 && s / 3 < 8) gelu_ld(s / 3); mma32(a3[2], f, as_frag(hf[s])); if (s % 3 == 2 && s / 3 < 8) gelu_do(s / 3); });
        frag_pipe<28, 4>([&](int s) { LAS char* a = wB + (ird ^ 64) + s * 2048; return lds_tr(a, a + 1024); },
                         [&](int s, bf16x8 f) { if (s % 6 == 1 && s / 6 < 4) gelu_ld(8 + s / 6); if (s >= 24) gelu_ld(12 + s - 24); mma32(a3[3], f, as_frag(hf[s])); if (s % 6 == 4 && s / 6 < 4) gelu_do(8 + s / 6); });
#pragma unroll
        for (int q = 0; q < 4; ++q) gelu_do(12 + q);
    }
    STAMP(7);
    wait_vm<0>();
    STAMP(19);
    bar();
    if (w == 0 && lane == 0) flag_set(ws, FL_X3 + PP * 128 + js * 4, tag);
    u32x4 hres[2]; u32x2 b2v[4];
    {
        const rsrc_t rB2 = mk_rsrc(b2, D_ * 2);
        hres[0] = ld16<SC1>(rH, lane * 16, (T32 * 28 + 4 * mem + 2 * kh) * 1024);
        hres[1] = ld16<SC1>(rH, lane * 16, (T32 * 28 + 4 * mem + 2 * kh + 1) * 1024);
#pragma unroll
        for (int q = 0; q < 4; ++q) b2v[q] = ld8<0>(rB2, colb + hl * 8, q * 16);
    }
    if (w == 0) flag_wait(ws, FL_X3 + PP * 128, 14, tag, lane);
    bar();
    STAMP(8);
    {
        constexpr int PD = PD4V;
        u32x4 gf[56];
        const unsigned gso = (T32 * 112 + 56 * kh) * 1024;
        auto g_so = [&](int s) { return (unsigned)(s * 1024); };
#pragma unroll
        for (int s = 0; s < PD; ++s) gf[s] = ld16<SC1>(rG, lane * 16, gso + g_so(s));
        sb();
        f32x16 a4[2] = {zero16(), zero16()};
        LAS char* wbp = smem + kh * 8192;
        auto run = [&](auto S0, auto NS) {
            constexpr int s0 = decltype(S0)::v, ns = decltype(NS)::v;
            frag_pipe<2 * ns, 4>(
                [&](int n) { const int s = s0 + (n >> 1); LAS char* a = wbp + (((s >> 2) + 7) % 9) * W2SLOT + (s & 3) * 2048 + (ird ^ ((n & 1) << 6)); return lds_tr(a, a + 1024); },
                [&](int n, bf16x8 f) { const int s = s0 + (n >> 1); if ((n & 1) == 0 && s + PD < 56) gf[s + PD] = ld16<SC1>(rG, lane * 16, gso + g_so(s + PD)); mma32(a4[n & 1], f, as_frag(gf[s])); });
        };
        struct I0 { enum { v = 0 }; }; struct I20 { enum { v = 20 }; }; struct I16 { enum { v = 16 }; }; struct I36 { enum { v = 36 }; };
        run(I0{}, I20{});
        bar();
        run(I20{}, I16{});
        bar();
        run(I36{}, I20{});
        STAMP(9);
        const f32x16 keep = kh ? a4[1] : a4[0], give = kh ? a4[0] : a4[1];
        LAS char* red = smem + L_RED;
#pragma unroll
        for (int q = 0; q < 4; ++q) *(LAS f32x4*)(red + (w * 4 + q) * 1024 + lane * 16) = (f32x4){give[4 * q], give[4 * q + 1], give[4 * q + 2], give[4 * q + 3]};
        bar();
        const rsrc_t rOut = mk_rsrc(out, S_ * D_ * 2);
#pragma unroll
        for (int q = 0; q < 4; ++q) {
            const f32x4 r = *(LAS f32x4*)(red + ((w ^ 2) * 4 + q) * 1024 + lane * 16);
            const unsigned ha = hres[q >> 1][2 * (q & 1)], hb = hres[q >> 1][2 * (q & 1) + 1], ba = b2v[q].x, bb = b2v[q].y;
            const u32x2 ov = {pk(bf_lo(ha) + (keep[4 * q] + r[0] + bf_lo(ba)), bf_hi(ha) + (keep[4 * q + 1] + r[1] + bf_hi(ba))),
                              pk(bf_lo(hb) + (keep[4 * q + 2] + r[2] + bf_lo(bb)), bf_hi(hb) + (keep[4 * q + 3] + r[3] + bf_hi(bb)))};
            st8<OUTAUX>(ov, rOut, rowo, q * 16);
        }
    }
    STAMP(10);
}

extern "C" void kernel_launch(void* const* d_in, const int* in_sizes, int n_in, void* d_out, int out_size, void* d_ws, size_t ws_size, hipStream_t stream) {
    (void)in_sizes; (void)n_in; (void)out_size; (void)ws_size;
    (void)hipFuncSetAttribute((const void*)k_fused, hipFuncAttributeMaxDynamicSharedMemorySize, LDS_BYTES);
    k_fused<<<256, 512, LDS_BYTES, stream>>>((const char*)d_in[0], (const char*)d_in[1], (const char*)d_in[2], (const char*)d_in[4], (const char*)d_in[5],
                                            (const char*)d_in[6], (const char*)d_in[7], (char*)d_out, (char*)d_ws);
}
```

```cpp
#include <hip/hip_runtime.h>
#include <stdint.h>

typedef __bf16 bf16x8 __attribute__((ext_vector_type(8)));
typedef __bf16 bf16x4 __attribute__((ext_vector_type(4)));
typedef __bf16 bf16x2 __attribute__((ext_vector_type(2)));
typedef float f32x16 __attribute__((ext_vector_type(16)));
typedef float f32x4 __attribute__((ext_vector_type(4)));
typedef float f32x2 __attribute__((ext_vector_type(2)));
typedef unsigned u32x4 __attribute__((ext_vector_type(4)));
typedef unsigned u32x2 __attribute__((ext_vector_type(2)));
#define LAS __attribute__((address_space(3)))
#define GAS __attribute__((address_space(1)))
#define DI __device__ __forceinline__
typedef __amdgpu_buffer_rsrc_t rsrc_t;
typedef GAS unsigned gu32;

constexpr int S_ = 2048, D_ = 448, F_ = 1792;
constexpr unsigned FL_X1 = 0, FL_X2 = 4096, FL_E = 8192, FL_X3 = 16384;
constexpr unsigned ST_OFF = 512u << 10, O_OFF = 1u << 20, H_OFF = 4u << 20, G_OFF = 6u << 20;
constexpr unsigned ST_BYTES = 64u * 14 * 256, O_BYTES = 64u * 32 * 1024, H_BYTES = 64u * 28 * 1024, G_BYTES = 64u * 112 * 1024;
constexpr int LDS_BYTES = 163840;
constexpr int SC1 = 16;
constexpr unsigned SPIN_CAP = 1u << 18;
#define POLL_SLEEP 5
#define PD3V 10
#define PD4V 10
#define LEAD1 1
#define ROT1 2
#define XBYC 0
#define TOUCH_AT 1
#define TLBW 1
#define OUTAUX 0
#define AFS 3
#define APS 21
#define BFS 4
#define RSLEEP 1
#define BPRE 0
#define BPD 2
#define PRIO 0
#define LPRIO 0

#define STAMP(i)
#define CSTAMP(i)
#define LSTAMP(i)

DI int rfl(int v) { return __builtin_amdgcn_readfirstlane(v); }
DI rsrc_t mk_rsrc(const void* p, unsigned bytes) { return __builtin_amdgcn_make_buffer_rsrc((void*)p, (short)0, (int)bytes, 0x00020000); }
DI void dma16(unsigned ldsaddr, unsigned voff, rsrc_t srd, unsigned soff) {
    asm volatile("s_mov_b32 m0, %0\n\ts_nop 0\n\tbuffer_load_dwordx4 %1, %2, %3 offen lds" ::"s"(ldsaddr), "v"(voff), "s"(srd), "s"(soff) : "memory");
}
DI void dma16_masked(unsigned ldsaddr, unsigned voff, rsrc_t srd, unsigned soff, unsigned long long off_mask) {
    unsigned long long keep;
    asm volatile("s_mov_b64 %0, exec\n\ts_andn2_b64 exec, exec, %5\n\ts_mov_b32 m0, %1\n\ts_nop 0\n\tbuffer_load_dwordx4 %2, %3, %4 offen lds\n\ts_mov_b64 exec, %0"
                 : "=&s"(keep) : "s"(ldsaddr), "v"(voff), "s"(srd), "s"(soff), "s"(off_mask) : "memory", "scc");
}
template <int N> DI void wait_vm() { asm volatile("s_waitcnt vmcnt(%0)" ::"i"(N) : "memory"); }
DI void bar() { asm volatile("s_waitcnt lgkmcnt(0)" ::: "memory"); __builtin_amdgcn_s_barrier(); asm volatile("" ::: "memory"); }
DI void sb() { __builtin_amdgcn_sched_barrier(0); }
template <int AUX> DI u32x4 ld16(rsrc_t r, unsigned voff, unsigned soff) { return __builtin_amdgcn_raw_buffer_load_b128(r, voff, soff, AUX); }
template <int AUX> DI u32x2 ld8(rsrc_t r, unsigned voff, unsigned soff) { return __builtin_amdgcn_raw_buffer_load_b64(r, voff, soff, AUX); }
template <int AUX> DI void st16(u32x4 v, rsrc_t r, unsigned voff, unsigned soff) { __builtin_amdgcn_raw_buffer_store_b128(v, r, voff + soff, 0, AUX); }
template <int AUX> DI void st8(u32x2 v, rsrc_t r, unsigned voff, unsigned soff) { __builtin_amdgcn_raw_buffer_store_b64(v, r, voff, soff, AUX); }
DI bf16x8 as_frag(u32x4 v) { return __builtin_bit_cast(bf16x8, v); }
DI bf16x8 lds_frag(LAS char* p) { return *(LAS bf16x8*)p; }
DI bf16x8 lds_tr(LAS char* p0, LAS char* p1) {
    bf16x4 a = __builtin_amdgcn_ds_read_tr16_b64_v4bf16((LAS bf16x4*)p0);
    bf16x4 b = __builtin_amdgcn_ds_read_tr16_b64_v4bf16((LAS bf16x4*)p1);
    return __builtin_shufflevector(a, b, 0, 1, 2, 3, 4, 5, 6, 7);
}
DI f32x4 mfma16(bf16x8 a, bf16x8 b, f32x4 c) { return __builtin_amdgcn_mfma_f32_16x16x32_bf16(a, b, c, 0, 0, 0); }
DI f32x16 mfma32(bf16x8 a, bf16x8 b, f32x16 c) { return __builtin_amdgcn_mfma_f32_32x32x16_bf16(a, b, c, 0, 0, 0); }
DI void mma16(f32x4& c, bf16x8 a, bf16x8 b) { c = mfma16(a, b, c); asm volatile("" ::"v"(c)); }
DI void mma32(f32x16& c, bf16x8 a, bf16x8 b) { c = mfma32(a, b, c); asm volatile("" ::"v"(c)); }
DI unsigned pk(float a, float b) { f32x2 v = {a, b}; bf16x2 r = __builtin_convertvector(v, bf16x2); return __builtin_bit_cast(unsigned, r); }
DI float bf_lo(unsigned v) { return __builtin_bit_cast(float, v << 16); }
DI float bf_hi(unsigned v) { return __builtin_bit_cast(float, v & 0xffff0000u); }
DI float dot2(unsigned a, unsigned b, float c) { return __builtin_amdgcn_fdot2_f32_bf16(__builtin_bit_cast(bf16x2, a), __builtin_bit_cast(bf16x2, b), c, false); }
DI unsigned f2u(float v) { return __builtin_bit_cast(unsigned, v); }
DI float u2f(unsigned v) { return __builtin_bit_cast(float, v); }
DI float sum16(float v) { auto a = __builtin_amdgcn_permlane16_swap(f2u(v), f2u(v), false, false); unsigned a0 = a[0], a1 = a[1]; return u2f(a0) + u2f(a1); }
DI float sum32(float v) { auto a = __builtin_amdgcn_permlane32_swap(f2u(v), f2u(v), false, false); unsigned a0 = a[0], a1 = a[1]; return u2f(a0) + u2f(a1); }
DI float max16(float v) { auto a = __builtin_amdgcn_permlane16_swap(f2u(v), f2u(v), false, false); unsigned a0 = a[0], a1 = a[1]; return fmaxf(u2f(a0), u2f(a1)); }
DI float max32(float v) { auto a = __builtin_amdgcn_permlane32_swap(f2u(v), f2u(v), false, false); unsigned a0 = a[0], a1 = a[1]; return fmaxf(u2f(a0), u2f(a1)); }
DI f32x16 zero16() { f32x16 z; for (int i = 0; i < 16; ++i) z[i] = 0.f; return z; }

template <int N, int PD, class LD, class MM> DI void frag_pipe(LD ld, MM mm) {
    bf16x8 buf[N];
#pragma unroll
    for (int i = 0; i < PD; ++i) buf[i] = ld(i);
    sb();
#pragma unroll
    for (int i = 0; i < N; ++i) {
        if (i + PD < N) buf[i + PD] = ld(i + PD);
        mm(i, buf[i]);
        sb();
    }
}

DI void flag_set(char* ws, unsigned off, unsigned tag) { __hip_atomic_store((gu32*)(ws + off), tag, __ATOMIC_RELAXED, __HIP_MEMORY_SCOPE_AGENT); }
DI void flag_wait(char* ws, unsigned off, int n, unsigned tag, int lane) {
    gu32* p = (gu32*)(ws + off) + (lane < n ? lane : 0);
    unsigned v0 = __hip_atomic_load(p, __ATOMIC_RELAXED, __HIP_MEMORY_SCOPE_AGENT);
    for (unsigned spin = 0; spin < SPIN_CAP; ++spin) {
        __builtin_amdgcn_s_sleep(POLL_SLEEP);
        const unsigned v1 = __hip_atomic_load(p, __ATOMIC_RELAXED, __HIP_MEMORY_SCOPE_AGENT);
        if (__all(v0 == tag)) break;
        __builtin_amdgcn_s_sleep(POLL_SLEEP);
        v0 = __hip_atomic_load(p, __ATOMIC_RELAXED, __HIP_MEMORY_SCOPE_AGENT);
        if (__all(v1 == tag)) break;
    }
    asm volatile("" ::: "memory");
}

DI unsigned img_src_lane(int lane) {
    const int i8 = lane >> 3, p8 = lane & 7;
    return (4 * ((p8 >> 2) ^ ((i8 >> 1) & 1)) + (p8 & 3)) * 16;
}
DI unsigned img_rd_lane(int lane) {
    const int G = lane >> 4, nh = G & 1, hh = G >> 1, q = (lane & 15) >> 2, p = lane & 3;
    return (4 * hh + q) * 128 + (q >> 1) * 64 + nh * 32 + 8 * p;
}

constexpr int SL1 = 32768, SL1W = 10240, L_TAB1 = 161792;
constexpr int L_STG = 131072;
DI constexpr int kst(int st) { return st < 7 ? 2 * st : 2 * (st - 7) + 1; }
DI constexpr int wo_pos(int m) { return m < 14 ? m * 4096 : 114688 + (m - 14) * 4096; }
constexpr int L_W1A = 0, L_W1B = 57344, L_RED = 147456;
constexpr int W2SLOT = 16384;

__global__ __launch_bounds__(512, 2) void k_fused(const char* __restrict__ x, const char* __restrict__ wqkv, const char* __restrict__ wo,
                                                  const char* __restrict__ w1, const char* __restrict__ b1, const char* __restrict__ w2,
                                                  const char* __restrict__ b2, char* __restrict__ out, char* __restrict__ ws) {
    extern __shared__ __attribute__((aligned(16))) char smem_[];
    LAS char* smem = (LAS char*)smem_;
    const unsigned lbase = (unsigned)(size_t)smem;
    const int tid = threadIdx.x, lane = tid & 63, w = rfl(tid >> 6);
    const int b = blockIdx.x, slot = b >> 3, T64 = (b & 7) * 4 + (slot >> 3), mem = slot & 7;
    const int PP = T64 >> 1, js = 7 * (T64 & 1) + mem;
    const int t0 = T64 * 64;
    const bool loader = w >= 4;
    const int rot1 = ROT1 == 0 ? 0 : rfl(ROT1 == 1 ? (2 * (T64 & 3) + mem) % 7 : (ROT1 == 2 ? (2 * (T64 & 3)) % 7 : mem % 7));
    STAMP(0);

    if (loader) {
        __builtin_amdgcn_s_setprio(LPRIO);
        const int lw = w - 4, i8 = lane >> 3;
        const rsrc_t rX = mk_rsrc(x, S_ * D_ * 2), rW = mk_rsrc(wqkv, D_ * 3 * D_ * 2);
        unsigned xv[3], xl[3];
#pragma unroll
        for (int m = 0; m < 3; ++m) {
            int rb = lw + 4 * m; rb = rb > 9 ? 9 : rb;
            const int rho = 8 * rb + i8;
            int tok = t0 - 16 + rho; tok = tok < 0 ? 0 : tok;
            xv[m] = tok * 896 + ((lane & 7) ^ ((rho >> 1) & 7)) * 16;
            xl[m] = lbase + rb * 1024;
        }
        const unsigned long long x0off = lw == 0 ? 0xFFull : 0ull;
        unsigned wv[6], wu[6]; unsigned long long woff[6];
#pragma unroll
        for (int m = 0; m < 6; ++m) {
            int u = lw + 4 * m; u = u > 21 ? 21 : u;
            const int c = 64 * u + lane, rs = c / 22, cc = c - rs * 22;
            const int q = rs & 31, bb = q >> 3, ii = q & 7;
            const int krow = (rs & 32) + (bb & 1) * 16 + (bb >> 1) * 4 + (ii & 3) + 8 * (ii >> 2);
            const int sec = cc / 7;
            wv[m] = krow * 2688 + (sec == 2 ? 0 : (sec + 1) * 896) + mem * 112 + (cc - sec * 7) * 16;
            wu[m] = lbase + SL1W + u * 1024;
            woff[m] = __ballot(cc == 21);
        }
        const bool lo2 = lw < 2;
        auto stage1 = [&](int st) {
            const unsigned sl = ((st + 1) & 3) * SL1;
            int kc = st + rot1; kc = kc >= 7 ? kc - 7 : kc;
            if (ROT1 != 0) { kc = rfl(kc); asm volatile("s_nop 4" : "+s"(kc)); }
            dma16_masked(xl[0] + sl, xv[0], rX, kc * 128, x0off);
            dma16(xl[1] + sl, xv[1], rX, kc * 128);
            if (lo2) dma16(xl[2] + sl, xv[2], rX, kc * 128);
#pragma unroll
            for (int m = 0; m < 5; ++m) dma16_masked(wu[m] + sl, wv[m], rW, kc * 64 * 2688, woff[m]);
            if (lo2) dma16_masked(wu[5] + sl, wv[5], rW, kc * 64 * 2688, woff[5]);
        };
        stage1(0); stage1(1); stage1(2);
        for (int st = 0; st < 7; ++st) {
            if (st >= 5) wait_vm<0>(); else if (lo2) wait_vm<9>(); else wait_vm<7>();
            bar();
            if (st <= 3) stage1(st + 3);
        }
        if (mem == 7) {
            bar(); bar(); bar();
            const int pi = (T64 & 3) * 4 + lw;
            unsigned acc = 0;
#pragma unroll 1
            for (int mtx = 0; mtx < 2; ++mtx) {
                const rsrc_t r = mk_rsrc(mtx ? w2 : w1, D_ * F_ * 2);
                unsigned v[13];
#pragma unroll
                for (int m = 0; m < 13; ++m) { const int l = (m * 16 + pi) * 64 + lane; v[m] = __builtin_amdgcn_raw_buffer_load_b32(r, l < 12544 ? l * 128 : 0x80000000u, 0, 0); }
#pragma unroll
                for (int m = 0; m < 13; ++m) acc ^= v[m];
            }
            asm volatile("" ::"v"(acc));
            return;
        }
        const unsigned isl = img_src_lane(lane);
        const rsrc_t rWo = mk_rsrc(wo, D_ * D_ * 2);
        const unsigned vo = (8 * lw + i8) * 896 + mem * 128 + isl;
        auto wo_unit = [&](int m) { dma16(lbase + wo_pos(m) + lw * 1024, vo, rWo, (m) * 28 * 896); };
#pragma unroll
        for (int m = 0; m < 12; ++m) wo_unit(m);
        LSTAMP(0);
        bar();
#pragma unroll
        for (int m = 12; m < 16; ++m) wo_unit(m);
        const rsrc_t rW1 = mk_rsrc(w1, D_ * F_ * 2);
        const unsigned v1o = i8 * 3584 + js * 256 + isl;
        auto w1_so = [&](int m) { return (unsigned)((lw + 4 * m) * 8 * 3584); };
#pragma unroll
        for (int m = 0; m < 14; ++m) dma16(lbase + L_W1B + (lw + 4 * m) * 1024, v1o + 128, rW1, w1_so(m));
        LSTAMP(1);
        bar();
        u32x4 w1r[14];
#pragma unroll
        for (int m = 0; m < 14; ++m) w1r[m] = ld16<0>(rW1, v1o, w1_so(m));
        LSTAMP(2);
        bar();
        LSTAMP(3);
        wait_vm<28>();
        LSTAMP(4);
        bar();
        wait_vm<0>();
        bar();
#pragma unroll
        for (int m = 0; m < 14; ++m) *(LAS u32x4*)(smem + L_W1A + (lw + 4 * m) * 1024 + lane * 16) = w1r[m];
        LSTAMP(5);
        bar();
        const rsrc_t rW2 = mk_rsrc(w2, F_ * D_ * 2);
        const unsigned v2o = (8 * lw + i8) * 896 + mem * 128 + isl;
        auto w2_so = [&](int st, int m) { return (unsigned)(((m >> 1) * 896 + 64 * kst(st) + 32 * (m & 1)) * 896); };
        auto w2_stage = [&](int st) {
#pragma unroll
            for (int m = 0; m < 4; ++m) dma16(lbase + ((st + 7) % 9) * W2SLOT + (m >> 1) * 8192 + ((m & 1) * 4 + lw) * 1024, v2o, rW2, w2_so(st, m));
        };
        w2_stage(0); w2_stage(1);
        u32x4 w5[16];
#pragma unroll
        for (int m = 0; m < 16; ++m) w5[m] = ld16<0>(rW2, v2o, w2_so(5 + (m >> 2), m & 3));
        bar();
        bar();
        w2_stage(2); w2_stage(3); w2_stage(4);
        u32x4 wr[20];
#pragma unroll
        for (int m = 0; m < 20; ++m) wr[m] = ld16<0>(rW2, v2o, w2_so(9 + (m >> 2), m & 3));
        LSTAMP(6);
        bar();
#pragma unroll
        for (int m = 0; m < 16; ++m)
            *(LAS u32x4*)(smem + ((5 + (m >> 2) + 7) % 9) * W2SLOT + ((m & 3) >> 1) * 8192 + ((m & 1) * 4 + lw) * 1024 + lane * 16) = w5[m];
        LSTAMP(7);
        wait_vm<0>();
        LSTAMP(8);
        bar();
        bar();
        auto refill = [&](int m) {
            *(LAS u32x4*)(smem + ((9 + (m >> 2) + 7) % 9) * W2SLOT + ((m & 3) >> 1) * 8192 + ((m & 1) * 4 + lw) * 1024 + lane * 16) = wr[m];
            if (RSLEEP) __builtin_amdgcn_s_sleep(RSLEEP);
        };
#pragma unroll
        for (int m = 0; m < 12; ++m) refill(m);
        bar();
#pragma unroll
        for (int m = 12; m < 20; ++m) refill(m);
        bar();
        bar();
        return;
    }

    __builtin_amdgcn_s_setprio(PRIO);
    unsigned tag = 0;
    {
        const u32x4 ones = {0x3F803F80u, 0x3F803F80u, 0x3F803F80u, 0x3F803F80u};
        if (lane < 8) *(LAS u32x4*)(smem + w * SL1 + lane * 16) = ones;
        const u32x4 e0 = {0x00003F80u, 0u, 0u, 0u};
        *(LAS u32x4*)(smem + w * SL1 + SL1W + lane * 352 + 336) = e0;
        tag = __hip_atomic_load((gu32*)(ws + FL_E + PP * 128), __ATOMIC_RELAXED, __HIP_MEMORY_SCOPE_AGENT) + 1u;
    }
    unsigned tch = 0;
    auto touch = [&]() {
        const char* tp = w == 0 ? wqkv : (w == 1 ? w1 : (w == 2 ? w2 : wo));
        const unsigned per = w == 0 ? 4704u : (w == 3 ? 1568u : 6272u);
        const rsrc_t rT = mk_rsrc(tp, per * 256);
        unsigned o = b * per + lane * 128; o = lane * 128 < per ? o : 0x80000000u;
        tch = __builtin_amdgcn_raw_buffer_load_b32(rT, o, 0, 0);
    };
    if (w == 0) touch();
    if (w == 2) {
        const rsrc_t rWs = mk_rsrc(ws, 16u << 20);
        unsigned o = 0x80000000u;
        if (lane < 2) o = O_OFF + ((2 * T64 + lane) * 32 + 4 * mem) * 1024;
        else if (mem < 7) {
            if (lane < 4) o = H_OFF + ((2 * T64 + lane - 2) * 28 + 4 * mem) * 1024;
            else if (lane < 6) o = ST_OFF + ((2 * T64 + lane - 4) * 14 + 2 * mem) * 256;
            else if (lane < 10) o = G_OFF + ((4 * PP + lane - 6) * 112 + 8 * js) * 1024;
        }
        __builtin_amdgcn_raw_buffer_store_b32(0u, rWs, o, 0, SC1);
    }
    if (w == 3 && mem < 7) {
        const rsrc_t rOutW = mk_rsrc(out, S_ * D_ * 2);
        __builtin_amdgcn_raw_buffer_store_b32(0u, rOutW, lane < 4 ? (t0 + 16 * lane) * 896 + mem * 128 : 0x80000000u, 0, 0);
    }
    unsigned tb = 0;
    if (w == 1) tb = __builtin_amdgcn_raw_buffer_load_b32(mk_rsrc(lane < 32 ? b1 : b1, F_ * 2), 0, 0, 0) ^ __builtin_amdgcn_raw_buffer_load_b32(mk_rsrc(b2, D_ * 2), 0, 0, 0);
    const int t = lane & 15, g = lane >> 4;
#pragma unroll
    for (int m = 0; m < 2; ++m) {
        const int it = tid + 256 * m, blk = it >> 4, tt = it & 15;
        if (it < 480) {
            const int sv_ = ((tt >> 1) & 1) | (((tt >> 3) & 1) << 1) | (((tt >> 2) & 1) << 2);
            const int off = (blk >= 10 && blk < 20) ? tt * 64 + ((7 ^ sv_) * 8) : 768 + tt * 16 + 8;
            *(LAS u32x2*)(smem + L_STG + blk * 1024 + off) = (u32x2){0u, 0u};
        }
    }
    LAS char* edst[3]; bool eok[3];
    {
        const int svt = ((t >> 1) & 1) | (((t >> 3) & 1) << 1) | (((t >> 2) & 1) << 2);
#pragma unroll
        for (int i = 0; i < 3; ++i) {
            const int n0 = (w * 3 + i) * 16 + 4 * g;
            const int arr = (n0 >= 56 ? 1 : 0) + (n0 >= 112 ? 1 : 0), rem = n0 - arr * 56, hh = rem >= 28 ? 1 : 0, dim = rem - 28 * hh;
            const int off = (arr == 1) ? t * 64 + (((dim >> 2) ^ svt) * 8) : (dim >> 3) * 256 + t * 16 + ((dim >> 2) & 1) * 8;
            edst[i] = smem + (n0 < 168 ? L_STG + (arr * 2 + hh) * 5 * 1024 + off : L_STG);
            eok[i] = n0 < 168;
            asm volatile("" : "+v"(edst[i]));
        }
    }
    const int uu = t - 4 * g, r0 = t & 1;
    const bool curA = uu - r0 >= 0, curB = uu - r0 - 2 >= 0;
    const bool noprev = (T64 == 0 && w == 0);
    const bool okA = curA || !noprev, okB = curB || !noprev;
    LAS char* aqk = smem + (L_STG + w * 1024 + lane * 16);
    LAS char* avv[2];
    unsigned odst;
    {
        const int vrow = 4 * g + ((lane & 15) >> 2);
        const int svr = ((vrow >> 1) & 1) | (((vrow >> 3) & 1) << 1) | (((vrow >> 2) & 1) << 2);
        avv[0] = smem + (L_STG + 10240 + w * 1024 + vrow * 64 + (((lane & 3) ^ svr) * 8)); avv[1] = smem + (L_STG + 10240 + w * 1024 + vrow * 64 + (((4 + (lane & 3)) ^ svr) * 8));
        const int T16 = 4 * T64 + w;
        odst = ((T16 >> 1) * 32 + (g >> 1) + 4 * mem) * 1024 + (32 * (g & 1) + 16 * (T16 & 1) + t) * 16;
        asm volatile("" : "+v"(aqk), "+v"(avv[0]), "+v"(avv[1]), "+v"(odst));
    }
    f32x4 acc1[5][3];
#pragma unroll
    for (int a = 0; a < 5; ++a)
#pragma unroll
        for (int i = 0; i < 3; ++i) acc1[a][i] = (f32x4){0.f, 0.f, 0.f, 0.f};
    {
        const int sg = (t >> 1) & 7;
        LAS char* xa[2]; LAS char* wa[2];
        xa[0] = smem + (SL1 + (t >> 3) * 1024 + (t & 7) * 128 + ((g ^ sg) * 16)); xa[1] = smem + (SL1 + (t >> 3) * 1024 + (t & 7) * 128 + (((4 + g) ^ sg) * 16));
        wa[0] = smem + (SL1 + SL1W + (8 * (g >> 1) + 4 * (g & 1) + ((lane & 15) >> 2)) * 352 + 8 * (lane & 3) + w * 96); wa[1] = wa[0] + 11264;
        auto trd = [&](LAS char* p) { return __builtin_amdgcn_ds_read_tr16_b64_v4bf16((LAS bf16x4*)p); };
#pragma unroll
        for (int a = 0; a < 5; ++a)
#pragma unroll
            for (int i = 0; i < 3; ++i) asm volatile("" : "+v"(acc1[a][i]));
        auto gemm1 = [&](auto W3) {
            constexpr bool w3 = decltype(W3)::v;
            bf16x4 wl[2][3], wh[2][3]; bf16x8 xf[2][5];
            auto rd = [&](int ks, int j) {
                if (j == 0) wl[ks][0] = trd(wa[ks]);
                else if (j == 1) wh[ks][0] = trd(wa[ks] + 5632);
                else if (j <= 6) xf[ks][j - 2] = lds_frag(xa[ks] + (j - 2) * 2048);
                else if (w3 && j >= 9) { }
                else if (j & 1) wl[ks][(j - 5) >> 1] = trd(wa[ks] + ((j - 5) >> 1) * 32);
                else wh[ks][(j - 6) >> 1] = trd(wa[ks] + 5632 + ((j - 6) >> 1) * 32);
            };
            bar();
            STAMP(1);
            if (w != 0) touch();
#pragma unroll
            for (int j = 0; j < 11; ++j) rd(0, j);
            xa[0] += SL1; wa[0] += SL1;
            sb();
#pragma unroll 1
            for (int st = 0; st < 7; ++st) {
                const int dd[2] = {((st + 3) & 3) == 0 ? -3 * SL1 : SL1, ((st + 2) & 3) == 0 ? -3 * SL1 : SL1};
#pragma unroll
                for (int ks = 0; ks < 2; ++ks) {
#pragma unroll
                    for (int n = 0; n < 15; ++n) {
                        const int i = n / 5, a = n % 5;
                        if (w3 && i == 2) mma16(acc1[a][2], xf[ks][a], xf[ks][a]);
                        else mma16(acc1[a][i], __builtin_shufflevector(wl[ks][i], wh[ks][i], 0, 1, 2, 3, 4, 5, 6, 7), xf[ks][a]);
                        if (n >= 1 && n < 12) rd(ks ^ 1, n - 1);
                        if (n == 13) { xa[ks ^ 1] += dd[ks ^ 1]; wa[ks ^ 1] += dd[ks ^ 1]; asm volatile("" : "+v"(xa[ks ^ 1]), "+v"(wa[ks ^ 1])); }
                        sb();
                    }
                }
                if (st < 6) { asm volatile("" ::: "memory"); __builtin_amdgcn_s_barrier(); asm volatile("" ::: "memory"); }
            }
        };
        struct BT { enum { v = 1 }; }; struct BF { enum { v = 0 }; };
        if (w == 3) gemm1(BT{}); else gemm1(BF{});
    }
    STAMP(2);
    LAS float* tst = (LAS float*)(smem + L_TAB1);
    if (w == 3) {
        const int r = t & 3;
        float d[5];
#pragma unroll
        for (int a = 0; a < 5; ++a) {
            const float da = (r & 1) ? acc1[a][2][1] : acc1[a][2][0], db = (r & 1) ? acc1[a][2][3] : acc1[a][2][2];
            d[a] = (r & 2) ? db : da;
        }
        LAS float* tp = tst + 2 * t;
        if (g == 2) {
#pragma unroll
            for (int a = 0; a < 5; ++a) tp[32 * a] = acc1[a][1][0];
        }
        if (g == (t >> 2)) {
#pragma unroll
            for (int a = 0; a < 5; ++a) tp[32 * a + 1] = d[a];
        }
    }
    asm volatile("" ::"v"(tch));
    asm volatile("" ::"v"(tb));
    wait_vm<0>();
    bar();
    STAMP(11);
    {
        f32x2 nmu[5], rs[5];
#pragma unroll
        for (int a = 0; a < 5; ++a) {
            const f32x2 s = *(LAS f32x2*)(tst + (16 * a + t) * 2);
            const float mu = s[0] * (1.0f / D_);
            const float r = __builtin_amdgcn_rsqf(fmaxf(s[1] * (1.0f / D_) - mu * mu, 0.0f) + 1e-5f);
            nmu[a] = (f32x2){-mu, -mu}; rs[a] = (f32x2){r, r};
        }
#pragma unroll
        for (int i = 0; i < 3; ++i) {
            f32x4 cs;
#pragma unroll
            for (int r = 0; r < 4; ++r) { const float c = acc1[0][i][r]; cs[r] = u2f(__builtin_amdgcn_update_dpp(0, (int)f2u(c), 0x150, 0xf, 0xf, true)); }
            u32x2 pv[5];
#pragma unroll
            for (int a = 0; a < 5; ++a) {
                const f32x2 lo = {acc1[a][i][0], acc1[a][i][1]}, hi = {acc1[a][i][2], acc1[a][i][3]};
                const f32x2 v01 = (nmu[a] * (f32x2){cs[0], cs[1]} + lo) * rs[a], v23 = (nmu[a] * (f32x2){cs[2], cs[3]} + hi) * rs[a];
                pv[a] = (u32x2){pk(v01[0], v01[1]), pk(v23[0], v23[1])};
            }
            if (eok[i]) {
#pragma unroll
                for (int a = 0; a < 5; ++a) *(LAS u32x2*)(edst[i] + a * 1024) = pv[a];
            }
        }
    }
    STAMP(12);
    bar();
    STAMP(13);
    const rsrc_t rO = mk_rsrc(ws + O_OFF, O_BYTES);
    {
        const float scl = 0.18898223650461362f * 1.4426950408889634f;
        const f32x4 z = {0.f, 0.f, 0.f, 0.f};
        bf16x8 qf[2], kp[2], kc[2], vf[2][2];
#pragma unroll
        for (int hh = 0; hh < 2; ++hh) {
            qf[hh] = lds_frag(aqk + 20480 + 1024 + hh * 5120); kp[hh] = lds_frag(aqk + hh * 5120); kc[hh] = lds_frag(aqk + 1024 + hh * 5120);
        }
#pragma unroll
        for (int hh = 0; hh < 2; ++hh)
#pragma unroll
            for (int dt = 0; dt < 2; ++dt) vf[hh][dt] = lds_tr(avv[dt] + hh * 5120, avv[dt] + hh * 5120 + 1024);
#pragma unroll
        for (int hh = 0; hh < 2; ++hh) {
            const f32x4 sp = mfma16(kp[hh], qf[hh], z), sc = mfma16(kc[hh], qf[hh], z);
            const float ac = r0 ? sc[1] : sc[0], ap = r0 ? sp[1] : sp[0], bc = r0 ? sc[3] : sc[2], bp = r0 ? sp[3] : sp[2];
            const float za = okA ? (curA ? ac : ap) * scl : -3.0e38f, zb = okB ? (curB ? bc : bp) * scl : -3.0e38f;
            const float mx = max32(max16(fmaxf(za, zb)));
            const float ea = __builtin_amdgcn_exp2f(za - mx), eb = __builtin_amdgcn_exp2f(zb - mx);
            const float inv = __builtin_amdgcn_rcpf(sum32(sum16(ea + eb)));
            const unsigned pw = pk(ea * inv, eb * inv);
            const unsigned wa_ = (pw & 0xffffu) << (16 * r0), wb_ = (pw >> 16) << (16 * r0);
            const u32x4 pu = {curA ? 0u : wa_, curB ? 0u : wb_, curA ? wa_ : 0u, curB ? wb_ : 0u};
            const f32x4 o0 = mfma16(vf[hh][0], as_frag(pu), z), o1 = mfma16(vf[hh][1], as_frag(pu), z);
            const u32x4 ou = {pk(o0[0], o0[1]), pk(o0[2], o0[3]), pk(o1[0], o1[1]), pk(o1[2], o1[3])};
            st16<SC1>(ou, rO, odst, hh * 2048);
        }
    }
    STAMP(3);
    wait_vm<0>();
    STAMP(14);
    bar();
    STAMP(15);
    tag = rfl(tag);
    if (w == 0 && lane == 0) flag_set(ws, FL_X1 + T64 * 128 + mem * 4, tag);
    if (mem == 7) return;
    const int th = w & 1, kh = w >> 1, t32 = lane & 31, hl = lane >> 5;
    const int T32 = 2 * T64 + th;
    const unsigned colb = (64 * mem + 32 * kh) * 2;
    const unsigned rowo = (T32 * 32 + t32) * 896 + colb + hl * 8;
    u32x2 xr[4];
    {
        const rsrc_t rX = mk_rsrc(x, S_ * D_ * 2);
#pragma unroll
        for (int q = 0; q < 4; ++q) xr[q] = ld8<0>(rX, rowo, q * 16);
    }
    if (w == 0) flag_wait(ws, FL_X1 + T64 * 128, 8, tag, lane);
    bar();
    STAMP(4);
    const unsigned ird = img_rd_lane(lane);
    const rsrc_t rH = mk_rsrc(ws + H_OFF, H_BYTES), rSt = mk_rsrc(ws + ST_OFF, ST_BYTES);
    {
        constexpr int PD2 = 8;
        u32x4 of[16];
        const unsigned oso = (T32 * 32 + 16 * kh) * 1024;
        auto o_so = [&](int s) { return (unsigned)(s * 1024); };
#pragma unroll
        for (int s = 0; s < PD2; ++s) of[s] = ld16<SC1>(rO, lane * 16, oso + o_so(s));
        sb();
        f32x16 a2[2] = {zero16(), zero16()};
        const unsigned wo_hi = kh ? 114688 - 14 * 4096 : 0;
        frag_pipe<32, 4>([&](int n) { const int s = n >> 1, hp_ = s >> 1; LAS char* a = smem + (ird ^ ((n & 1) << 6)) + kh * 8 * 4096 + hp_ * 4096 + (hp_ >= 6 ? wo_hi : 0) + (s & 1) * 1024; return lds_tr(a, a + 2048); },
                         [&](int n, bf16x8 f) { const int s = n >> 1; if ((n & 1) == 0 && s + PD2 < 16) of[s + PD2] = ld16<SC1>(rO, lane * 16, oso + o_so(s + PD2)); mma32(a2[n & 1], f, as_frag(of[s])); });
        const f32x16 keep = kh ? a2[1] : a2[0], give = kh ? a2[0] : a2[1];
        LAS char* red = smem + L_RED;
#pragma unroll
        for (int q = 0; q < 4; ++q) *(LAS f32x4*)(red + (w * 4 + q) * 1024 + lane * 16) = (f32x4){give[4 * q], give[4 * q + 1], give[4 * q + 2], give[4 * q + 3]};
        bar();
        float hv[16];
        float s1 = 0.f, s2 = 0.f;
        unsigned hp[8];
#pragma unroll
        for (int q = 0; q < 4; ++q) {
            const f32x4 r = *(LAS f32x4*)(red + ((w ^ 2) * 4 + q) * 1024 + lane * 16);
            const unsigned xa = xr[q].x, xb = xr[q].y;
            hv[4 * q] = bf_lo(xa) + (keep[4 * q] + r[0]); hv[4 * q + 1] = bf_hi(xa) + (keep[4 * q + 1] + r[1]);
            hv[4 * q + 2] = bf_lo(xb) + (keep[4 * q + 2] + r[2]); hv[4 * q + 3] = bf_hi(xb) + (keep[4 * q + 3] + r[3]);
            hp[2 * q] = pk(hv[4 * q], hv[4 * q + 1]); hp[2 * q + 1] = pk(hv[4 * q + 2], hv[4 * q + 3]);
        }
        st16<SC1>((u32x4){hp[0], hp[1], hp[2], hp[3]}, rH, lane * 16, (T32 * 28 + 4 * mem + 2 * kh) * 1024);
        st16<SC1>((u32x4){hp[4], hp[5], hp[6], hp[7]}, rH, lane * 16, (T32 * 28 + 4 * mem + 2 * kh + 1) * 1024);
#pragma unroll
        for (int q = 0; q < 8; ++q) { const float lo = bf_lo(hp[q]), hi = bf_hi(hp[q]); s1 += lo + hi; s2 = fmaf(lo, lo, fmaf(hi, hi, s2)); }
        s1 = sum32(s1); s2 = sum32(s2);
        if (hl == 0) st8<SC1>((u32x2){f2u(s1), f2u(s2)}, rSt, t32 * 8, (T32 * 14 + 2 * mem + kh) * 256);
    }
    STAMP(5);
    unsigned short b1v = 0;
    if (tid < 128) { const rsrc_t rB1 = mk_rsrc(b1, F_ * 2); b1v = __builtin_amdgcn_raw_buffer_load_b16(rB1, (js * 128 + tid) * 2, 0, 0); }
    wait_vm<0>();
    STAMP(16);
    bar();
    STAMP(17);
    if (w == 0 && lane == 0) flag_set(ws, FL_X2 + PP * 128 + js * 4, tag);
    LAS float* tab = (LAS float*)(smem + L_RED);
    if (tid < 128) tab[128 + tid] = u2f((unsigned)b1v << 16);
    {
        const u32x4 onesu = {0x3F803F80u, 0x3F803F80u, 0x3F803F80u, 0x3F803F80u};
        f32x16 cs = zero16();
        LAS char* a0 = smem + (w >> 1) * 57344 + (ird ^ ((w & 1) << 6));
        frag_pipe<28, 4>([&](int s) { return lds_tr(a0 + s * 2048, a0 + s * 2048 + 1024); }, [&](int s, bf16x8 f) { mma32(cs, f, as_frag(onesu)); });
        if (t32 == 0) {
#pragma unroll
            for (int q = 0; q < 4; ++q) *(LAS f32x4*)(tab + 32 * w + 8 * q + 4 * hl) = (f32x4){cs[4 * q], cs[4 * q + 1], cs[4 * q + 2], cs[4 * q + 3]};
        }
    }
    if (w == 0) {
        flag_wait(ws, FL_X2 + PP * 128, 14, tag, lane);
        if (js == 0 && lane == 0) flag_set(ws, FL_E + PP * 128, tag);
    }
    bar();
    STAMP(6);
    const rsrc_t rG = mk_rsrc(ws + G_OFF, G_BYTES);
    const unsigned fx4 = FL_X3 + T32 * 128 + 7 * kh * 4;
    gu32* pA = (gu32*)(ws + fx4) + (lane < 7 ? lane : 0);
    unsigned fA = 0;
    const unsigned fx3 = FL_X3 + (4 * PP + w) * 128 + js * 4, fx3b = fx3 + 56;
    u32x4 gf[56];
    const unsigned gso = (T32 * 112 + 56 * kh) * 1024;
    auto g_so = [&](int s) { return (unsigned)((4 * kst(s >> 2) + (s & 3)) * 1024); };
    {
        const int TP = 4 * PP + w;
        u32x2 sv[7];
#pragma unroll
        for (int q = 0; q < 7; ++q) sv[q] = ld8<SC1>(rSt, t32 * 8 + hl * 256, (TP * 14 + 2 * q) * 256);
        constexpr int PD3 = PD3V;
        u32x4 hf[28];
        const unsigned hso = TP * 28 * 1024;
        auto h_so = [&](int s) { return (unsigned)(s * 1024); };
#pragma unroll
        for (int s = 0; s < PD3; ++s) hf[s] = ld16<SC1>(rH, lane * 16, hso + h_so(s));
        sb();
        f32x16 a3[4] = {zero16(), zero16(), zero16(), zero16()};
        LAS char* wA = smem + L_W1A; LAS char* wB = smem + L_W1B;
        frag_pipe<56, 4>([&](int n) { LAS char* a = wA + (ird ^ ((n & 1) << 6)) + (n >> 1) * 2048; return lds_tr(a, a + 1024); },
                         [&](int n, bf16x8 f) { const int s = n >> 1; if ((n & 1) == 0 && s + PD3 < 28) hf[s + PD3] = ld16<SC1>(rH, lane * 16, hso + h_so(s + PD3)); mma32(a3[n & 1], f, as_frag(hf[s])); });
        float s1 = 0.f, s2 = 0.f;
#pragma unroll
        for (int q = 0; q < 7; ++q) { const unsigned sa = sv[q].x, sb_ = sv[q].y; s1 += u2f(sa); s2 += u2f(sb_); }
        s1 = sum32(s1); s2 = sum32(s2);
        const float mu = s1 * (1.0f / D_);
        const float rstd = __builtin_amdgcn_rsqf(fmaxf(s2 * (1.0f / D_) - mu * mu, 0.0f) + 1e-5f);
        const f32x2 rs2 = {rstd, rstd}, nm2 = {-mu * rstd, -mu * rstd};
        const f32x2 c02 = {-2.0f * 0.7978845608028654f * 1.4426950408889634f, -2.0f * 0.7978845608028654f * 1.4426950408889634f};
        const f32x2 c12 = c02 * 0.044715f, one2 = {1.0f, 1.0f};
        STAMP(18);
        bar();
        f32x4 tcs_[16], tbb_[16];
        unsigned gq[8];
        LAS float* tabl = tab + 4 * hl;
        asm volatile("" : "+v"(tabl));
        auto gelu_ld = [&](int m) { const int i = m >> 2, q = m & 3; tcs_[m] = *(LAS f32x4*)(tabl + 32 * i + 8 * q); tbb_[m] = *(LAS f32x4*)(tabl + 128 + 32 * i + 8 * q); };
        auto gelu_do = [&](int m) {
            const int i = m >> 2, q = m & 3;
            const f32x4 cs = tcs_[m], bb = tbb_[m];
            f32x2 y0 = (f32x2){a3[i][4 * q], a3[i][4 * q + 1]} * rs2 + (nm2 * (f32x2){cs[0], cs[1]} + (f32x2){bb[0], bb[1]});
            f32x2 y1 = (f32x2){a3[i][4 * q + 2], a3[i][4 * q + 3]} * rs2 + (nm2 * (f32x2){cs[2], cs[3]} + (f32x2){bb[2], bb[3]});
            const f32x2 z0 = y0 * ((y0 * y0) * c12 + c02), z1 = y1 * ((y1 * y1) * c12 + c02);
            const f32x2 d0 = (f32x2){__builtin_amdgcn_exp2f(z0[0]), __builtin_amdgcn_exp2f(z0[1])} + one2;
            const f32x2 d1 = (f32x2){__builtin_amdgcn_exp2f(z1[0]), __builtin_amdgcn_exp2f(z1[1])} + one2;
            y0 = y0 * (f32x2){__builtin_amdgcn_rcpf(d0[0]), __builtin_amdgcn_rcpf(d0[1])};
            y1 = y1 * (f32x2){__builtin_amdgcn_rcpf(d1[0]), __builtin_amdgcn_rcpf(d1[1])};
            gq[2 * q] = pk(y0[0], y0[1]); gq[2 * q + 1] = pk(y1[0], y1[1]);
            if (q & 1) st16<SC1>((u32x4){gq[2 * q - 2], gq[2 * q - 1], gq[2 * q], gq[2 * q + 1]}, rG, lane * 16, (TP * 112 + 8 * js + 2 * i + (q >> 1)) * 1024);
        };
        frag_pipe<28, 4>([&](int s) { LAS char* a = wB + ird + s * 2048; return lds_tr(a, a + 1024); },
                         [&](int s, bf16x8 f) { if (s % 3 == 0 && s / 3 < 8) gelu_ld(s / 3); mma32(a3[2], f, as_frag(hf[s])); if (s % 3 == 2 && s / 3 < 8) gelu_do(s / 3); });
        frag_pipe<28, 4>([&](int s) { LAS char* a = wB + (ird ^ 64) + s * 2048; return lds_tr(a, a + 1024); },
                         [&](int s, bf16x8 f) {
                             if (s % 6 == 1 && s / 6 < 4) gelu_ld(8 + s / 6);
                             if (s >= 24) gelu_ld(12 + s - 24);
                             mma32(a3[3], f, as_frag(hf[s]));
                             if (s == AFS) { wait_vm<0>(); if (lane == 0) flag_set(ws, fx3, tag); }
                             if (s == APS) fA = __hip_atomic_load(pA, __ATOMIC_RELAXED, __HIP_MEMORY_SCOPE_AGENT);
                             if (s % 6 == 4 && s / 6 < 4) gelu_do(8 + s / 6); });
        bar();
        if (!__all(fA == tag)) flag_wait(ws, fx4, 7, tag, lane);
        asm volatile("" ::: "memory");
#pragma unroll
        for (int s = 0; s < 8; ++s) gf[s] = ld16<SC1>(rG, lane * 16, gso + g_so(s));
        sb();
#pragma unroll
        for (int q = 0; q < 4; ++q) gelu_do(12 + q);
        sb();
#pragma unroll
        for (int s = 8; s < PD4V; ++s) gf[s] = ld16<SC1>(rG, lane * 16, gso + g_so(s));
        STAMP(7);
    }
    STAMP(19);
    u32x4 hres[2]; u32x2 b2v[4];
    STAMP(8);
    {
        constexpr int PD = PD4V;
        gu32* pB = (gu32*)(ws + fx4 + 56) + (lane < 7 ? lane : 0);
        unsigned fB = 0;
        bar();
        sb();
        f32x16 a4[2] = {zero16(), zero16()};
        LAS char* wbp = smem + kh * 8192;
        auto run = [&](auto S0, auto NS) {
            constexpr int s0 = decltype(S0)::v, ns = decltype(NS)::v;
            frag_pipe<2 * ns, 4>(
                [&](int n) { const int s = s0 + (n >> 1); LAS char* a = wbp + (((s >> 2) + 7) % 9) * W2SLOT + (s & 3) * 2048 + (ird ^ ((n & 1) << 6)); return lds_tr(a, a + 1024); },
                [&](int n, bf16x8 f) {
                    const int s = s0 + (n >> 1);
                    if ((n & 1) == 0 && s + PD == 28 - BPD) fB = __hip_atomic_load(pB, __ATOMIC_RELAXED, __HIP_MEMORY_SCOPE_AGENT);
                    if ((n & 1) == 0 && s + PD == 28) { if (!__all(fB == tag)) flag_wait(ws, fx4 + 56, 7, tag, lane); asm volatile("" ::: "memory"); }
                    if ((n & 1) == 0 && s + PD < 56) gf[s + PD] = ld16<SC1>(rG, lane * 16, gso + g_so(s + PD));
                    if (!BPRE && (n & 1) == 0 && s == BFS) { wait_vm<PD - 8 + BFS + 1>(); if (lane == 0) flag_set(ws, fx3b, tag); }
                    mma32(a4[n & 1], f, as_frag(gf[s])); });
        };
        struct I0 { enum { v = 0 }; }; struct I20 { enum { v = 20 }; }; struct I16 { enum { v = 16 }; }; struct I36 { enum { v = 36 }; }; struct I12 { enum { v = 12 }; }; struct I48 { enum { v = 48 }; }; struct I8 { enum { v = 8 }; };
        run(I0{}, I20{});
        {
            const rsrc_t rB2 = mk_rsrc(b2, D_ * 2);
            hres[0] = ld16<SC1>(rH, lane * 16, (T32 * 28 + 4 * mem + 2 * kh) * 1024);
            hres[1] = ld16<SC1>(rH, lane * 16, (T32 * 28 + 4 * mem + 2 * kh + 1) * 1024);
#pragma unroll
            for (int q = 0; q < 4; ++q) b2v[q] = ld8<0>(rB2, colb + hl * 8, q * 16);
        }
        bar();
        run(I20{}, I16{});
        bar();
        run(I36{}, I12{});
        bar();
        run(I48{}, I8{});
        STAMP(9);
        const f32x16 keep = kh ? a4[1] : a4[0], give = kh ? a4[0] : a4[1];
        LAS char* red = smem + L_RED;
#pragma unroll
        for (int q = 0; q < 4; ++q) *(LAS f32x4*)(red + (w * 4 + q) * 1024 + lane * 16) = (f32x4){give[4 * q], give[4 * q + 1], give[4 * q + 2], give[4 * q + 3]};
        bar();
        const rsrc_t rOut = mk_rsrc(out, S_ * D_ * 2);
#pragma unroll
        for (int q = 0; q < 4; ++q) {
            const f32x4 r = *(LAS f32x4*)(red + ((w ^ 2) * 4 + q) * 1024 + lane * 16);
            const unsigned ha = hres[q >> 1][2 * (q & 1)], hb = hres[q >> 1][2 * (q & 1) + 1], ba = b2v[q].x, bb = b2v[q].y;
            const u32x2 ov = {pk(bf_lo(ha) + (keep[4 * q] + r[0] + bf_lo(ba)), bf_hi(ha) + (keep[4 * q + 1] + r[1] + bf_hi(ba))),
                              pk(bf_lo(hb) + (keep[4 * q + 2] + r[2] + bf_lo(bb)), bf_hi(hb) + (keep[4 * q + 3] + r[3] + bf_hi(bb)))};
            st8<OUTAUX>(ov, rOut, rowo, q * 16);
        }
    }
    STAMP(10);
}

extern "C" void kernel_launch(void* const* d_in, const int* in_sizes, int n_in, void* d_out, int out_size, void* d_ws, size_t ws_size, hipStream_t stream) {
    (void)in_sizes; (void)n_in; (void)out_size; (void)ws_size;
    (void)hipFuncSetAttribute((const void*)k_fused, hipFuncAttributeMaxDynamicSharedMemorySize, LDS_BYTES);
    k_fused<<<256, 512, LDS_BYTES, stream>>>((const char*)d_in[0], (const char*)d_in[1], (const char*)d_in[2], (const char*)d_in[4], (const char*)d_in[5],
                                            (const char*)d_in[6], (const char*)d_in[7], (char*)d_out, (char*)d_ws);
}
```

```cpp
#define SKEWK 0
#include <hip/hip_runtime.h>
#include <stdint.h>

typedef __bf16 bf16x8 __attribute__((ext_vector_type(8)));
typedef __bf16 bf16x4 __attribute__((ext_vector_type(4)));
typedef __bf16 bf16x2 __attribute__((ext_vector_type(2)));
typedef float f32x16 __attribute__((ext_vector_type(16)));
typedef float f32x4 __attribute__((ext_vector_type(4)));
typedef float f32x2 __attribute__((ext_vector_type(2)));
typedef unsigned u32x4 __attribute__((ext_vector_type(4)));
typedef unsigned u32x2 __attribute__((ext_vector_type(2)));
#define LAS __attribute__((address_space(3)))
#define GAS __attribute__((address_space(1)))
#define DI __device__ __forceinline__
typedef __amdgpu_buffer_rsrc_t rsrc_t;
typedef GAS unsigned gu32;

constexpr int S_ = 2048, D_ = 448, F_ = 1792;
constexpr unsigned FL_X1 = 0, FL_X2 = 4096, FL_E = 8192, FL_X3 = 16384;
constexpr unsigned ST_OFF = 512u << 10, O_OFF = 1u << 20, H_OFF = 4u << 20, G_OFF = 6u << 20;
constexpr unsigned ST_BYTES = 64u * 14 * 256, O_BYTES = 64u * 32 * 1024, H_BYTES = 64u * 28 * 1024, G_BYTES = 64u * 112 * 1024;
constexpr int LDS_BYTES = 163840;
constexpr int SC1 = 16;
constexpr unsigned SPIN_CAP = 1u << 18;
#define POLL_SLEEP 5
#define PD3V 10
#define PD4V 10
#define LEAD1 1
#define ROT1 2
#define XBYC 0
#define TOUCH_AT 1
#define TLBW 1
#define OUTAUX 0
#define AFS 3
#define APS 21
#define BFS 4
#define RSLEEP 1
#define BPRE 0
#define BPD 2
#define PRIO 0
#define LPRIO 0

#define STAMP(i)
#define CSTAMP(i)
#define LSTAMP(i)

DI int rfl(int v) { return __builtin_amdgcn_readfirstlane(v); }
DI rsrc_t mk_rsrc(const void* p, unsigned bytes) { return __builtin_amdgcn_make_buffer_rsrc((void*)p, (short)0, (int)bytes, 0x00020000); }
DI void dma16(unsigned ldsaddr, unsigned voff, rsrc_t srd, unsigned soff) {
    asm volatile("s_mov_b32 m0, %0\n\ts_nop 0\n\tbuffer_load_dwordx4 %1, %2, %3 offen lds" ::"s"(ldsaddr), "v"(voff), "s"(srd), "s"(soff) : "memory");
}
DI void dma16_masked(unsigned ldsaddr, unsigned voff, rsrc_t srd, unsigned soff, unsigned long long off_mask) {
    unsigned long long keep;
    asm volatile("s_mov_b64 %0, exec\n\ts_andn2_b64 exec, exec, %5\n\ts_mov_b32 m0, %1\n\ts_nop 0\n\tbuffer_load_dwordx4 %2, %3, %4 offen lds\n\ts_mov_b64 exec, %0"
                 : "=&s"(keep) : "s"(ldsaddr), "v"(voff), "s"(srd), "s"(soff), "s"(off_mask) : "memory", "scc");
}
template <int N> DI void wait_vm() { asm volatile("s_waitcnt vmcnt(%0)" ::"i"(N) : "memory"); }
DI void bar() { asm volatile("s_waitcnt lgkmcnt(0)" ::: "memory"); __builtin_amdgcn_s_barrier(); asm volatile("" ::: "memory"); }
DI void sb() { __builtin_amdgcn_sched_barrier(0); }
template <int AUX> DI u32x4 ld16(rsrc_t r, unsigned voff, unsigned soff) { return __builtin_amdgcn_raw_buffer_load_b128(r, voff, soff, AUX); }
template <int AUX> DI u32x2 ld8(rsrc_t r, unsigned voff, unsigned soff) { return __builtin_amdgcn_raw_buffer_load_b64(r, voff, soff, AUX); }
template <int AUX> DI void st16(u32x4 v, rsrc_t r, unsigned voff, unsigned soff) { __builtin_amdgcn_raw_buffer_store_b128(v, r, voff + soff, 0, AUX); }
template <int AUX> DI void st8(u32x2 v, rsrc_t r, unsigned voff, unsigned soff) { __builtin_amdgcn_raw_buffer_store_b64(v, r, voff, soff, AUX); }
DI bf16x8 as_frag(u32x4 v) { return __builtin_bit_cast(bf16x8, v); }
DI bf16x8 lds_frag(LAS char* p) { return *(LAS bf16x8*)p; }
DI bf16x8 lds_tr(LAS char* p0, LAS char* p1) {
    bf16x4 a = __builtin_amdgcn_ds_read_tr16_b64_v4bf16((LAS bf16x4*)p0);
    bf16x4 b = __builtin_amdgcn_ds_read_tr16_b64_v4bf16((LAS bf16x4*)p1);
    return __builtin_shufflevector(a, b, 0, 1, 2, 3, 4, 5, 6, 7);
}
DI f32x4 mfma16(bf16x8 a, bf16x8 b, f32x4 c) { return __builtin_amdgcn_mfma_f32_16x16x32_bf16(a, b, c, 0, 0, 0); }
DI f32x16 mfma32(bf16x8 a, bf16x8 b, f32x16 c) { return __builtin_amdgcn_mfma_f32_32x32x16_bf16(a, b, c, 0, 0, 0); }
DI void mma16(f32x4& c, bf16x8 a, bf16x8 b) { c = mfma16(a, b, c); asm volatile("" ::"v"(c)); }
DI void mma32(f32x16& c, bf16x8 a, bf16x8 b) { c = mfma32(a, b, c); asm volatile("" ::"v"(c)); }
DI unsigned pk(float a, float b) { f32x2 v = {a, b}; bf16x2 r = __builtin_convertvector(v, bf16x2); return __builtin_bit_cast(unsigned, r); }
DI float bf_lo(unsigned v) { return __builtin_bit_cast(float, v << 16); }
DI float bf_hi(unsigned v) { return __builtin_bit_cast(float, v & 0xffff0000u); }
DI float dot2(unsigned a, unsigned b, float c) { return __builtin_amdgcn_fdot2_f32_bf16(__builtin_bit_cast(bf16x2, a), __builtin_bit_cast(bf16x2, b), c, false); }
DI unsigned f2u(float v) { return __builtin_bit_cast(unsigned, v); }
DI float u2f(unsigned v) { return __builtin_bit_cast(float, v); }
DI float sum16(float v) { auto a = __builtin_amdgcn_permlane16_swap(f2u(v), f2u(v), false, false); unsigned a0 = a[0], a1 = a[1]; return u2f(a0) + u2f(a1); }
DI float sum32(float v) { auto a = __builtin_amdgcn_permlane32_swap(f2u(v), f2u(v), false, false); unsigned a0 = a[0], a1 = a[1]; return u2f(a0) + u2f(a1); }
DI float max16(float v) { auto a = __builtin_amdgcn_permlane16_swap(f2u(v), f2u(v), false, false); unsigned a0 = a[0], a1 = a[1]; return fmaxf(u2f(a0), u2f(a1)); }
DI float max32(float v) { auto a = __builtin_amdgcn_permlane32_swap(f2u(v), f2u(v), false, false); unsigned a0 = a[0], a1 = a[1]; return fmaxf(u2f(a0), u2f(a1)); }
DI f32x16 zero16() { f32x16 z; for (int i = 0; i < 16; ++i) z[i] = 0.f; return z; }

template <int N, int PD, class LD, class MM> DI void frag_pipe(LD ld, MM mm) {
    bf16x8 buf[N];
#pragma unroll
    for (int i = 0; i < PD; ++i) buf[i] = ld(i);
    sb();
#pragma unroll
    for (int i = 0; i < N; ++i) {
        if (i + PD < N) buf[i + PD] = ld(i + PD);
        mm(i, buf[i]);
        sb();
    }
}

DI void flag_set(char* ws, unsigned off, unsigned tag) { __hip_atomic_store((gu32*)(ws + off), tag, __ATOMIC_RELAXED, __HIP_MEMORY_SCOPE_AGENT); }
DI void flag_wait(char* ws, unsigned off, int n, unsigned tag, int lane) {
    gu32* p = (gu32*)(ws + off) + (lane < n ? lane : 0);
    unsigned v0 = __hip_atomic_load(p, __ATOMIC_RELAXED, __HIP_MEMORY_SCOPE_AGENT);
    for (unsigned spin = 0; spin < SPIN_CAP; ++spin) {
        __builtin_amdgcn_s_sleep(POLL_SLEEP);
        const unsigned v1 = __hip_atomic_load(p, __ATOMIC_RELAXED, __HIP_MEMORY_SCOPE_AGENT);
        if (__all(v0 == tag)) break;
        __builtin_amdgcn_s_sleep(POLL_SLEEP);
        v0 = __hip_atomic_load(p, __ATOMIC_RELAXED, __HIP_MEMORY_SCOPE_AGENT);
        if (__all(v1 == tag)) break;
    }
    asm volatile("" ::: "memory");
}

DI unsigned img_src_lane(int lane) {
    const int i8 = lane >> 3, p8 = lane & 7;
    return (4 * ((p8 >> 2) ^ ((i8 >> 1) & 1)) + (p8 & 3)) * 16;
}
DI unsigned img_rd_lane(int lane) {
    const int G = lane >> 4, nh = G & 1, hh = G >> 1, q = (lane & 15) >> 2, p = lane & 3;
    return (4 * hh + q) * 128 + (q >> 1) * 64 + nh * 32 + 8 * p;
}

constexpr int SL1 = 32768, SL1W = 10240, L_TAB1 = 161792;
constexpr int L_STG = 131072;
DI constexpr int kst(int st) { return st < 7 ? 2 * st : 2 * (st - 7) + 1; }
DI constexpr int wo_pos(int m) { return m < 14 ? m * 4096 : 114688 + (m - 14) * 4096; }
constexpr int L_W1A = 0, L_W1B = 57344, L_RED = 147456;
constexpr int W2SLOT = 16384;

__global__ __launch_bounds__(512, 2) void k_fused(const char* __restrict__ x, const char* __restrict__ wqkv, const char* __restrict__ wo,
                                                  const char* __restrict__ w1, const char* __restrict__ b1, const char* __restrict__ w2,
                                                  const char* __restrict__ b2, char* __restrict__ out, char* __restrict__ ws) {
    extern __shared__ __attribute__((aligned(16))) char smem_[];
    LAS char* smem = (LAS char*)smem_;
    const unsigned lbase = (unsigned)(size_t)smem;
    const int tid = threadIdx.x, lane = tid & 63, w = rfl(tid >> 6);
    const int b = blockIdx.x, slot = b >> 3, T64 = (b & 7) * 4 + (slot >> 3), mem = slot & 7;
    const int PP = T64 >> 1, js = 7 * (T64 & 1) + mem;
    const int t0 = T64 * 64;
    const bool loader = w >= 4;
    const int rot1 = ROT1 == 0 ? 0 : rfl(ROT1 == 1 ? (2 * (T64 & 3) + mem) % 7 : (ROT1 == 2 ? (2 * (T64 & 3)) % 7 : mem % 7));
    if (threadIdx.x == 0) __hip_atomic_store((GAS unsigned long long*)(ws + 307200) + blockIdx.x, (unsigned long long)__builtin_amdgcn_s_memrealtime(), __ATOMIC_RELAXED, __HIP_MEMORY_SCOPE_AGENT);
    STAMP(0);

    if (loader) {
        __builtin_amdgcn_s_setprio(LPRIO);
        const int lw = w - 4, i8 = lane >> 3;
        const rsrc_t rX = mk_rsrc(x, S_ * D_ * 2), rW = mk_rsrc(wqkv, D_ * 3 * D_ * 2);
        unsigned xv[3], xl[3];
#pragma unroll
        for (int m = 0; m < 3; ++m) {
            int rb = lw + 4 * m; rb = rb > 9 ? 9 : rb;
            const int rho = 8 * rb + i8;
            int tok = t0 - 16 + rho; tok = tok < 0 ? 0 : tok;
            xv[m] = tok * 896 + ((lane & 7) ^ ((rho >> 1) & 7)) * 16;
            xl[m] = lbase + rb * 1024;
        }
        const unsigned long long x0off = lw == 0 ? 0xFFull : 0ull;
        unsigned wv[6], wu[6]; unsigned long long woff[6];
#pragma unroll
        for (int m = 0; m < 6; ++m) {
            int u = lw + 4 * m; u = u > 21 ? 21 : u;
            const int c = 64 * u + lane, rs = c / 22, cc = c - rs * 22;
            const int q = rs & 31, bb = q >> 3, ii = q & 7;
            const int krow = (rs & 32) + (bb & 1) * 16 + (bb >> 1) * 4 + (ii & 3) + 8 * (ii >> 2);
            const int sec = cc / 7;
            wv[m] = krow * 2688 + (sec == 2 ? 0 : (sec + 1) * 896) + mem * 112 + (cc - sec * 7) * 16;
            wu[m] = lbase + SL1W + u * 1024;
            woff[m] = __ballot(cc == 21);
        }
        const bool lo2 = lw < 2;
        auto stage1 = [&](int st) {
            const unsigned sl = ((st + 1) & 3) * SL1;
            int kc = st + rot1; kc = kc >= 7 ? kc - 7 : kc;
            if (ROT1 != 0) { kc = rfl(kc); asm volatile("s_nop 4" : "+s"(kc)); }
            dma16_masked(xl[0] + sl, xv[0], rX, kc * 128, x0off);
            dma16(xl[1] + sl, xv[1], rX, kc * 128);
            if (lo2) dma16(xl[2] + sl, xv[2], rX, kc * 128);
#pragma unroll
            for (int m = 0; m < 5; ++m) dma16_masked(wu[m] + sl, wv[m], rW, kc * 64 * 2688, woff[m]);
            if (lo2) dma16_masked(wu[5] + sl, wv[5], rW, kc * 64 * 2688, woff[5]);
        };
        stage1(0); stage1(1); stage1(2);
        for (int st = 0; st < 7; ++st) {
            if (st >= 5) wait_vm<0>(); else if (lo2) wait_vm<9>(); else wait_vm<7>();
            bar();
            if (st <= 3) stage1(st + 3);
        }
        if (mem == 7) {
            bar(); bar(); bar();
            const int pi = (T64 & 3) * 4 + lw;
            unsigned acc = 0;
#pragma unroll 1
            for (int mtx = 0; mtx < 2; ++mtx) {
                const rsrc_t r = mk_rsrc(mtx ? w2 : w1, D_ * F_ * 2);
                unsigned v[13];
#pragma unroll
                for (int m = 0; m < 13; ++m) { const int l = (m * 16 + pi) * 64 + lane; v[m] = __builtin_amdgcn_raw_buffer_load_b32(r, l < 12544 ? l * 128 : 0x80000000u, 0, 0); }
#pragma unroll
                for (int m = 0; m < 13; ++m) acc ^= v[m];
            }
            asm volatile("" ::"v"(acc));
            return;
        }
        const unsigned isl = img_src_lane(lane);
        const rsrc_t rWo = mk_rsrc(wo, D_ * D_ * 2);
        const unsigned vo = (8 * lw + i8) * 896 + mem * 128 + isl;
        auto wo_unit = [&](int m) { dma16(lbase + wo_pos(m) + lw * 1024, vo, rWo, (m) * 28 * 896); };
#pragma unroll
        for (int m = 0; m < 12; ++m) wo_unit(m);
        LSTAMP(0);
        bar();
#pragma unroll
        for (int m = 12; m < 16; ++m) wo_unit(m);
        const rsrc_t rW1 = mk_rsrc(w1, D_ * F_ * 2);
        const unsigned v1o = i8 * 3584 + js * 256 + isl;
        auto w1_so = [&](int m) { return (unsigned)((lw + 4 * m) * 8 * 3584); };
#pragma unroll
        for (int m = 0; m < 14; ++m) dma16(lbase + L_W1B + (lw + 4 * m) * 1024, v1o + 128, rW1, w1_so(m));
        LSTAMP(1);
        bar();
        u32x4 w1r[14];
#pragma unroll
        for (int m = 0; m < 14; ++m) w1r[m] = ld16<0>(rW1, v1o, w1_so(m));
        LSTAMP(2);
        bar();
        LSTAMP(3);
        wait_vm<28>();
        LSTAMP(4);
        bar();
        wait_vm<0>();
        bar();
#pragma unroll
        for (int m = 0; m < 14; ++m) *(LAS u32x4*)(smem + L_W1A + (lw + 4 * m) * 1024 + lane * 16) = w1r[m];
        LSTAMP(5);
        bar();
        const rsrc_t rW2 = mk_rsrc(w2, F_ * D_ * 2);
        const unsigned v2o = (8 * lw + i8) * 896 + mem * 128 + isl;
        auto w2_so = [&](int st, int m) { return (unsigned)(((m >> 1) * 896 + 64 * kst(st) + 32 * (m & 1)) * 896); };
        auto w2_stage = [&](int st) {
#pragma unroll
            for (int m = 0; m < 4; ++m) dma16(lbase + ((st + 7) % 9) * W2SLOT + (m >> 1) * 8192 + ((m & 1) * 4 + lw) * 1024, v2o, rW2, w2_so(st, m));
        };
        w2_stage(0); w2_stage(1);
        u32x4 w5[16];
#pragma unroll
        for (int m = 0; m < 16; ++m) w5[m] = ld16<0>(rW2, v2o, w2_so(5 + (m >> 2), m & 3));
        bar();
        bar();
        w2_stage(2); w2_stage(3); w2_stage(4);
        u32x4 wr[20];
#pragma unroll
        for (int m = 0; m < 20; ++m) wr[m] = ld16<0>(rW2, v2o, w2_so(9 + (m >> 2), m & 3));
        LSTAMP(6);
        bar();
#pragma unroll
        for (int m = 0; m < 16; ++m)
            *(LAS u32x4*)(smem + ((5 + (m >> 2) + 7) % 9) * W2SLOT + ((m & 3) >> 1) * 8192 + ((m & 1) * 4 + lw) * 1024 + lane * 16) = w5[m];
        LSTAMP(7);
        wait_vm<0>();
        LSTAMP(8);
        bar();
        bar();
        auto refill = [&](int m) {
            *(LAS u32x4*)(smem + ((9 + (m >> 2) + 7) % 9) * W2SLOT + ((m & 3) >> 1) * 8192 + ((m & 1) * 4 + lw) * 1024 + lane * 16) = wr[m];
            if (RSLEEP) __builtin_amdgcn_s_sleep(RSLEEP);
        };
#pragma unroll
        for (int m = 0; m < 12; ++m) refill(m);
        bar();
#pragma unroll
        for (int m = 12; m < 20; ++m) refill(m);
        bar();
        bar();
        return;
    }

    __builtin_amdgcn_s_setprio(PRIO);
    unsigned tag = 0;
    {
        const u32x4 ones = {0x3F803F80u, 0x3F803F80u, 0x3F803F80u, 0x3F803F80u};
        if (lane < 8) *(LAS u32x4*)(smem + w * SL1 + lane * 16) = ones;
        const u32x4 e0 = {0x00003F80u, 0u, 0u, 0u};
        *(LAS u32x4*)(smem + w * SL1 + SL1W + lane * 352 + 336) = e0;
        tag = __hip_atomic_load((gu32*)(ws + FL_E + PP * 128), __ATOMIC_RELAXED, __HIP_MEMORY_SCOPE_AGENT) + 1u;
    }
    unsigned tch = 0;
    auto touch = [&]() {
        const char* tp = w == 0 ? wqkv : (w == 1 ? w1 : (w == 2 ? w2 : wo));
        const unsigned per = w == 0 ? 4704u : (w == 3 ? 1568u : 6272u);
        const rsrc_t rT = mk_rsrc(tp, per * 256);
        unsigned o = b * per + lane * 128; o = lane * 128 < per ? o : 0x80000000u;
        tch = __builtin_amdgcn_raw_buffer_load_b32(rT, o, 0, 0);
    };
    if (w == 0) touch();
    if (w == 2) {
        const rsrc_t rWs = mk_rsrc(ws, 16u << 20);
        unsigned o = 0x80000000u;
        if (lane < 2) o = O_OFF + ((2 * T64 + lane) * 32 + 4 * mem) * 1024;
        else if (mem < 7) {
            if (lane < 4) o = H_OFF + ((2 * T64 + lane - 2) * 28 + 4 * mem) * 1024;
            else if (lane < 6) o = ST_OFF + ((2 * T64 + lane - 4) * 14 + 2 * mem) * 256;
            else if (lane < 10) o = G_OFF + ((4 * PP + lane - 6) * 112 + 8 * js) * 1024;
        }
        __builtin_amdgcn_raw_buffer_store_b32(0u, rWs, o, 0, SC1);
    }
    if (w == 3 && mem < 7) {
        const rsrc_t rOutW = mk_rsrc(out, S_ * D_ * 2);
        __builtin_amdgcn_raw_buffer_store_b32(0u, rOutW, lane < 4 ? (t0 + 16 * lane) * 896 + mem * 128 : 0x80000000u, 0, 0);
    }
    unsigned tb = 0;
    if (w == 1) tb = __builtin_amdgcn_raw_buffer_load_b32(mk_rsrc(lane < 32 ? b1 : b1, F_ * 2), 0, 0, 0) ^ __builtin_amdgcn_raw_buffer_load_b32(mk_rsrc(b2, D_ * 2), 0, 0, 0);
    const int t = lane & 15, g = lane >> 4;
#pragma unroll
    for (int m = 0; m < 2; ++m) {
        const int it = tid + 256 * m, blk = it >> 4, tt = it & 15;
        if (it < 480) {
            const int sv_ = ((tt >> 1) & 1) | (((tt >> 3) & 1) << 1) | (((tt >> 2) & 1) << 2);
            const int off = (blk >= 10 && blk < 20) ? tt * 64 + ((7 ^ sv_) * 8) : 768 + tt * 16 + 8;
            *(LAS u32x2*)(smem + L_STG + blk * 1024 + off) = (u32x2){0u, 0u};
        }
    }
    LAS char* edst[3]; bool eok[3];
    {
        const int svt = ((t >> 1) & 1) | (((t >> 3) & 1) << 1) | (((t >> 2) & 1) << 2);
#pragma unroll
        for (int i = 0; i < 3; ++i) {
            const int n0 = (w * 3 + i) * 16 + 4 * g;
            const int arr = (n0 >= 56 ? 1 : 0) + (n0 >= 112 ? 1 : 0), rem = n0 - arr * 56, hh = rem >= 28 ? 1 : 0, dim = rem - 28 * hh;
            const int off = (arr == 1) ? t * 64 + (((dim >> 2) ^ svt) * 8) : (dim >> 3) * 256 + t * 16 + ((dim >> 2) & 1) * 8;
            edst[i] = smem + (n0 < 168 ? L_STG + (arr * 2 + hh) * 5 * 1024 + off : L_STG);
            eok[i] = n0 < 168;
            asm volatile("" : "+v"(edst[i]));
        }
    }
    const int uu = t - 4 * g, r0 = t & 1;
    const bool curA = uu - r0 >= 0, curB = uu - r0 - 2 >= 0;
    const bool noprev = (T64 == 0 && w == 0);
    const bool okA = curA || !noprev, okB = curB || !noprev;
    LAS char* aqk = smem + (L_STG + w * 1024 + lane * 16);
    LAS char* avv[2];
    unsigned odst;
    {
        const int vrow = 4 * g + ((lane & 15) >> 2);
        const int svr = ((vrow >> 1) & 1) | (((vrow >> 3) & 1) << 1) | (((vrow >> 2) & 1) << 2);
        avv[0] = smem + (L_STG + 10240 + w * 1024 + vrow * 64 + (((lane & 3) ^ svr) * 8)); avv[1] = smem + (L_STG + 10240 + w * 1024 + vrow * 64 + (((4 + (lane & 3)) ^ svr) * 8));
        const int T16 = 4 * T64 + w;
        odst = ((T16 >> 1) * 32 + (g >> 1) + 4 * mem) * 1024 + (32 * (g & 1) + 16 * (T16 & 1) + t) * 16;
        asm volatile("" : "+v"(aqk), "+v"(avv[0]), "+v"(avv[1]), "+v"(odst));
    }
    f32x4 acc1[5][3];
#pragma unroll
    for (int a = 0; a < 5; ++a)
#pragma unroll
        for (int i = 0; i < 3; ++i) acc1[a][i] = (f32x4){0.f, 0.f, 0.f, 0.f};
    {
        const int sg = (t >> 1) & 7;
        LAS char* xa[2]; LAS char* wa[2];
        xa[0] = smem + (SL1 + (t >> 3) * 1024 + (t & 7) * 128 + ((g ^ sg) * 16)); xa[1] = smem + (SL1 + (t >> 3) * 1024 + (t & 7) * 128 + (((4 + g) ^ sg) * 16));
        wa[0] = smem + (SL1 + SL1W + (8 * (g >> 1) + 4 * (g & 1) + ((lane & 15) >> 2)) * 352 + 8 * (lane & 3) + w * 96); wa[1] = wa[0] + 11264;
        auto trd = [&](LAS char* p) { return __builtin_amdgcn_ds_read_tr16_b64_v4bf16((LAS bf16x4*)p); };
#pragma unroll
        for (int a = 0; a < 5; ++a)
#pragma unroll
            for (int i = 0; i < 3; ++i) asm volatile("" : "+v"(acc1[a][i]));
        auto gemm1 = [&](auto W3) {
            constexpr bool w3 = decltype(W3)::v;
            bf16x4 wl[2][3], wh[2][3]; bf16x8 xf[2][5];
            auto rd = [&](int ks, int j) {
                if (j == 0) wl[ks][0] = trd(wa[ks]);
                else if (j == 1) wh[ks][0] = trd(wa[ks] + 5632);
                else if (j <= 6) xf[ks][j - 2] = lds_frag(xa[ks] + (j - 2) * 2048);
                else if (w3 && j >= 9) { }
                else if (j & 1) wl[ks][(j - 5) >> 1] = trd(wa[ks] + ((j - 5) >> 1) * 32);
                else wh[ks][(j - 6) >> 1] = trd(wa[ks] + 5632 + ((j - 6) >> 1) * 32);
            };
            bar();
            STAMP(1);
            if (w != 0) touch();
#pragma unroll
            for (int j = 0; j < 11; ++j) rd(0, j);
            xa[0] += SL1; wa[0] += SL1;
            sb();
#pragma unroll 1
            for (int st = 0; st < 7; ++st) {
                const int dd[2] = {((st + 3) & 3) == 0 ? -3 * SL1 : SL1, ((st + 2) & 3) == 0 ? -3 * SL1 : SL1};
#pragma unroll
                for (int ks = 0; ks < 2; ++ks) {
#pragma unroll
                    for (int n = 0; n < 15; ++n) {
                        const int i = n / 5, a = n % 5;
                        if (w3 && i == 2) mma16(acc1[a][2], xf[ks][a], xf[ks][a]);
                        else mma16(acc1[a][i], __builtin_shufflevector(wl[ks][i], wh[ks][i], 0, 1, 2, 3, 4, 5, 6, 7), xf[ks][a]);
                        if (n >= 1 && n < 12) rd(ks ^ 1, n - 1);
                        if (n == 13) { xa[ks ^ 1] += dd[ks ^ 1]; wa[ks ^ 1] += dd[ks ^ 1]; asm volatile("" : "+v"(xa[ks ^ 1]), "+v"(wa[ks ^ 1])); }
                        sb();
                    }
                }
                if (st < 6) { asm volatile("" ::: "memory"); __builtin_amdgcn_s_barrier(); asm volatile("" ::: "memory"); }
            }
        };
        struct BT { enum { v = 1 }; }; struct BF { enum { v = 0 }; };
        if (w == 3) gemm1(BT{}); else gemm1(BF{});
    }
    STAMP(2);
    LAS float* tst = (LAS float*)(smem + L_TAB1);
    if (w == 3) {
        const int r = t & 3;
        float d[5];
#pragma unroll
        for (int a = 0; a < 5; ++a) {
            const float da = (r & 1) ? acc1[a][2][1] : acc1[a][2][0], db = (r & 1) ? acc1[a][2][3] : acc1[a][2][2];
            d[a] = (r & 2) ? db : da;
        }
        LAS float* tp = tst + 2 * t;
        if (g == 2) {
#pragma unroll
            for (int a = 0; a < 5; ++a) tp[32 * a] = acc1[a][1][0];
        }
        if (g == (t >> 2)) {
#pragma unroll
            for (int a = 0; a < 5; ++a) tp[32 * a + 1] = d[a];
        }
    }
    asm volatile("" ::"v"(tch));
    asm volatile("" ::"v"(tb));
    wait_vm<0>();
    bar();
    STAMP(11);
    {
        f32x2 nmu[5], rs[5];
#pragma unroll
        for (int a = 0; a < 5; ++a) {
            const f32x2 s = *(LAS f32x2*)(tst + (16 * a + t) * 2);
            const float mu = s[0] * (1.0f / D_);
            const float r = __builtin_amdgcn_rsqf(fmaxf(s[1] * (1.0f / D_) - mu * mu, 0.0f) + 1e-5f);
            nmu[a] = (f32x2){-mu, -mu}; rs[a] = (f32x2){r, r};
        }
#pragma unroll
        for (int i = 0; i < 3; ++i) {
            f32x4 cs;
#pragma unroll
            for (int r = 0; r < 4; ++r) { const float c = acc1[0][i][r]; cs[r] = u2f(__builtin_amdgcn_update_dpp(0, (int)f2u(c), 0x150, 0xf, 0xf, true)); }
            u32x2 pv[5];
#pragma unroll
            for (int a = 0; a < 5; ++a) {
                const f32x2 lo = {acc1[a][i][0], acc1[a][i][1]}, hi = {acc1[a][i][2], acc1[a][i][3]};
                const f32x2 v01 = (nmu[a] * (f32x2){cs[0], cs[1]} + lo) * rs[a], v23 = (nmu[a] * (f32x2){cs[2], cs[3]} + hi) * rs[a];
                pv[a] = (u32x2){pk(v01[0], v01[1]), pk(v23[0], v23[1])};
            }
            if (eok[i]) {
#pragma unroll
                for (int a = 0; a < 5; ++a) *(LAS u32x2*)(edst[i] + a * 1024) = pv[a];
            }
        }
    }
    STAMP(12);
    bar();
    STAMP(13);
    const rsrc_t rO = mk_rsrc(ws + O_OFF, O_BYTES);
    {
        const float scl = 0.18898223650461362f * 1.4426950408889634f;
        const f32x4 z = {0.f, 0.f, 0.f, 0.f};
        bf16x8 qf[2], kp[2], kc[2], vf[2][2];
#pragma unroll
        for (int hh = 0; hh < 2; ++hh) {
            qf[hh] = lds_frag(aqk + 20480 + 1024 + hh * 5120); kp[hh] = lds_frag(aqk + hh * 5120); kc[hh] = lds_frag(aqk + 1024 + hh * 5120);
        }
#pragma unroll
        for (int hh = 0; hh < 2; ++hh)
#pragma unroll
            for (int dt = 0; dt < 2; ++dt) vf[hh][dt] = lds_tr(avv[dt] + hh * 5120, avv[dt] + hh * 5120 + 1024);
#pragma unroll
        for (int hh = 0; hh < 2; ++hh) {
            const f32x4 sp = mfma16(kp[hh], qf[hh], z), sc = mfma16(kc[hh], qf[hh], z);
            const float ac = r0 ? sc[1] : sc[0], ap = r0 ? sp[1] : sp[0], bc = r0 ? sc[3] : sc[2], bp = r0 ? sp[3] : sp[2];
            const float za = okA ? (curA ? ac : ap) * scl : -3.0e38f, zb = okB ? (curB ? bc : bp) * scl : -3.0e38f;
            const float mx = max32(max16(fmaxf(za, zb)));
            const float ea = __builtin_amdgcn_exp2f(za - mx), eb = __builtin_amdgcn_exp2f(zb - mx);
            const float inv = __builtin_amdgcn_rcpf(sum32(sum16(ea + eb)));
            const unsigned pw = pk(ea * inv, eb * inv);
            const unsigned wa_ = (pw & 0xffffu) << (16 * r0), wb_ = (pw >> 16) << (16 * r0);
            const u32x4 pu = {curA ? 0u : wa_, curB ? 0u : wb_, curA ? wa_ : 0u, curB ? wb_ : 0u};
            const f32x4 o0 = mfma16(vf[hh][0], as_frag(pu), z), o1 = mfma16(vf[hh][1], as_frag(pu), z);
            const u32x4 ou = {pk(o0[0], o0[1]), pk(o0[2], o0[3]), pk(o1[0], o1[1]), pk(o1[2], o1[3])};
            st16<SC1>(ou, rO, odst, hh * 2048);
        }
    }
    STAMP(3);
    wait_vm<0>();
    STAMP(14);
    bar();
    STAMP(15);
    tag = rfl(tag);
    if (w == 0 && lane == 0) flag_set(ws, FL_X1 + T64 * 128 + mem * 4, tag);
    if (mem == 7) return;
    const int th = w & 1, kh = w >> 1, t32 = lane & 31, hl = lane >> 5;
    const int T32 = 2 * T64 + th;
    const unsigned colb = (64 * mem + 32 * kh) * 2;
    const unsigned rowo = (T32 * 32 + t32) * 896 + colb + hl * 8;
    u32x2 xr[4];
    {
        const rsrc_t rX = mk_rsrc(x, S_ * D_ * 2);
#pragma unroll
        for (int q = 0; q < 4; ++q) xr[q] = ld8<0>(rX, rowo, q * 16);
    }
    if (w == 0) flag_wait(ws, FL_X1 + T64 * 128, 8, tag, lane);
    bar();
    STAMP(4);
    const unsigned ird = img_rd_lane(lane);
    const rsrc_t rH = mk_rsrc(ws + H_OFF, H_BYTES), rSt = mk_rsrc(ws + ST_OFF, ST_BYTES);
    {
        constexpr int PD2 = 8;
        u32x4 of[16];
        const unsigned oso = (T32 * 32 + 16 * kh) * 1024;
        auto o_so = [&](int s) { return (unsigned)(s * 1024); };
#pragma unroll
        for (int s = 0; s < PD2; ++s) of[s] = ld16<SC1>(rO, lane * 16, oso + o_so(s));
        sb();
        f32x16 a2[2] = {zero16(), zero16()};
        const unsigned wo_hi = kh ? 114688 - 14 * 4096 : 0;
        frag_pipe<32, 4>([&](int n) { const int s = n >> 1, hp_ = s >> 1; LAS char* a = smem + (ird ^ ((n & 1) << 6)) + kh * 8 * 4096 + hp_ * 4096 + (hp_ >= 6 ? wo_hi : 0) + (s & 1) * 1024; return lds_tr(a, a + 2048); },
                         [&](int n, bf16x8 f) { const int s = n >> 1; if ((n & 1) == 0 && s + PD2 < 16) of[s + PD2] = ld16<SC1>(rO, lane * 16, oso + o_so(s + PD2)); mma32(a2[n & 1], f, as_frag(of[s])); });
        const f32x16 keep = kh ? a2[1] : a2[0], give = kh ? a2[0] : a2[1];
        LAS char* red = smem + L_RED;
#pragma unroll
        for (int q = 0; q < 4; ++q) *(LAS f32x4*)(red + (w * 4 + q) * 1024 + lane * 16) = (f32x4){give[4 * q], give[4 * q + 1], give[4 * q + 2], give[4 * q + 3]};
        bar();
        float hv[16];
        float s1 = 0.f, s2 = 0.f;
        unsigned hp[8];
#pragma unroll
        for (int q = 0; q < 4; ++q) {
            const f32x4 r = *(LAS f32x4*)(red + ((w ^ 2) * 4 + q) * 1024 + lane * 16);
            const unsigned xa = xr[q].x, xb = xr[q].y;
            hv[4 * q] = bf_lo(xa) + (keep[4 * q] + r[0]); hv[4 * q + 1] = bf_hi(xa) + (keep[4 * q + 1] + r[1]);
            hv[4 * q + 2] = bf_lo(xb) + (keep[4 * q + 2] + r[2]); hv[4 * q + 3] = bf_hi(xb) + (keep[4 * q + 3] + r[3]);
            hp[2 * q] = pk(hv[4 * q], hv[4 * q + 1]); hp[2 * q + 1] = pk(hv[4 * q + 2], hv[4 * q + 3]);
        }
        st16<SC1>((u32x4){hp[0], hp[1], hp[2], hp[3]}, rH, lane * 16, (T32 * 28 + 4 * mem + 2 * kh) * 1024);
        st16<SC1>((u32x4){hp[4], hp[5], hp[6], hp[7]}, rH, lane * 16, (T32 * 28 + 4 * mem + 2 * kh + 1) * 1024);
#pragma unroll
        for (int q = 0; q < 8; ++q) { const float lo = bf_lo(hp[q]), hi = bf_hi(hp[q]); s1 += lo + hi; s2 = fmaf(lo, lo, fmaf(hi, hi, s2)); }
        s1 = sum32(s1); s2 = sum32(s2);
        if (hl == 0) st8<SC1>((u32x2){f2u(s1), f2u(s2)}, rSt, t32 * 8, (T32 * 14 + 2 * mem + kh) * 256);
    }
    STAMP(5);
    unsigned short b1v = 0;
    if (tid < 128) { const rsrc_t rB1 = mk_rsrc(b1, F_ * 2); b1v = __builtin_amdgcn_raw_buffer_load_b16(rB1, (js * 128 + tid) * 2, 0, 0); }
    wait_vm<0>();
    STAMP(16);
    bar();
    STAMP(17);
    if (w == 0 && lane == 0) flag_set(ws, FL_X2 + PP * 128 + js * 4, tag);
    LAS float* tab = (LAS float*)(smem + L_RED);
    if (tid < 128) tab[128 + tid] = u2f((unsigned)b1v << 16);
    {
        const u32x4 onesu = {0x3F803F80u, 0x3F803F80u, 0x3F803F80u, 0x3F803F80u};
        f32x16 cs = zero16();
        LAS char* a0 = smem + (w >> 1) * 57344 + (ird ^ ((w & 1) << 6));
        frag_pipe<28, 4>([&](int s) { return lds_tr(a0 + s * 2048, a0 + s * 2048 + 1024); }, [&](int s, bf16x8 f) { mma32(cs, f, as_frag(onesu)); });
        if (t32 == 0) {
#pragma unroll
            for (int q = 0; q < 4; ++q) *(LAS f32x4*)(tab + 32 * w + 8 * q + 4 * hl) = (f32x4){cs[4 * q], cs[4 * q + 1], cs[4 * q + 2], cs[4 * q + 3]};
        }
    }
    if (w == 0) {
        flag_wait(ws, FL_X2 + PP * 128, 14, tag, lane);
        if (js == 0 && lane == 0) flag_set(ws, FL_E + PP * 128, tag);
    }
    bar();
    STAMP(6);
    const rsrc_t rG = mk_rsrc(ws + G_OFF, G_BYTES);
    const unsigned fx4 = FL_X3 + T32 * 128 + 7 * kh * 4;
    gu32* pA = (gu32*)(ws + fx4) + (lane < 7 ? lane : 0);
    unsigned fA = 0;
    const unsigned fx3 = FL_X3 + (4 * PP + w) * 128 + js * 4, fx3b = fx3 + 56;
    u32x4 gf[56];
    const unsigned gso = (T32 * 112 + 56 * kh) * 1024;
    auto g_so = [&](int s) { return (unsigned)((4 * kst(s >> 2) + (s & 3)) * 1024); };
    {
        const int TP = 4 * PP + w;
        u32x2 sv[7];
#pragma unroll
        for (int q = 0; q < 7; ++q) sv[q] = ld8<SC1>(rSt, t32 * 8 + hl * 256, (TP * 14 + 2 * q) * 256);
        constexpr int PD3 = PD3V;
        u32x4 hf[28];
        const unsigned hso = TP * 28 * 1024;
        auto h_so = [&](int s) { return (unsigned)(s * 1024); };
#pragma unroll
        for (int s = 0; s < PD3; ++s) hf[s] = ld16<SC1>(rH, lane * 16, hso + h_so(s));
        sb();
        f32x16 a3[4] = {zero16(), zero16(), zero16(), zero16()};
        LAS char* wA = smem + L_W1A; LAS char* wB = smem + L_W1B;
        frag_pipe<56, 4>([&](int n) { LAS char* a = wA + (ird ^ ((n & 1) << 6)) + (n >> 1) * 2048; return lds_tr(a, a + 1024); },
                         [&](int n, bf16x8 f) { const int s = n >> 1; if ((n & 1) == 0 && s + PD3 < 28) hf[s + PD3] = ld16<SC1>(rH, lane * 16, hso + h_so(s + PD3)); mma32(a3[n & 1], f, as_frag(hf[s])); });
        float s1 = 0.f, s2 = 0.f;
#pragma unroll
        for (int q = 0; q < 7; ++q) { const unsigned sa = sv[q].x, sb_ = sv[q].y; s1 += u2f(sa); s2 += u2f(sb_); }
        s1 = sum32(s1); s2 = sum32(s2);
        const float mu = s1 * (1.0f / D_);
        const float rstd = __builtin_amdgcn_rsqf(fmaxf(s2 * (1.0f / D_) - mu * mu, 0.0f) + 1e-5f);
        const f32x2 rs2 = {rstd, rstd}, nm2 = {-mu * rstd, -mu * rstd};
        const f32x2 c02 = {-2.0f * 0.7978845608028654f * 1.4426950408889634f, -2.0f * 0.7978845608028654f * 1.4426950408889634f};
        const f32x2 c12 = c02 * 0.044715f, one2 = {1.0f, 1.0f};
        STAMP(18);
        bar();
        f32x4 tcs_[16], tbb_[16];
        unsigned gq[8];
        LAS float* tabl = tab + 4 * hl;
        asm volatile("" : "+v"(tabl));
        auto gelu_ld = [&](int m) { const int i = m >> 2, q = m & 3; tcs_[m] = *(LAS f32x4*)(tabl + 32 * i + 8 * q); tbb_[m] = *(LAS f32x4*)(tabl + 128 + 32 * i + 8 * q); };
        auto gelu_do = [&](int m) {
            const int i = m >> 2, q = m & 3;
            const f32x4 cs = tcs_[m], bb = tbb_[m];
            f32x2 y0 = (f32x2){a3[i][4 * q], a3[i][4 * q + 1]} * rs2 + (nm2 * (f32x2){cs[0], cs[1]} + (f32x2){bb[0], bb[1]});
            f32x2 y1 = (f32x2){a3[i][4 * q + 2], a3[i][4 * q + 3]} * rs2 + (nm2 * (f32x2){cs[2], cs[3]} + (f32x2){bb[2], bb[3]});
            const f32x2 z0 = y0 * ((y0 * y0) * c12 + c02), z1 = y1 * ((y1 * y1) * c12 + c02);
            const f32x2 d0 = (f32x2){__builtin_amdgcn_exp2f(z0[0]), __builtin_amdgcn_exp2f(z0[1])} + one2;
            const f32x2 d1 = (f32x2){__builtin_amdgcn_exp2f(z1[0]), __builtin_amdgcn_exp2f(z1[1])} + one2;
            y0 = y0 * (f32x2){__builtin_amdgcn_rcpf(d0[0]), __builtin_amdgcn_rcpf(d0[1])};
            y1 = y1 * (f32x2){__builtin_amdgcn_rcpf(d1[0]), __builtin_amdgcn_rcpf(d1[1])};
            gq[2 * q] = pk(y0[0], y0[1]); gq[2 * q + 1] = pk(y1[0], y1[1]);
            if (q & 1) st16<SC1>((u32x4){gq[2 * q - 2], gq[2 * q - 1], gq[2 * q], gq[2 * q + 1]}, rG, lane * 16, (TP * 112 + 8 * js + 2 * i + (q >> 1)) * 1024);
        };
        frag_pipe<28, 4>([&](int s) { LAS char* a = wB + ird + s * 2048; return lds_tr(a, a + 1024); },
                         [&](int s, bf16x8 f) { if (s % 3 == 0 && s / 3 < 8) gelu_ld(s / 3); mma32(a3[2], f, as_frag(hf[s])); if (s % 3 == 2 && s / 3 < 8) gelu_do(s / 3); });
        frag_pipe<28, 4>([&](int s) { LAS char* a = wB + (ird ^ 64) + s * 2048; return lds_tr(a, a + 1024); },
                         [&](int s, bf16x8 f) {
                             if (s % 6 == 1 && s / 6 < 4) gelu_ld(8 + s / 6);
                             if (s >= 24) gelu_ld(12 + s - 24);
                             mma32(a3[3], f, as_frag(hf[s]));
                             if (s == AFS) { wait_vm<0>(); if (lane == 0) flag_set(ws, fx3, tag); }
                             if (s == APS) fA = __hip_atomic_load(pA, __ATOMIC_RELAXED, __HIP_MEMORY_SCOPE_AGENT);
                             if (s % 6 == 4 && s / 6 < 4) gelu_do(8 + s / 6); });
        bar();
        if (!__all(fA == tag)) flag_wait(ws, fx4, 7, tag, lane);
        asm volatile("" ::: "memory");
#pragma unroll
        for (int s = 0; s < 8; ++s) gf[s] = ld16<SC1>(rG, lane * 16, gso + g_so(s));
        sb();
#pragma unroll
        for (int q = 0; q < 4; ++q) gelu_do(12 + q);
        sb();
#pragma unroll
        for (int s = 8; s < PD4V; ++s) gf[s] = ld16<SC1>(rG, lane * 16, gso + g_so(s));
        STAMP(7);
    }
    STAMP(19);
    u32x4 hres[2]; u32x2 b2v[4];
    STAMP(8);
    {
        constexpr int PD = PD4V;
        gu32* pB = (gu32*)(ws + fx4 + 56) + (lane < 7 ? lane : 0);
        unsigned fB = 0;
        bar();
        sb();
        f32x16 a4[2] = {zero16(), zero16()};
        LAS char* wbp = smem + kh * 8192;
        auto run = [&](auto S0, auto NS) {
            constexpr int s0 = decltype(S0)::v, ns = decltype(NS)::v;
            frag_pipe<2 * ns, 4>(
                [&](int n) { const int s = s0 + (n >> 1); LAS char* a = wbp + (((s >> 2) + 7) % 9) * W2SLOT + (s & 3) * 2048 + (ird ^ ((n & 1) << 6)); return lds_tr(a, a + 1024); },
                [&](int n, bf16x8 f) {
                    const int s = s0 + (n >> 1);
                    if ((n & 1) == 0 && s + PD == 28 - BPD) fB = __hip_atomic_load(pB, __ATOMIC_RELAXED, __HIP_MEMORY_SCOPE_AGENT);
                    if ((n & 1) == 0 && s + PD == 28) { if (!__all(fB == tag)) flag_wait(ws, fx4 + 56, 7, tag, lane); asm volatile("" ::: "memory"); }
                    if ((n & 1) == 0 && s + PD < 56) gf[s + PD] = ld16<SC1>(rG, lane * 16, gso + g_so(s + PD));
                    if (!BPRE && (n & 1) == 0 && s == BFS) { wait_vm<PD - 8 + BFS + 1>(); if (lane == 0) flag_set(ws, fx3b, tag); }
                    mma32(a4[n & 1], f, as_frag(gf[s])); });
        };
        struct I0 { enum { v = 0 }; }; struct I20 { enum { v = 20 }; }; struct I16 { enum { v = 16 }; }; struct I36 { enum { v = 36 }; }; struct I12 { enum { v = 12 }; }; struct I48 { enum { v = 48 }; }; struct I8 { enum { v = 8 }; };
        run(I0{}, I20{});
        {
            const rsrc_t rB2 = mk_rsrc(b2, D_ * 2);
            hres[0] = ld16<SC1>(rH, lane * 16, (T32 * 28 + 4 * mem + 2 * kh) * 1024);
            hres[1] = ld16<SC1>(rH, lane * 16, (T32 * 28 + 4 * mem + 2 * kh + 1) * 1024);
#pragma unroll
            for (int q = 0; q < 4; ++q) b2v[q] = ld8<0>(rB2, colb + hl * 8, q * 16);
        }
        bar();
        run(I20{}, I16{});
        bar();
        run(I36{}, I12{});
        bar();
        run(I48{}, I8{});
        STAMP(9);
        const f32x16 keep = kh ? a4[1] : a4[0], give = kh ? a4[0] : a4[1];
        LAS char* red = smem + L_RED;
#pragma unroll
        for (int q = 0; q < 4; ++q) *(LAS f32x4*)(red + (w * 4 + q) * 1024 + lane * 16) = (f32x4){give[4 * q], give[4 * q + 1], give[4 * q + 2], give[4 * q + 3]};
        bar();
        const rsrc_t rOut = mk_rsrc(out, S_ * D_ * 2);
#pragma unroll
        for (int q = 0; q < 4; ++q) {
            const f32x4 r = *(LAS f32x4*)(red + ((w ^ 2) * 4 + q) * 1024 + lane * 16);
            const unsigned ha = hres[q >> 1][2 * (q & 1)], hb = hres[q >> 1][2 * (q & 1) + 1], ba = b2v[q].x, bb = b2v[q].y;
            const u32x2 ov = {pk(bf_lo(ha) + (keep[4 * q] + r[0] + bf_lo(ba)), bf_hi(ha) + (keep[4 * q + 1] + r[1] + bf_hi(ba))),
                              pk(bf_lo(hb) + (keep[4 * q + 2] + r[2] + bf_lo(bb)), bf_hi(hb) + (keep[4 * q + 3] + r[3] + bf_hi(bb)))};
            st8<OUTAUX>(ov, rOut, rowo, q * 16);
        }
    }
    if (w == 0) {
        unsigned long long mn = ~0ull, mx = 0;
        for (int i = 0; i < 4; ++i) { const unsigned long long v = __hip_atomic_load((GAS unsigned long long*)(ws + 307200) + lane + 64 * i, __ATOMIC_RELAXED, __HIP_MEMORY_SCOPE_AGENT); mn = v < mn ? v : mn; mx = v > mx ? v : mx; }
        for (int o = 1; o < 64; o <<= 1) { const unsigned long long a = __shfl_xor(mn, o), c = __shfl_xor(mx, o); mn = a < mn ? a : mn; mx = c > mx ? c : mx; }
        unsigned long long d = mx - mn; d = d > 3000 ? 3000 : d;
        const unsigned long long until = __builtin_amdgcn_s_memrealtime() + SKEWK * d;
        while (__builtin_amdgcn_s_memrealtime() < until) __builtin_amdgcn_s_sleep(1);
    }
    STAMP(10);
}

extern "C" void kernel_launch(void* const* d_in, const int* in_sizes, int n_in, void* d_out, int out_size, void* d_ws, size_t ws_size, hipStream_t stream) {
    (void)in_sizes; (void)n_in; (void)out_size; (void)ws_size;
    (void)hipFuncSetAttribute((const void*)k_fused, hipFuncAttributeMaxDynamicSharedMemorySize, LDS_BYTES);
    k_fused<<<256, 512, LDS_BYTES, stream>>>((const char*)d_in[0], (const char*)d_in[1], (const char*)d_in[2], (const char*)d_in[4], (const char*)d_in[5],
                                            (const char*)d_in[6], (const char*)d_in[7], (char*)d_out, (char*)d_ws);
}
```

```cpp
#include <hip/hip_runtime.h>
#include <stdint.h>

typedef __bf16 bf16x8 __attribute__((ext_vector_type(8)));
typedef __bf16 bf16x4 __attribute__((ext_vector_type(4)));
typedef __bf16 bf16x2 __attribute__((ext_vector_type(2)));
typedef float f32x16 __attribute__((ext_vector_type(16)));
typedef float f32x4 __attribute__((ext_vector_type(4)));
typedef float f32x2 __attribute__((ext_vector_type(2)));
typedef unsigned u32x4 __attribute__((ext_vector_type(4)));
typedef unsigned u32x2 __attribute__((ext_vector_type(2)));
#define LAS __attribute__((address_space(3)))
#define GAS __attribute__((address_space(1)))
#define DI __device__ __forceinline__
typedef __amdgpu_buffer_rsrc_t rsrc_t;
typedef GAS unsigned gu32;

constexpr int S_ = 2048, D_ = 448, F_ = 1792;
constexpr unsigned FL_X1 = 0, FL_X2 = 4096, FL_E = 8192, FL_X3 = 16384;
constexpr unsigned ST_OFF = 512u << 10, O_OFF = 1u << 20, H_OFF = 4u << 20, G_OFF = 6u << 20;
constexpr unsigned ST_BYTES = 64u * 14 * 256, O_BYTES = 64u * 32 * 1024, H_BYTES = 64u * 28 * 1024, G_BYTES = 64u * 112 * 1024;
constexpr int LDS_BYTES = 163840;
constexpr int SC1 = 16;
constexpr unsigned SPIN_CAP = 1u << 18;
#define POLL_SLEEP 5
#define PD3V 10
#define PD4V 10
#define LEAD1 1
#define ROT1 2
#define XBYC 0
#define TOUCH_AT 1
#define TLBW 0
#define OUTAUX 0
#define AFS 3
#define APS 21
#define BFS 4
#define RSLEEP 1
#define BPRE 0
#define BPD 2
#define TOUCH_X 0
#define WR_X1 0
#define WPACE 1
#define WPACE2 1
#define PWARM 0
#define GSPLIT 1
#define PRIO 1
#define LPRIO 0

#define STAMP(i)
#define CSTAMP(i)
#define LSTAMP(i)

DI int rfl(int v) { return __builtin_amdgcn_readfirstlane(v); }
DI rsrc_t mk_rsrc(const void* p, unsigned bytes) { return __builtin_amdgcn_make_buffer_rsrc((void*)p, (short)0, (int)bytes, 0x00020000); }
DI void dma16(unsigned ldsaddr, unsigned voff, rsrc_t srd, unsigned soff) {
    asm volatile("s_mov_b32 m0, %0\n\ts_nop 0\n\tbuffer_load_dwordx4 %1, %2, %3 offen lds" ::"s"(ldsaddr), "v"(voff), "s"(srd), "s"(soff) : "memory");
}
DI void dma16_masked(unsigned ldsaddr, unsigned voff, rsrc_t srd, unsigned soff, unsigned long long off_mask) {
    unsigned long long keep;
    asm volatile("s_mov_b64 %0, exec\n\ts_andn2_b64 exec, exec, %5\n\ts_mov_b32 m0, %1\n\ts_nop 0\n\tbuffer_load_dwordx4 %2, %3, %4 offen lds\n\ts_mov_b64 exec, %0"
                 : "=&s"(keep) : "s"(ldsaddr), "v"(voff), "s"(srd), "s"(soff), "s"(off_mask) : "memory", "scc");
}
template <int N> DI void wait_vm() { asm volatile("s_waitcnt vmcnt(%0)" ::"i"(N) : "memory"); }
DI void bar() { asm volatile("s_waitcnt lgkmcnt(0)" ::: "memory"); __builtin_amdgcn_s_barrier(); asm volatile("" ::: "memory"); }
DI void sb() { __builtin_amdgcn_sched_barrier(0); }
template <int AUX> DI u32x4 ld16(rsrc_t r, unsigned voff, unsigned soff) { return __builtin_amdgcn_raw_buffer_load_b128(r, voff, soff, AUX); }
template <int AUX> DI u32x2 ld8(rsrc_t r, unsigned voff, unsigned soff) { return __builtin_amdgcn_raw_buffer_load_b64(r, voff, soff, AUX); }
template <int AUX> DI void st16(u32x4 v, rsrc_t r, unsigned voff, unsigned soff) { __builtin_amdgcn_raw_buffer_store_b128(v, r, voff + soff, 0, AUX); }
template <int AUX> DI void st8(u32x2 v, rsrc_t r, unsigned voff, unsigned soff) { __builtin_amdgcn_raw_buffer_store_b64(v, r, voff, soff, AUX); }
DI bf16x8 as_frag(u32x4 v) { return __builtin_bit_cast(bf16x8, v); }
DI bf16x8 lds_frag(LAS char* p) { return *(LAS bf16x8*)p; }
DI bf16x8 lds_tr(LAS char* p0, LAS char* p1) {
    bf16x4 a = __builtin_amdgcn_ds_read_tr16_b64_v4bf16((LAS bf16x4*)p0);
    bf16x4 b = __builtin_amdgcn_ds_read_tr16_b64_v4bf16((LAS bf16x4*)p1);
    return __builtin_shufflevector(a, b, 0, 1, 2, 3, 4, 5, 6, 7);
}
DI f32x4 mfma16(bf16x8 a, bf16x8 b, f32x4 c) { return __builtin_amdgcn_mfma_f32_16x16x32_bf16(a, b, c, 0, 0, 0); }
DI f32x16 mfma32(bf16x8 a, bf16x8 b, f32x16 c) { return __builtin_amdgcn_mfma_f32_32x32x16_bf16(a, b, c, 0, 0, 0); }
DI void mma16(f32x4& c, bf16x8 a, bf16x8 b) { c = mfma16(a, b, c); asm volatile("" ::"v"(c)); }
DI void mma32(f32x16& c, bf16x8 a, bf16x8 b) { c = mfma32(a, b, c); asm volatile("" ::"v"(c)); }
DI unsigned pk(float a, float b) { f32x2 v = {a, b}; bf16x2 r = __builtin_convertvector(v, bf16x2); return __builtin_bit_cast(unsigned, r); }
DI float bf_lo(unsigned v) { return __builtin_bit_cast(float, v << 16); }
DI float bf_hi(unsigned v) { return __builtin_bit_cast(float, v & 0xffff0000u); }
DI float dot2(unsigned a, unsigned b, float c) { return __builtin_amdgcn_fdot2_f32_bf16(__builtin_bit_cast(bf16x2, a), __builtin_bit_cast(bf16x2, b), c, false); }
DI unsigned f2u(float v) { return __builtin_bit_cast(unsigned, v); }
DI float u2f(unsigned v) { return __builtin_bit_cast(float, v); }
DI float sum16(float v) { auto a = __builtin_amdgcn_permlane16_swap(f2u(v), f2u(v), false, false); unsigned a0 = a[0], a1 = a[1]; return u2f(a0) + u2f(a1); }
DI float sum32(float v) { auto a = __builtin_amdgcn_permlane32_swap(f2u(v), f2u(v), false, false); unsigned a0 = a[0], a1 = a[1]; return u2f(a0) + u2f(a1); }
DI float max16(float v) { auto a = __builtin_amdgcn_permlane16_swap(f2u(v), f2u(v), false, false); unsigned a0 = a[0], a1 = a[1]; return fmaxf(u2f(a0), u2f(a1)); }
DI float max32(float v) { auto a = __builtin_amdgcn_permlane32_swap(f2u(v), f2u(v), false, false); unsigned a0 = a[0], a1 = a[1]; return fmaxf(u2f(a0), u2f(a1)); }
DI f32x16 zero16() { f32x16 z; for (int i = 0; i < 16; ++i) z[i] = 0.f; return z; }

template <int N, int PD, class LD, class MM> DI void frag_pipe(LD ld, MM mm) {
    bf16x8 buf[N];
#pragma unroll
    for (int i = 0; i < PD; ++i) buf[i] = ld(i);
    sb();
#pragma unroll
    for (int i = 0; i < N; ++i) {
        if (i + PD < N) buf[i + PD] = ld(i + PD);
        mm(i, buf[i]);
        sb();
    }
}

DI void flag_set(char* ws, unsigned off, unsigned tag) { __hip_atomic_store((gu32*)(ws + off), tag, __ATOMIC_RELAXED, __HIP_MEMORY_SCOPE_AGENT); }
DI void flag_wait(char* ws, unsigned off, int n, unsigned tag, int lane) {
    gu32* p = (gu32*)(ws + off) + (lane < n ? lane : 0);
    unsigned v0 = __hip_atomic_load(p, __ATOMIC_RELAXED, __HIP_MEMORY_SCOPE_AGENT);
    for (unsigned spin = 0; spin < SPIN_CAP; ++spin) {
        __builtin_amdgcn_s_sleep(POLL_SLEEP);
        const unsigned v1 = __hip_atomic_load(p, __ATOMIC_RELAXED, __HIP_MEMORY_SCOPE_AGENT);
        if (__all(v0 == tag)) break;
        __builtin_amdgcn_s_sleep(POLL_SLEEP);
        v0 = __hip_atomic_load(p, __ATOMIC_RELAXED, __HIP_MEMORY_SCOPE_AGENT);
        if (__all(v1 == tag)) break;
    }
    asm volatile("" ::: "memory");
}

DI unsigned img_src_lane(int lane) {
    const int i8 = lane >> 3, p8 = lane & 7;
    return (4 * ((p8 >> 2) ^ ((i8 >> 1) & 1)) + (p8 & 3)) * 16;
}
DI unsigned img_rd_lane(int lane) {
    const int G = lane >> 4, nh = G & 1, hh = G >> 1, q = (lane & 15) >> 2, p = lane & 3;
    return (4 * hh + q) * 128 + (q >> 1) * 64 + nh * 32 + 8 * p;
}

constexpr int SL1 = 32768, SL1W = 10240, L_TAB1 = 161792;
constexpr int L_STG = 131072;
DI constexpr int kst(int st) { return st < 7 ? 2 * st : 2 * (st - 7) + 1; }
DI constexpr int wo_pos(int m) { return m < 14 ? m * 4096 : 114688 + (m - 14) * 4096; }
constexpr int L_W1A = 0, L_W1B = 57344, L_RED = 147456;
constexpr int W2SLOT = 16384;

__global__ __launch_bounds__(512, 2) void k_fused(const char* __restrict__ x, const char* __restrict__ wqkv, const char* __restrict__ wo,
                                                  const char* __restrict__ w1, const char* __restrict__ b1, const char* __restrict__ w2,
                                                  const char* __restrict__ b2, char* __restrict__ out, char* __restrict__ ws) {
    extern __shared__ __attribute__((aligned(16))) char smem_[];
    LAS char* smem = (LAS char*)smem_;
    const unsigned lbase = (unsigned)(size_t)smem;
    const int tid = threadIdx.x, lane = tid & 63, w = rfl(tid >> 6);
    const int b = blockIdx.x, slot = b >> 3, T64 = (b & 7) * 4 + (slot >> 3), mem = slot & 7;
    const int PP = T64 >> 1, js = 7 * (T64 & 1) + mem;
    const int t0 = T64 * 64;
    const bool loader = w >= 4;
    const int rot1 = ROT1 == 0 ? 0 : rfl(ROT1 == 1 ? (2 * (T64 & 3) + mem) % 7 : (ROT1 == 2 ? (2 * (T64 & 3)) % 7 : mem % 7));
    STAMP(0);

    if (loader) {
        __builtin_amdgcn_s_setprio(LPRIO);
        const int lw = w - 4, i8 = lane >> 3;
        const rsrc_t rX = mk_rsrc(x, S_ * D_ * 2), rW = mk_rsrc(wqkv, D_ * 3 * D_ * 2);
        unsigned xv[3], xl[3];
#pragma unroll
        for (int m = 0; m < 3; ++m) {
            int rb = lw + 4 * m; rb = rb > 9 ? 9 : rb;
            const int rho = 8 * rb + i8;
            int tok = t0 - 16 + rho; tok = tok < 0 ? 0 : tok;
            xv[m] = tok * 896 + ((lane & 7) ^ ((rho >> 1) & 7)) * 16;
            xl[m] = lbase + rb * 1024;
        }
        const unsigned long long x0off = lw == 0 ? 0xFFull : 0ull;
        unsigned wv[6], wu[6]; unsigned long long woff[6];
#pragma unroll
        for (int m = 0; m < 6; ++m) {
            int u = lw + 4 * m; u = u > 21 ? 21 : u;
            const int c = 64 * u + lane, rs = c / 22, cc = c - rs * 22;
            const int q = rs & 31, bb = q >> 3, ii = q & 7;
            const int krow = (rs & 32) + (bb & 1) * 16 + (bb >> 1) * 4 + (ii & 3) + 8 * (ii >> 2);
            const int sec = cc / 7;
            wv[m] = krow * 2688 + (sec == 2 ? 0 : (sec + 1) * 896) + mem * 112 + (cc - sec * 7) * 16;
            wu[m] = lbase + SL1W + u * 1024;
            woff[m] = __ballot(cc == 21);
        }
        const bool lo2 = lw < 2;
        auto stage1 = [&](int st) {
            const unsigned sl = ((st + 1) & 3) * SL1;
            int kc = st + rot1; kc = kc >= 7 ? kc - 7 : kc;
            if (ROT1 != 0) { kc = rfl(kc); asm volatile("s_nop 4" : "+s"(kc)); }
            dma16_masked(xl[0] + sl, xv[0], rX, kc * 128, x0off);
            dma16(xl[1] + sl, xv[1], rX, kc * 128);
            if (lo2) dma16(xl[2] + sl, xv[2], rX, kc * 128);
#pragma unroll
            for (int m = 0; m < 5; ++m) dma16_masked(wu[m] + sl, wv[m], rW, kc * 64 * 2688, woff[m]);
            if (lo2) dma16_masked(wu[5] + sl, wv[5], rW, kc * 64 * 2688, woff[5]);
        };
        stage1(0); stage1(1); stage1(2);
        for (int st = 0; st < 7; ++st) {
            if (st >= 5) wait_vm<0>(); else if (lo2) wait_vm<9>(); else wait_vm<7>();
            bar();
            if (st <= 3) stage1(st + 3);
        }
        if (mem == 7) {
            bar(); bar(); bar();
            const int pi = (T64 & 3) * 4 + lw;
            unsigned acc = 0;
#pragma unroll 1
            for (int mtx = 0; mtx < 2; ++mtx) {
                const rsrc_t r = mk_rsrc(mtx ? w2 : w1, D_ * F_ * 2);
                unsigned v[13];
#pragma unroll
                for (int m = 0; m < 13; ++m) { const int l = (m * 16 + pi) * 64 + lane; v[m] = __builtin_amdgcn_raw_buffer_load_b32(r, l < 12544 ? l * 128 : 0x80000000u, 0, 0); }
#pragma unroll
                for (int m = 0; m < 13; ++m) acc ^= v[m];
            }
            asm volatile("" ::"v"(acc));
            return;
        }
        const unsigned isl = img_src_lane(lane);
        const rsrc_t rWo = mk_rsrc(wo, D_ * D_ * 2);
        const unsigned vo = (8 * lw + i8) * 896 + mem * 128 + isl;
        auto wo_unit = [&](int m) { dma16(lbase + wo_pos(m) + lw * 1024, vo, rWo, (m) * 28 * 896); };
#pragma unroll
        for (int m = 0; m < 12; ++m) wo_unit(m);
        LSTAMP(0);
        bar();
#pragma unroll
        for (int m = 12; m < 16; ++m) wo_unit(m);
        const rsrc_t rW1 = mk_rsrc(w1, D_ * F_ * 2);
        const unsigned v1o = i8 * 3584 + js * 256 + isl;
        auto w1_so = [&](int m) { return (unsigned)((lw + 4 * m) * 8 * 3584); };
#pragma unroll
        for (int m = 0; m < 14; ++m) dma16(lbase + L_W1B + (lw + 4 * m) * 1024, v1o + 128, rW1, w1_so(m));
        LSTAMP(1);
        bar();
        u32x4 w1r[14];
#pragma unroll
        for (int m = 0; m < 14; ++m) w1r[m] = ld16<0>(rW1, v1o, w1_so(m));
        LSTAMP(2);
        bar();
        const rsrc_t rW2 = mk_rsrc(w2, F_ * D_ * 2);
        const unsigned v2o = (8 * lw + i8) * 896 + mem * 128 + isl;
        auto w2_so = [&](int st, int m) { return (unsigned)(((m >> 1) * 896 + 64 * kst(st) + 32 * (m & 1)) * 896); };
        u32x4 wr[20];
        LSTAMP(3);
        wait_vm<28>();
        LSTAMP(4);
        bar();
        wait_vm<0>();
        bar();
#pragma unroll
        for (int m = 0; m < 14; ++m) *(LAS u32x4*)(smem + L_W1A + (lw + 4 * m) * 1024 + lane * 16) = w1r[m];
        LSTAMP(5);
        bar();
        auto w2_stage = [&](int st) {
#pragma unroll
            for (int m = 0; m < 4; ++m) dma16(lbase + ((st + 7) % 9) * W2SLOT + (m >> 1) * 8192 + ((m & 1) * 4 + lw) * 1024, v2o, rW2, w2_so(st, m));
        };
        w2_stage(0); w2_stage(1);
        u32x4 w5[16];
#pragma unroll
        for (int m = 0; m < 16; ++m) { w5[m] = ld16<0>(rW2, v2o, w2_so(5 + (m >> 2), m & 3)); if (WPACE2) { sb(); __builtin_amdgcn_s_sleep(WPACE2); sb(); } }
        bar();
        bar();
        w2_stage(2); w2_stage(3); w2_stage(4);
#pragma unroll
        for (int m = 0; m < 20; ++m) wr[m] = ld16<0>(rW2, v2o, w2_so(9 + (m >> 2), m & 3));
        LSTAMP(6);
        bar();
#pragma unroll
        for (int m = 0; m < 16; ++m)
            *(LAS u32x4*)(smem + ((5 + (m >> 2) + 7) % 9) * W2SLOT + ((m & 3) >> 1) * 8192 + ((m & 1) * 4 + lw) * 1024 + lane * 16) = w5[m];
        LSTAMP(7);
        wait_vm<0>();
        LSTAMP(8);
        bar();
        bar();
        auto refill = [&](int m) {
            *(LAS u32x4*)(smem + ((9 + (m >> 2) + 7) % 9) * W2SLOT + ((m & 3) >> 1) * 8192 + ((m & 1) * 4 + lw) * 1024 + lane * 16) = wr[m];
            if (RSLEEP) __builtin_amdgcn_s_sleep(RSLEEP);
        };
#pragma unroll
        for (int m = 0; m < 12; ++m) refill(m);
        bar();
#pragma unroll
        for (int m = 12; m < 20; ++m) refill(m);
        bar();
        bar();
        return;
    }

    __builtin_amdgcn_s_setprio(PRIO);
    unsigned tag = 0;
    {
        const u32x4 ones = {0x3F803F80u, 0x3F803F80u, 0x3F803F80u, 0x3F803F80u};
        if (lane < 8) *(LAS u32x4*)(smem + w * SL1 + lane * 16) = ones;
        const u32x4 e0 = {0x00003F80u, 0u, 0u, 0u};
        *(LAS u32x4*)(smem + w * SL1 + SL1W + lane * 352 + 336) = e0;
        tag = __hip_atomic_load((gu32*)(ws + FL_E + PP * 128), __ATOMIC_RELAXED, __HIP_MEMORY_SCOPE_AGENT) + 1u;
    }
    unsigned tch = 0;
    auto touch = [&]() {
        const char* tp = w == 0 ? wqkv : (w == 1 ? w1 : (w == 2 ? w2 : wo));
        const unsigned per = w == 0 ? 4704u : (w == 3 ? 1568u : 6272u);
        const rsrc_t rT = mk_rsrc(tp, per * 256);
        unsigned o = b * per + lane * 128; o = lane * 128 < per ? o : 0x80000000u;
        tch = __builtin_amdgcn_raw_buffer_load_b32(rT, o, 0, 0);
    };
    if (w == 0) touch();
    const int t = lane & 15, g = lane >> 4;
#pragma unroll
    for (int m = 0; m < 2; ++m) {
        const int it = tid + 256 * m, blk = it >> 4, tt = it & 15;
        if (it < 480) {
            const int sv_ = ((tt >> 1) & 1) | (((tt >> 3) & 1) << 1) | (((tt >> 2) & 1) << 2);
            const int off = (blk >= 10 && blk < 20) ? tt * 64 + ((7 ^ sv_) * 8) : 768 + tt * 16 + 8;
            *(LAS u32x2*)(smem + L_STG + blk * 1024 + off) = (u32x2){0u, 0u};
        }
    }
    LAS char* edst[3]; bool eok[3];
    {
        const int svt = ((t >> 1) & 1) | (((t >> 3) & 1) << 1) | (((t >> 2) & 1) << 2);
#pragma unroll
        for (int i = 0; i < 3; ++i) {
            const int n0 = (w * 3 + i) * 16 + 4 * g;
            const int arr = (n0 >= 56 ? 1 : 0) + (n0 >= 112 ? 1 : 0), rem = n0 - arr * 56, hh = rem >= 28 ? 1 : 0, dim = rem - 28 * hh;
            const int off = (arr == 1) ? t * 64 + (((dim >> 2) ^ svt) * 8) : (dim >> 3) * 256 + t * 16 + ((dim >> 2) & 1) * 8;
            edst[i] = smem + (n0 < 168 ? L_STG + (arr * 2 + hh) * 5 * 1024 + off : L_STG);
            eok[i] = n0 < 168;
            asm volatile("" : "+v"(edst[i]));
        }
    }
    const int uu = t - 4 * g, r0 = t & 1;
    const bool curA = uu - r0 >= 0, curB = uu - r0 - 2 >= 0;
    const bool noprev = (T64 == 0 && w == 0);
    const bool okA = curA || !noprev, okB = curB || !noprev;
    LAS char* aqk = smem + (L_STG + w * 1024 + lane * 16);
    LAS char* avv[2];
    unsigned odst;
    {
        const int vrow = 4 * g + ((lane & 15) >> 2);
        const int svr = ((vrow >> 1) & 1) | (((vrow >> 3) & 1) << 1) | (((vrow >> 2) & 1) << 2);
        avv[0] = smem + (L_STG + 10240 + w * 1024 + vrow * 64 + (((lane & 3) ^ svr) * 8)); avv[1] = smem + (L_STG + 10240 + w * 1024 + vrow * 64 + (((4 + (lane & 3)) ^ svr) * 8));
        const int T16 = 4 * T64 + w;
        odst = ((T16 >> 1) * 32 + (g >> 1) + 4 * mem) * 1024 + (32 * (g & 1) + 16 * (T16 & 1) + t) * 16;
        asm volatile("" : "+v"(aqk), "+v"(avv[0]), "+v"(avv[1]), "+v"(odst));
    }
    f32x4 acc1[5][3];
#pragma unroll
    for (int a = 0; a < 5; ++a)
#pragma unroll
        for (int i = 0; i < 3; ++i) acc1[a][i] = (f32x4){0.f, 0.f, 0.f, 0.f};
    {
        const int sg = (t >> 1) & 7;
        LAS char* xa[2]; LAS char* wa[2];
        xa[0] = smem + (SL1 + (t >> 3) * 1024 + (t & 7) * 128 + ((g ^ sg) * 16)); xa[1] = smem + (SL1 + (t >> 3) * 1024 + (t & 7) * 128 + (((4 + g) ^ sg) * 16));
        wa[0] = smem + (SL1 + SL1W + (8 * (g >> 1) + 4 * (g & 1) + ((lane & 15) >> 2)) * 352 + 8 * (lane & 3) + w * 96); wa[1] = wa[0] + 11264;
        auto trd = [&](LAS char* p) { return __builtin_amdgcn_ds_read_tr16_b64_v4bf16((LAS bf16x4*)p); };
#pragma unroll
        for (int a = 0; a < 5; ++a)
#pragma unroll
            for (int i = 0; i < 3; ++i) asm volatile("" : "+v"(acc1[a][i]));
        auto gemm1 = [&](auto W3) {
            constexpr bool w3 = decltype(W3)::v;
            bf16x4 wl[2][3], wh[2][3]; bf16x8 xf[2][5];
            auto rd = [&](int ks, int j) {
                if (j == 0) wl[ks][0] = trd(wa[ks]);
                else if (j == 1) wh[ks][0] = trd(wa[ks] + 5632);
                else if (j <= 6) xf[ks][j - 2] = lds_frag(xa[ks] + (j - 2) * 2048);
                else if (w3 && j >= 9) { }
                else if (j & 1) wl[ks][(j - 5) >> 1] = trd(wa[ks] + ((j - 5) >> 1) * 32);
                else wh[ks][(j - 6) >> 1] = trd(wa[ks] + 5632 + ((j - 6) >> 1) * 32);
            };
            bar();
            STAMP(1);
            if (w != 0) touch();
#pragma unroll
            for (int j = 0; j < 11; ++j) rd(0, j);
            xa[0] += SL1; wa[0] += SL1;
            sb();
#pragma unroll 1
            for (int st = 0; st < 7; ++st) {
                const int dd[2] = {((st + 3) & 3) == 0 ? -3 * SL1 : SL1, ((st + 2) & 3) == 0 ? -3 * SL1 : SL1};
#pragma unroll
                for (int ks = 0; ks < 2; ++ks) {
#pragma unroll
                    for (int n = 0; n < 15; ++n) {
                        const int i = n / 5, a = n % 5;
                        if (w3 && i == 2) mma16(acc1[a][2], xf[ks][a], xf[ks][a]);
                        else mma16(acc1[a][i], __builtin_shufflevector(wl[ks][i], wh[ks][i], 0, 1, 2, 3, 4, 5, 6, 7), xf[ks][a]);
                        if (n >= 1 && n < 12) rd(ks ^ 1, n - 1);
                        if (n == 13) { xa[ks ^ 1] += dd[ks ^ 1]; wa[ks ^ 1] += dd[ks ^ 1]; asm volatile("" : "+v"(xa[ks ^ 1]), "+v"(wa[ks ^ 1])); }
                        sb();
                    }
                }
                if (st < 6) { asm volatile("" ::: "memory"); __builtin_amdgcn_s_barrier(); asm volatile("" ::: "memory"); }
            }
        };
        struct BT { enum { v = 1 }; }; struct BF { enum { v = 0 }; };
        if (w == 3) gemm1(BT{}); else gemm1(BF{});
    }
    STAMP(2);
    LAS float* tst = (LAS float*)(smem + L_TAB1);
    if (w == 3) {
        const int r = t & 3;
        float d[5];
#pragma unroll
        for (int a = 0; a < 5; ++a) {
            const float da = (r & 1) ? acc1[a][2][1] : acc1[a][2][0], db = (r & 1) ? acc1[a][2][3] : acc1[a][2][2];
            d[a] = (r & 2) ? db : da;
        }
        LAS float* tp = tst + 2 * t;
        if (g == 2) {
#pragma unroll
            for (int a = 0; a < 5; ++a) tp[32 * a] = acc1[a][1][0];
        }
        if (g == (t >> 2)) {
#pragma unroll
            for (int a = 0; a < 5; ++a) tp[32 * a + 1] = d[a];
        }
    }
    asm volatile("" ::"v"(tch));
    bar();
    STAMP(11);
    {
        f32x2 nmu[5], rs[5];
#pragma unroll
        for (int a = 0; a < 5; ++a) {
            const f32x2 s = *(LAS f32x2*)(tst + (16 * a + t) * 2);
            const float mu = s[0] * (1.0f / D_);
            const float r = __builtin_amdgcn_rsqf(fmaxf(s[1] * (1.0f / D_) - mu * mu, 0.0f) + 1e-5f);
            nmu[a] = (f32x2){-mu, -mu}; rs[a] = (f32x2){r, r};
        }
#pragma unroll
        for (int i = 0; i < 3; ++i) {
            f32x4 cs;
#pragma unroll
            for (int r = 0; r < 4; ++r) { const float c = acc1[0][i][r]; cs[r] = u2f(__builtin_amdgcn_update_dpp(0, (int)f2u(c), 0x150, 0xf, 0xf, true)); }
            u32x2 pv[5];
#pragma unroll
            for (int a = 0; a < 5; ++a) {
                const f32x2 lo = {acc1[a][i][0], acc1[a][i][1]}, hi = {acc1[a][i][2], acc1[a][i][3]};
                const f32x2 v01 = (nmu[a] * (f32x2){cs[0], cs[1]} + lo) * rs[a], v23 = (nmu[a] * (f32x2){cs[2], cs[3]} + hi) * rs[a];
                pv[a] = (u32x2){pk(v01[0], v01[1]), pk(v23[0], v23[1])};
            }
            if (eok[i]) {
#pragma unroll
                for (int a = 0; a < 5; ++a) *(LAS u32x2*)(edst[i] + a * 1024) = pv[a];
            }
        }
    }
    STAMP(12);
    bar();
    STAMP(13);
    const rsrc_t rO = mk_rsrc(ws + O_OFF, O_BYTES);
    {
        const float scl = 0.18898223650461362f * 1.4426950408889634f;
        const f32x4 z = {0.f, 0.f, 0.f, 0.f};
        bf16x8 qf[2], kp[2], kc[2], vf[2][2];
#pragma unroll
        for (int hh = 0; hh < 2; ++hh) {
            qf[hh] = lds_frag(aqk + 20480 + 1024 + hh * 5120); kp[hh] = lds_frag(aqk + hh * 5120); kc[hh] = lds_frag(aqk + 1024 + hh * 5120);
        }
#pragma unroll
        for (int hh = 0; hh < 2; ++hh)
#pragma unroll
            for (int dt = 0; dt < 2; ++dt) vf[hh][dt] = lds_tr(avv[dt] + hh * 5120, avv[dt] + hh * 5120 + 1024);
#pragma unroll
        for (int hh = 0; hh < 2; ++hh) {
            const f32x4 sp = mfma16(kp[hh], qf[hh], z), sc = mfma16(kc[hh], qf[hh], z);
            const float ac = r0 ? sc[1] : sc[0], ap = r0 ? sp[1] : sp[0], bc = r0 ? sc[3] : sc[2], bp = r0 ? sp[3] : sp[2];
            const float za = okA ? (curA ? ac : ap) * scl : -3.0e38f, zb = okB ? (curB ? bc : bp) * scl : -3.0e38f;
            const float mx = max32(max16(fmaxf(za, zb)));
            const float ea = __builtin_amdgcn_exp2f(za - mx), eb = __builtin_amdgcn_exp2f(zb - mx);
            const float inv = __builtin_amdgcn_rcpf(sum32(sum16(ea + eb)));
            const unsigned pw = pk(ea * inv, eb * inv);
            const unsigned wa_ = (pw & 0xffffu) << (16 * r0), wb_ = (pw >> 16) << (16 * r0);
            const u32x4 pu = {curA ? 0u : wa_, curB ? 0u : wb_, curA ? wa_ : 0u, curB ? wb_ : 0u};
            const f32x4 o0 = mfma16(vf[hh][0], as_frag(pu), z), o1 = mfma16(vf[hh][1], as_frag(pu), z);
            const u32x4 ou = {pk(o0[0], o0[1]), pk(o0[2], o0[3]), pk(o1[0], o1[1]), pk(o1[2], o1[3])};
            st16<SC1>(ou, rO, odst, hh * 2048);
        }
    }
    STAMP(3);
    wait_vm<0>();
    STAMP(14);
    bar();
    STAMP(15);
    tag = rfl(tag);
    if (w == 0 && lane == 0) flag_set(ws, FL_X1 + T64 * 128 + mem * 4, tag);
    if (mem == 7) return;
    const int th = w & 1, kh = w >> 1, t32 = lane & 31, hl = lane >> 5;
    const int T32 = 2 * T64 + th;
    const unsigned colb = (64 * mem + 32 * kh) * 2;
    const unsigned rowo = (T32 * 32 + t32) * 896 + colb + hl * 8;
    u32x2 xr[4];
    {
        const rsrc_t rX = mk_rsrc(x, S_ * D_ * 2);
#pragma unroll
        for (int q = 0; q < 4; ++q) xr[q] = ld8<0>(rX, rowo, q * 16);
    }
    if (w == 0) flag_wait(ws, FL_X1 + T64 * 128, 8, tag, lane);
    bar();
    STAMP(4);
    const unsigned ird = img_rd_lane(lane);
    const rsrc_t rH = mk_rsrc(ws + H_OFF, H_BYTES), rSt = mk_rsrc(ws + ST_OFF, ST_BYTES);
    {
        constexpr int PD2 = 8;
        u32x4 of[16];
        const unsigned oso = (T32 * 32 + 16 * kh) * 1024;
        auto o_so = [&](int s) { return (unsigned)(s * 1024); };
#pragma unroll
        for (int s = 0; s < PD2; ++s) of[s] = ld16<SC1>(rO, lane * 16, oso + o_so(s));
        sb();
        f32x16 a2[2] = {zero16(), zero16()};
        const unsigned wo_hi = kh ? 114688 - 14 * 4096 : 0;
        frag_pipe<32, 4>([&](int n) { const int s = n >> 1, hp_ = s >> 1; LAS char* a = smem + (ird ^ ((n & 1) << 6)) + kh * 8 * 4096 + hp_ * 4096 + (hp_ >= 6 ? wo_hi : 0) + (s & 1) * 1024; return lds_tr(a, a + 2048); },
                         [&](int n, bf16x8 f) { const int s = n >> 1; if ((n & 1) == 0 && s + PD2 < 16) of[s + PD2] = ld16<SC1>(rO, lane * 16, oso + o_so(s + PD2)); mma32(a2[n & 1], f, as_frag(of[s])); });
        const f32x16 keep = kh ? a2[1] : a2[0], give = kh ? a2[0] : a2[1];
        LAS char* red = smem + L_RED;
#pragma unroll
        for (int q = 0; q < 4; ++q) *(LAS f32x4*)(red + (w * 4 + q) * 1024 + lane * 16) = (f32x4){give[4 * q], give[4 * q + 1], give[4 * q + 2], give[4 * q + 3]};
        bar();
        float hv[16];
        float s1 = 0.f, s2 = 0.f;
        unsigned hp[8];
#pragma unroll
        for (int q = 0; q < 4; ++q) {
            const f32x4 r = *(LAS f32x4*)(red + ((w ^ 2) * 4 + q) * 1024 + lane * 16);
            const unsigned xa = xr[q].x, xb = xr[q].y;
            hv[4 * q] = bf_lo(xa) + (keep[4 * q] + r[0]); hv[4 * q + 1] = bf_hi(xa) + (keep[4 * q + 1] + r[1]);
            hv[4 * q + 2] = bf_lo(xb) + (keep[4 * q + 2] + r[2]); hv[4 * q + 3] = bf_hi(xb) + (keep[4 * q + 3] + r[3]);
            hp[2 * q] = pk(hv[4 * q], hv[4 * q + 1]); hp[2 * q + 1] = pk(hv[4 * q + 2], hv[4 * q + 3]);
        }
        st16<SC1>((u32x4){hp[0], hp[1], hp[2], hp[3]}, rH, lane * 16, (T32 * 28 + 4 * mem + 2 * kh) * 1024);
        st16<SC1>((u32x4){hp[4], hp[5], hp[6], hp[7]}, rH, lane * 16, (T32 * 28 + 4 * mem + 2 * kh + 1) * 1024);
#pragma unroll
        for (int q = 0; q < 8; ++q) { const float lo = bf_lo(hp[q]), hi = bf_hi(hp[q]); s1 += lo + hi; s2 = fmaf(lo, lo, fmaf(hi, hi, s2)); }
        s1 = sum32(s1); s2 = sum32(s2);
        if (hl == 0) st8<SC1>((u32x2){f2u(s1), f2u(s2)}, rSt, t32 * 8, (T32 * 14 + 2 * mem + kh) * 256);
    }
    STAMP(5);
    unsigned short b1v = 0;
    if (tid < 128) { const rsrc_t rB1 = mk_rsrc(b1, F_ * 2); b1v = __builtin_amdgcn_raw_buffer_load_b16(rB1, (js * 128 + tid) * 2, 0, 0); }
    wait_vm<0>();
    STAMP(16);
    bar();
    STAMP(17);
    if (w == 0 && lane == 0) flag_set(ws, FL_X2 + PP * 128 + js * 4, tag);
    LAS float* tab = (LAS float*)(smem + L_RED);
    if (tid < 128) tab[128 + tid] = u2f((unsigned)b1v << 16);
    {
        const u32x4 onesu = {0x3F803F80u, 0x3F803F80u, 0x3F803F80u, 0x3F803F80u};
        f32x16 cs = zero16();
        LAS char* a0 = smem + (w >> 1) * 57344 + (ird ^ ((w & 1) << 6));
        frag_pipe<28, 4>([&](int s) { return lds_tr(a0 + s * 2048, a0 + s * 2048 + 1024); }, [&](int s, bf16x8 f) { mma32(cs, f, as_frag(onesu)); });
        if (t32 == 0) {
#pragma unroll
            for (int q = 0; q < 4; ++q) *(LAS f32x4*)(tab + 32 * w + 8 * q + 4 * hl) = (f32x4){cs[4 * q], cs[4 * q + 1], cs[4 * q + 2], cs[4 * q + 3]};
        }
    }
    if (w == 0) {
        flag_wait(ws, FL_X2 + PP * 128, 14, tag, lane);
        if (js == 0 && lane == 0) flag_set(ws, FL_E + PP * 128, tag);
    }
    bar();
    STAMP(6);
    const rsrc_t rG = mk_rsrc(ws + G_OFF, G_BYTES);
    const unsigned fx4 = FL_X3 + T32 * 128 + 7 * kh * 4;
    gu32* pA = (gu32*)(ws + fx4) + (lane < 7 ? lane : 0);
    unsigned fA = 0, pw3 = 0, pw4 = 0;
    const unsigned fx3 = FL_X3 + (4 * PP + w) * 128 + js * 4, fx3b = fx3 + 56;
    u32x4 gf[56];
    const unsigned gso = (T32 * 112 + 56 * kh) * 1024;
    auto g_so = [&](int s) { return (unsigned)((4 * kst(s >> 2) + (s & 3)) * 1024); };
    {
        const int TP = 4 * PP + w;
        u32x2 sv[7];
#pragma unroll
        for (int q = 0; q < 7; ++q) sv[q] = ld8<SC1>(rSt, t32 * 8 + hl * 256, (TP * 14 + 2 * q) * 256);
        constexpr int PD3 = PD3V;
        u32x4 hf[28];
        const unsigned hso = TP * 28 * 1024;
        auto h_so = [&](int s) { return (unsigned)(s * 1024); };
#pragma unroll
        for (int s = 0; s < PD3; ++s) hf[s] = ld16<SC1>(rH, lane * 16, hso + h_so(s));
        sb();
        f32x16 a3[4] = {zero16(), zero16(), zero16(), zero16()};
        LAS char* wA = smem + L_W1A; LAS char* wB = smem + L_W1B;
        frag_pipe<56, 4>([&](int n) { LAS char* a = wA + (ird ^ ((n & 1) << 6)) + (n >> 1) * 2048; return lds_tr(a, a + 1024); },
                         [&](int n, bf16x8 f) { const int s = n >> 1; if ((n & 1) == 0 && s + PD3 < 28) hf[s + PD3] = ld16<SC1>(rH, lane * 16, hso + h_so(s + PD3)); mma32(a3[n & 1], f, as_frag(hf[s])); });
        float s1 = 0.f, s2 = 0.f;
#pragma unroll
        for (int q = 0; q < 7; ++q) { const unsigned sa = sv[q].x, sb_ = sv[q].y; s1 += u2f(sa); s2 += u2f(sb_); }
        s1 = sum32(s1); s2 = sum32(s2);
        const float mu = s1 * (1.0f / D_);
        const float rstd = __builtin_amdgcn_rsqf(fmaxf(s2 * (1.0f / D_) - mu * mu, 0.0f) + 1e-5f);
        const f32x2 rs2 = {rstd, rstd}, nm2 = {-mu * rstd, -mu * rstd};
        const f32x2 c02 = {-2.0f * 0.7978845608028654f * 1.4426950408889634f, -2.0f * 0.7978845608028654f * 1.4426950408889634f};
        const f32x2 c12 = c02 * 0.044715f, one2 = {1.0f, 1.0f};
        STAMP(18);
        bar();
        f32x4 tcs_[16], tbb_[16];
        unsigned gq[8];
        LAS float* tabl = tab + 4 * hl;
        asm volatile("" : "+v"(tabl));
        auto gelu_ld = [&](int m) { const int i = m >> 2, q = m & 3; tcs_[m] = *(LAS f32x4*)(tabl + 32 * i + 8 * q); tbb_[m] = *(LAS f32x4*)(tabl + 128 + 32 * i + 8 * q); };
        f32x2 gy0[16], gy1[16], gd0[16], gd1[16];
        auto gelu_a = [&](int m) {
            const int i = m >> 2, q = m & 3;
            const f32x4 cs = tcs_[m], bb = tbb_[m];
            const f32x2 y0 = (f32x2){a3[i][4 * q], a3[i][4 * q + 1]} * rs2 + (nm2 * (f32x2){cs[0], cs[1]} + (f32x2){bb[0], bb[1]});
            const f32x2 y1 = (f32x2){a3[i][4 * q + 2], a3[i][4 * q + 3]} * rs2 + (nm2 * (f32x2){cs[2], cs[3]} + (f32x2){bb[2], bb[3]});
            gy0[m] = y0; gy1[m] = y1;
            gd0[m] = y0 * ((y0 * y0) * c12 + c02); gd1[m] = y1 * ((y1 * y1) * c12 + c02);
            asm volatile("" : "+v"(gd0[m]), "+v"(gd1[m]));
        };
        auto gelu_b = [&](int m) {
            gd0[m] = (f32x2){__builtin_amdgcn_exp2f(gd0[m][0]), __builtin_amdgcn_exp2f(gd0[m][1])} + one2;
            gd1[m] = (f32x2){__builtin_amdgcn_exp2f(gd1[m][0]), __builtin_amdgcn_exp2f(gd1[m][1])} + one2;
            asm volatile("" : "+v"(gd0[m]), "+v"(gd1[m]));
        };
        auto gelu_c = [&](int m) {
            const int i = m >> 2, q = m & 3;
            const f32x2 y0 = gy0[m] * (f32x2){__builtin_amdgcn_rcpf(gd0[m][0]), __builtin_amdgcn_rcpf(gd0[m][1])};
            const f32x2 y1 = gy1[m] * (f32x2){__builtin_amdgcn_rcpf(gd1[m][0]), __builtin_amdgcn_rcpf(gd1[m][1])};
            gq[2 * q] = pk(y0[0], y0[1]); gq[2 * q + 1] = pk(y1[0], y1[1]);
            if (q & 1) st16<SC1>((u32x4){gq[2 * q - 2], gq[2 * q - 1], gq[2 * q], gq[2 * q + 1]}, rG, lane * 16, (TP * 112 + 8 * js + 2 * i + (q >> 1)) * 1024);
            else asm volatile("" : "+v"(gq[2 * q]), "+v"(gq[2 * q + 1]));
        };
        auto gelu_do = [&](int m) { gelu_a(m); gelu_b(m); gelu_c(m); };
        frag_pipe<28, 4>([&](int s) { LAS char* a = wB + ird + s * 2048; return lds_tr(a, a + 1024); },
                         [&](int s, bf16x8 f) {
                             if (s % 3 == 0 && s / 3 < 8) gelu_ld(s / 3);
                             mma32(a3[2], f, as_frag(hf[s]));
                             if (s >= 2 && s < 26) { const int m = (s - 2) / 3, p = (s - 2) % 3; if (p == 0) gelu_a(m); else if (p == 1) gelu_b(m); else gelu_c(m); }
                         });
        frag_pipe<28, 4>([&](int s) { LAS char* a = wB + (ird ^ 64) + s * 2048; return lds_tr(a, a + 1024); },
                         [&](int s, bf16x8 f) {
                             if (s % 6 == 1 && s / 6 < 4) gelu_ld(8 + s / 6);
                             if (s >= 24) gelu_ld(12 + s - 24);
                             mma32(a3[3], f, as_frag(hf[s]));
                             if (s == AFS) { wait_vm<0>(); if (lane == 0) flag_set(ws, fx3, tag);
                             }
                             if (s == APS) fA = __hip_atomic_load(pA, __ATOMIC_RELAXED, __HIP_MEMORY_SCOPE_AGENT);
                             if (s % 6 == 3 && s / 6 < 4) gelu_a(8 + s / 6);
                             if (s % 6 == 4 && s / 6 < 4) gelu_b(8 + s / 6);
                             if (s % 6 == 5 && s / 6 < 4) gelu_c(8 + s / 6);
                         });
        bar();
        if (!__all(fA == tag)) flag_wait(ws, fx4, 7, tag, lane);
        asm volatile("" ::: "memory");
#pragma unroll
        for (int s = 0; s < 8; ++s) gf[s] = ld16<SC1>(rG, lane * 16, gso + g_so(s));
        sb();
#pragma unroll
        for (int q = 0; q < 4; ++q) gelu_do(12 + q);
        sb();
#pragma unroll
        for (int s = 8; s < PD4V; ++s) gf[s] = ld16<SC1>(rG, lane * 16, gso + g_so(s));
        STAMP(7);
    }
    STAMP(19);
    u32x4 hres[2]; u32x2 b2v[4];
    STAMP(8);
    {
        constexpr int PD = PD4V;
        gu32* pB = (gu32*)(ws + fx4 + 56) + (lane < 7 ? lane : 0);
        unsigned fB = 0;
        bar();
        sb();
        f32x16 a4[2] = {zero16(), zero16()};
        LAS char* wbp = smem + kh * 8192;
        auto run = [&](auto S0, auto NS) {
            constexpr int s0 = decltype(S0)::v, ns = decltype(NS)::v;
            frag_pipe<2 * ns, 4>(
                [&](int n) { const int s = s0 + (n >> 1); LAS char* a = wbp + (((s >> 2) + 7) % 9) * W2SLOT + (s & 3) * 2048 + (ird ^ ((n & 1) << 6)); return lds_tr(a, a + 1024); },
                [&](int n, bf16x8 f) {
                    const int s = s0 + (n >> 1);
                    if ((n & 1) == 0 && s + PD == 28 - BPD) fB = __hip_atomic_load(pB, __ATOMIC_RELAXED, __HIP_MEMORY_SCOPE_AGENT);
                    if ((n & 1) == 0 && s + PD == 28) { if (!__all(fB == tag)) flag_wait(ws, fx4 + 56, 7, tag, lane); asm volatile("" ::: "memory"); }
                    if ((n & 1) == 0 && s + PD < 56) gf[s + PD] = ld16<SC1>(rG, lane * 16, gso + g_so(s + PD));
                    if (!BPRE && (n & 1) == 0 && s == BFS) { wait_vm<PD - 8 + BFS + 1>(); if (lane == 0) flag_set(ws, fx3b, tag);
                    }
                    mma32(a4[n & 1], f, as_frag(gf[s])); });
        };
        struct I0 { enum { v = 0 }; }; struct I20 { enum { v = 20 }; }; struct I16 { enum { v = 16 }; }; struct I36 { enum { v = 36 }; }; struct I12 { enum { v = 12 }; }; struct I48 { enum { v = 48 }; }; struct I8 { enum { v = 8 }; };
        run(I0{}, I20{});
        {
            const rsrc_t rB2 = mk_rsrc(b2, D_ * 2);
            hres[0] = ld16<SC1>(rH, lane * 16, (T32 * 28 + 4 * mem + 2 * kh) * 1024);
            hres[1] = ld16<SC1>(rH, lane * 16, (T32 * 28 + 4 * mem + 2 * kh + 1) * 1024);
#pragma unroll
            for (int q = 0; q < 4; ++q) b2v[q] = ld8<0>(rB2, colb + hl * 8, q * 16);
        }
        bar();
        run(I20{}, I16{});
        bar();
        run(I36{}, I12{});
        bar();
        run(I48{}, I8{});
        STAMP(9);
        const f32x16 keep = kh ? a4[1] : a4[0], give = kh ? a4[0] : a4[1];
        LAS char* red = smem + L_RED;
#pragma unroll
        for (int q = 0; q < 4; ++q) *(LAS f32x4*)(red + (w * 4 + q) * 1024 + lane * 16) = (f32x4){give[4 * q], give[4 * q + 1], give[4 * q + 2], give[4 * q + 3]};
        bar();
        const rsrc_t rOut = mk_rsrc(out, S_ * D_ * 2);
#pragma unroll
        for (int q = 0; q < 4; ++q) {
            const f32x4 r = *(LAS f32x4*)(red + ((w ^ 2) * 4 + q) * 1024 + lane * 16);
            const unsigned ha = hres[q >> 1][2 * (q & 1)], hb = hres[q >> 1][2 * (q & 1) + 1], ba = b2v[q].x, bb = b2v[q].y;
            const u32x2 ov = {pk(bf_lo(ha) + (keep[4 * q] + r[0] + bf_lo(ba)), bf_hi(ha) + (keep[4 * q + 1] + r[1] + bf_hi(ba))),
                              pk(bf_lo(hb) + (keep[4 * q + 2] + r[2] + bf_lo(bb)), bf_hi(hb) + (keep[4 * q + 3] + r[3] + bf_hi(bb)))};
            st8<OUTAUX>(ov, rOut, rowo, q * 16);
        }
    }
    STAMP(10);
}

extern "C" void kernel_launch(void* const* d_in, const int* in_sizes, int n_in, void* d_out, int out_size, void* d_ws, size_t ws_size, hipStream_t stream) {
    (void)in_sizes; (void)n_in; (void)out_size; (void)ws_size;
    (void)hipFuncSetAttribute((const void*)k_fused, hipFuncAttributeMaxDynamicSharedMemorySize, LDS_BYTES);
    k_fused<<<256, 512, LDS_BYTES, stream>>>((const char*)d_in[0], (const char*)d_in[1], (const char*)d_in[2], (const char*)d_in[4], (const char*)d_in[5],
                                            (const char*)d_in[6], (const char*)d_in[7], (char*)d_out, (char*)d_ws);
}
```

```cpp
#include <hip/hip_runtime.h>
#include <stdint.h>

typedef __bf16 bf16x8 __attribute__((ext_vector_type(8)));
typedef __bf16 bf16x4 __attribute__((ext_vector_type(4)));
typedef __bf16 bf16x2 __attribute__((ext_vector_type(2)));
typedef float f32x16 __attribute__((ext_vector_type(16)));
typedef float f32x4 __attribute__((ext_vector_type(4)));
typedef float f32x2 __attribute__((ext_vector_type(2)));
typedef unsigned u32x4 __attribute__((ext_vector_type(4)));
typedef unsigned u32x2 __attribute__((ext_vector_type(2)));
#define LAS __attribute__((address_space(3)))
#define GAS __attribute__((address_space(1)))
#define DI __device__ __forceinline__
typedef __amdgpu_buffer_rsrc_t rsrc_t;
typedef GAS unsigned gu32;

constexpr int S_ = 2048, D_ = 448, F_ = 1792;
constexpr unsigned FL_X1 = 0, FL_X2 = 4096, FL_E = 8192, FL_X3 = 16384;
constexpr unsigned ST_OFF = 512u << 10, O_OFF = 1u << 20, H_OFF = 4u << 20, G_OFF = 6u << 20;
constexpr unsigned ST_BYTES = 64u * 14 * 256, O_BYTES = 64u * 32 * 1024, H_BYTES = 64u * 28 * 1024, G_BYTES = 64u * 112 * 1024;
constexpr int LDS_BYTES = 163840;
constexpr int SC1 = 16;
constexpr unsigned SPIN_CAP = 1u << 18;
#define POLL_SLEEP 5
#define PD3V 10
#define PD4V 10
#define LEAD1 1
#define ROT1 2
#define XBYC 0
#define TOUCH_AT 1
#define TLBW 0
#define OUTAUX 16
#define AFS 3
#define APS 21
#define BFS 4
#define RSLEEP 1
#define BPRE 0
#define BPD 2
#define TOUCH_X 0
#define WR_X1 0
#define WPACE 1
#define WPACE2 1
#define PWARM 0
#define GSPLIT 1
#define OUTLDS 1
#define PRIO 1
#define LPRIO 0

#define STAMP(i)
#define CSTAMP(i)
#define LSTAMP(i)

DI int rfl(int v) { return __builtin_amdgcn_readfirstlane(v); }
DI rsrc_t mk_rsrc(const void* p, unsigned bytes) { return __builtin_amdgcn_make_buffer_rsrc((void*)p, (short)0, (int)bytes, 0x00020000); }
DI void dma16(unsigned ldsaddr, unsigned voff, rsrc_t srd, unsigned soff) {
    asm volatile("s_mov_b32 m0, %0\n\ts_nop 0\n\tbuffer_load_dwordx4 %1, %2, %3 offen lds" ::"s"(ldsaddr), "v"(voff), "s"(srd), "s"(soff) : "memory");
}
DI void dma16_masked(unsigned ldsaddr, unsigned voff, rsrc_t srd, unsigned soff, unsigned long long off_mask) {
    unsigned long long keep;
    asm volatile("s_mov_b64 %0, exec\n\ts_andn2_b64 exec, exec, %5\n\ts_mov_b32 m0, %1\n\ts_nop 0\n\tbuffer_load_dwordx4 %2, %3, %4 offen lds\n\ts_mov_b64 exec, %0"
                 : "=&s"(keep) : "s"(ldsaddr), "v"(voff), "s"(srd), "s"(soff), "s"(off_mask) : "memory", "scc");
}
template <int N> DI void wait_vm() { asm volatile("s_waitcnt vmcnt(%0)" ::"i"(N) : "memory"); }
DI void bar() { asm volatile("s_waitcnt lgkmcnt(0)" ::: "memory"); __builtin_amdgcn_s_barrier(); asm volatile("" ::: "memory"); }
DI void sb() { __builtin_amdgcn_sched_barrier(0); }
template <int AUX> DI u32x4 ld16(rsrc_t r, unsigned voff, unsigned soff) { return __builtin_amdgcn_raw_buffer_load_b128(r, voff, soff, AUX); }
template <int AUX> DI u32x2 ld8(rsrc_t r, unsigned voff, unsigned soff) { return __builtin_amdgcn_raw_buffer_load_b64(r, voff, soff, AUX); }
template <int AUX> DI void st16(u32x4 v, rsrc_t r, unsigned voff, unsigned soff) { __builtin_amdgcn_raw_buffer_store_b128(v, r, voff + soff, 0, AUX); }
template <int AUX> DI void st8(u32x2 v, rsrc_t r, unsigned voff, unsigned soff) { __builtin_amdgcn_raw_buffer_store_b64(v, r, voff, soff, AUX); }
DI bf16x8 as_frag(u32x4 v) { return __builtin_bit_cast(bf16x8, v); }
DI bf16x8 lds_frag(LAS char* p) { return *(LAS bf16x8*)p; }
DI bf16x8 lds_tr(LAS char* p0, LAS char* p1) {
    bf16x4 a = __builtin_amdgcn_ds_read_tr16_b64_v4bf16((LAS bf16x4*)p0);
    bf16x4 b = __builtin_amdgcn_ds_read_tr16_b64_v4bf16((LAS bf16x4*)p1);
    return __builtin_shufflevector(a, b, 0, 1, 2, 3, 4, 5, 6, 7);
}
DI f32x4 mfma16(bf16x8 a, bf16x8 b, f32x4 c) { return __builtin_amdgcn_mfma_f32_16x16x32_bf16(a, b, c, 0, 0, 0); }
DI f32x16 mfma32(bf16x8 a, bf16x8 b, f32x16 c) { return __builtin_amdgcn_mfma_f32_32x32x16_bf16(a, b, c, 0, 0, 0); }
DI void mma16(f32x4& c, bf16x8 a, bf16x8 b) { c = mfma16(a, b, c); asm volatile("" ::"v"(c)); }
DI void mma32(f32x16& c, bf16x8 a, bf16x8 b) { c = mfma32(a, b, c); asm volatile("" ::"v"(c)); }
DI unsigned pk(float a, float b) { f32x2 v = {a, b}; bf16x2 r = __builtin_convertvector(v, bf16x2); return __builtin_bit_cast(unsigned, r); }
DI float bf_lo(unsigned v) { return __builtin_bit_cast(float, v << 16); }
DI float bf_hi(unsigned v) { return __builtin_bit_cast(float, v & 0xffff0000u); }
DI float dot2(unsigned a, unsigned b, float c) { return __builtin_amdgcn_fdot2_f32_bf16(__builtin_bit_cast(bf16x2, a), __builtin_bit_cast(bf16x2, b), c, false); }
DI unsigned f2u(float v) { return __builtin_bit_cast(unsigned, v); }
DI float u2f(unsigned v) { return __builtin_bit_cast(float, v); }
DI float sum16(float v) { auto a = __builtin_amdgcn_permlane16_swap(f2u(v), f2u(v), false, false); unsigned a0 = a[0], a1 = a[1]; return u2f(a0) + u2f(a1); }
DI float sum32(float v) { auto a = __builtin_amdgcn_permlane32_swap(f2u(v), f2u(v), false, false); unsigned a0 = a[0], a1 = a[1]; return u2f(a0) + u2f(a1); }
DI float max16(float v) { auto a = __builtin_amdgcn_permlane16_swap(f2u(v), f2u(v), false, false); unsigned a0 = a[0], a1 = a[1]; return fmaxf(u2f(a0), u2f(a1)); }
DI float max32(float v) { auto a = __builtin_amdgcn_permlane32_swap(f2u(v), f2u(v), false, false); unsigned a0 = a[0], a1 = a[1]; return fmaxf(u2f(a0), u2f(a1)); }
DI f32x16 zero16() { f32x16 z; for (int i = 0; i < 16; ++i) z[i] = 0.f; return z; }

template <int N, int PD, class LD, class MM> DI void frag_pipe(LD ld, MM mm) {
    bf16x8 buf[N];
#pragma unroll
    for (int i = 0; i < PD; ++i) buf[i] = ld(i);
    sb();
#pragma unroll
    for (int i = 0; i < N; ++i) {
        if (i + PD < N) buf[i + PD] = ld(i + PD);
        mm(i, buf[i]);
        sb();
    }
}

DI void flag_set(char* ws, unsigned off, unsigned tag) { __hip_atomic_store((gu32*)(ws + off), tag, __ATOMIC_RELAXED, __HIP_MEMORY_SCOPE_AGENT); }
DI void flag_wait(char* ws, unsigned off, int n, unsigned tag, int lane) {
    gu32* p = (gu32*)(ws + off) + (lane < n ? lane : 0);
    unsigned v0 = __hip_atomic_load(p, __ATOMIC_RELAXED, __HIP_MEMORY_SCOPE_AGENT);
    for (unsigned spin = 0; spin < SPIN_CAP; ++spin) {
        __builtin_amdgcn_s_sleep(POLL_SLEEP);
        const unsigned v1 = __hip_atomic_load(p, __ATOMIC_RELAXED, __HIP_MEMORY_SCOPE_AGENT);
        if (__all(v0 == tag)) break;
        __builtin_amdgcn_s_sleep(POLL_SLEEP);
        v0 = __hip_atomic_load(p, __ATOMIC_RELAXED, __HIP_MEMORY_SCOPE_AGENT);
        if (__all(v1 == tag)) break;
    }
    asm volatile("" ::: "memory");
}

DI unsigned img_src_lane(int lane) {
    const int i8 = lane >> 3, p8 = lane & 7;
    return (4 * ((p8 >> 2) ^ ((i8 >> 1) & 1)) + (p8 & 3)) * 16;
}
DI unsigned img_rd_lane(int lane) {
    const int G = lane >> 4, nh = G & 1, hh = G >> 1, q = (lane & 15) >> 2, p = lane & 3;
    return (4 * hh + q) * 128 + (q >> 1) * 64 + nh * 32 + 8 * p;
}

constexpr int SL1 = 32768, SL1W = 10240, L_TAB1 = 161792;
constexpr int L_STG = 131072;
DI constexpr int kst(int st) { return st < 7 ? 2 * st : 2 * (st - 7) + 1; }
DI constexpr int wo_pos(int m) { return m < 14 ? m * 4096 : 114688 + (m - 14) * 4096; }
constexpr int L_W1A = 0, L_W1B = 57344, L_RED = 147456;
constexpr int W2SLOT = 16384;

__global__ __launch_bounds__(512, 2) void k_fused(const char* __restrict__ x, const char* __restrict__ wqkv, const char* __restrict__ wo,
                                                  const char* __restrict__ w1, const char* __restrict__ b1, const char* __restrict__ w2,
                                                  const char* __restrict__ b2, char* __restrict__ out, char* __restrict__ ws) {
    extern __shared__ __attribute__((aligned(16))) char smem_[];
    LAS char* smem = (LAS char*)smem_;
    const unsigned lbase = (unsigned)(size_t)smem;
    const int tid = threadIdx.x, lane = tid & 63, w = rfl(tid >> 6);
    const int b = blockIdx.x, slot = b >> 3, T64 = (b & 7) * 4 + (slot >> 3), mem = slot & 7;
    const int PP = T64 >> 1, js = 7 * (T64 & 1) + mem;
    const int t0 = T64 * 64;
    const bool loader = w >= 4;
    const int rot1 = ROT1 == 0 ? 0 : rfl(ROT1 == 1 ? (2 * (T64 & 3) + mem) % 7 : (ROT1 == 2 ? (2 * (T64 & 3)) % 7 : mem % 7));
    STAMP(0);

    if (loader) {
        __builtin_amdgcn_s_setprio(LPRIO);
        const int lw = w - 4, i8 = lane >> 3;
        const rsrc_t rX = mk_rsrc(x, S_ * D_ * 2), rW = mk_rsrc(wqkv, D_ * 3 * D_ * 2);
        unsigned xv[3], xl[3];
#pragma unroll
        for (int m = 0; m < 3; ++m) {
            int rb = lw + 4 * m; rb = rb > 9 ? 9 : rb;
            const int rho = 8 * rb + i8;
            int tok = t0 - 16 + rho; tok = tok < 0 ? 0 : tok;
            xv[m] = tok * 896 + ((lane & 7) ^ ((rho >> 1) & 7)) * 16;
            xl[m] = lbase + rb * 1024;
        }
        const unsigned long long x0off = lw == 0 ? 0xFFull : 0ull;
        unsigned wv[6], wu[6]; unsigned long long woff[6];
#pragma unroll
        for (int m = 0; m < 6; ++m) {
            int u = lw + 4 * m; u = u > 21 ? 21 : u;
            const int c = 64 * u + lane, rs = c / 22, cc = c - rs * 22;
            const int q = rs & 31, bb = q >> 3, ii = q & 7;
            const int krow = (rs & 32) + (bb & 1) * 16 + (bb >> 1) * 4 + (ii & 3) + 8 * (ii >> 2);
            const int sec = cc / 7;
            wv[m] = krow * 2688 + (sec == 2 ? 0 : (sec + 1) * 896) + mem * 112 + (cc - sec * 7) * 16;
            wu[m] = lbase + SL1W + u * 1024;
            woff[m] = __ballot(cc == 21);
        }
        const bool lo2 = lw < 2;
        auto stage1 = [&](int st) {
            const unsigned sl = ((st + 1) & 3) * SL1;
            int kc = st + rot1; kc = kc >= 7 ? kc - 7 : kc;
            if (ROT1 != 0) { kc = rfl(kc); asm volatile("s_nop 4" : "+s"(kc)); }
            dma16_masked(xl[0] + sl, xv[0], rX, kc * 128, x0off);
            dma16(xl[1] + sl, xv[1], rX, kc * 128);
            if (lo2) dma16(xl[2] + sl, xv[2], rX, kc * 128);
#pragma unroll
            for (int m = 0; m < 5; ++m) dma16_masked(wu[m] + sl, wv[m], rW, kc * 64 * 2688, woff[m]);
            if (lo2) dma16_masked(wu[5] + sl, wv[5], rW, kc * 64 * 2688, woff[5]);
        };
        stage1(0); stage1(1); stage1(2);
        for (int st = 0; st < 7; ++st) {
            if (st >= 5) wait_vm<0>(); else if (lo2) wait_vm<9>(); else wait_vm<7>();
            bar();
            if (st <= 3) stage1(st + 3);
        }
        if (mem == 7) {
            bar(); bar(); bar();
            const int pi = (T64 & 3) * 4 + lw;
            unsigned acc = 0;
#pragma unroll 1
            for (int mtx = 0; mtx < 2; ++mtx) {
                const rsrc_t r = mk_rsrc(mtx ? w2 : w1, D_ * F_ * 2);
                unsigned v[13];
#pragma unroll
                for (int m = 0; m < 13; ++m) { const int l = (m * 16 + pi) * 64 + lane; v[m] = __builtin_amdgcn_raw_buffer_load_b32(r, l < 12544 ? l * 128 : 0x80000000u, 0, 0); }
#pragma unroll
                for (int m = 0; m < 13; ++m) acc ^= v[m];
            }
            asm volatile("" ::"v"(acc));
            return;
        }
        const unsigned isl = img_src_lane(lane);
        const rsrc_t rWo = mk_rsrc(wo, D_ * D_ * 2);
        const unsigned vo = (8 * lw + i8) * 896 + mem * 128 + isl;
        auto wo_unit = [&](int m) { dma16(lbase + wo_pos(m) + lw * 1024, vo, rWo, (m) * 28 * 896); };
#pragma unroll
        for (int m = 0; m < 12; ++m) wo_unit(m);
        LSTAMP(0);
        bar();
#pragma unroll
        for (int m = 12; m < 16; ++m) wo_unit(m);
        const rsrc_t rW1 = mk_rsrc(w1, D_ * F_ * 2);
        const unsigned v1o = i8 * 3584 + js * 256 + isl;
        auto w1_so = [&](int m) { return (unsigned)((lw + 4 * m) * 8 * 3584); };
#pragma unroll
        for (int m = 0; m < 14; ++m) dma16(lbase + L_W1B + (lw + 4 * m) * 1024, v1o + 128, rW1, w1_so(m));
        LSTAMP(1);
        bar();
        u32x4 w1r[14];
#pragma unroll
        for (int m = 0; m < 14; ++m) w1r[m] = ld16<0>(rW1, v1o, w1_so(m));
        LSTAMP(2);
        bar();
        const rsrc_t rW2 = mk_rsrc(w2, F_ * D_ * 2);
        const unsigned v2o = (8 * lw + i8) * 896 + mem * 128 + isl;
        auto w2_so = [&](int st, int m) { return (unsigned)(((m >> 1) * 896 + 64 * kst(st) + 32 * (m & 1)) * 896); };
        u32x4 wr[20];
        LSTAMP(3);
        wait_vm<28>();
        LSTAMP(4);
        bar();
        wait_vm<0>();
        bar();
#pragma unroll
        for (int m = 0; m < 14; ++m) *(LAS u32x4*)(smem + L_W1A + (lw + 4 * m) * 1024 + lane * 16) = w1r[m];
        LSTAMP(5);
        bar();
        auto w2_stage = [&](int st) {
#pragma unroll
            for (int m = 0; m < 4; ++m) dma16(lbase + ((st + 7) % 9) * W2SLOT + (m >> 1) * 8192 + ((m & 1) * 4 + lw) * 1024, v2o, rW2, w2_so(st, m));
        };
        w2_stage(0); w2_stage(1);
        u32x4 w5[16];
#pragma unroll
        for (int m = 0; m < 16; ++m) { w5[m] = ld16<0>(rW2, v2o, w2_so(5 + (m >> 2), m & 3)); if (WPACE2) { sb(); __builtin_amdgcn_s_sleep(WPACE2); sb(); } }
        bar();
        bar();
        w2_stage(2); w2_stage(3); w2_stage(4);
#pragma unroll
        for (int m = 0; m < 20; ++m) wr[m] = ld16<0>(rW2, v2o, w2_so(9 + (m >> 2), m & 3));
        LSTAMP(6);
        bar();
#pragma unroll
        for (int m = 0; m < 16; ++m)
            *(LAS u32x4*)(smem + ((5 + (m >> 2) + 7) % 9) * W2SLOT + ((m & 3) >> 1) * 8192 + ((m & 1) * 4 + lw) * 1024 + lane * 16) = w5[m];
        LSTAMP(7);
        wait_vm<0>();
        LSTAMP(8);
        bar();
        bar();
        auto refill = [&](int m) {
            *(LAS u32x4*)(smem + ((9 + (m >> 2) + 7) % 9) * W2SLOT + ((m & 3) >> 1) * 8192 + ((m & 1) * 4 + lw) * 1024 + lane * 16) = wr[m];
            if (RSLEEP) __builtin_amdgcn_s_sleep(RSLEEP);
        };
#pragma unroll
        for (int m = 0; m < 12; ++m) refill(m);
        bar();
#pragma unroll
        for (int m = 12; m < 20; ++m) refill(m);
        bar();
        bar();
        return;
    }

    __builtin_amdgcn_s_setprio(PRIO);
    unsigned tag = 0;
    {
        const u32x4 ones = {0x3F803F80u, 0x3F803F80u, 0x3F803F80u, 0x3F803F80u};
        if (lane < 8) *(LAS u32x4*)(smem + w * SL1 + lane * 16) = ones;
        const u32x4 e0 = {0x00003F80u, 0u, 0u, 0u};
        *(LAS u32x4*)(smem + w * SL1 + SL1W + lane * 352 + 336) = e0;
        tag = __hip_atomic_load((gu32*)(ws + FL_E + PP * 128), __ATOMIC_RELAXED, __HIP_MEMORY_SCOPE_AGENT) + 1u;
    }
    unsigned tch = 0;
    auto touch = [&]() {
        const char* tp = w == 0 ? wqkv : (w == 1 ? w1 : (w == 2 ? w2 : wo));
        const unsigned per = w == 0 ? 4704u : (w == 3 ? 1568u : 6272u);
        const rsrc_t rT = mk_rsrc(tp, per * 256);
        unsigned o = b * per + lane * 128; o = lane * 128 < per ? o : 0x80000000u;
        tch = __builtin_amdgcn_raw_buffer_load_b32(rT, o, 0, 0);
    };
    if (w == 0) touch();
    const int t = lane & 15, g = lane >> 4;
#pragma unroll
    for (int m = 0; m < 2; ++m) {
        const int it = tid + 256 * m, blk = it >> 4, tt = it & 15;
        if (it < 480) {
            const int sv_ = ((tt >> 1) & 1) | (((tt >> 3) & 1) << 1) | (((tt >> 2) & 1) << 2);
            const int off = (blk >= 10 && blk < 20) ? tt * 64 + ((7 ^ sv_) * 8) : 768 + tt * 16 + 8;
            *(LAS u32x2*)(smem + L_STG + blk * 1024 + off) = (u32x2){0u, 0u};
        }
    }
    LAS char* edst[3]; bool eok[3];
    {
        const int svt = ((t >> 1) & 1) | (((t >> 3) & 1) << 1) | (((t >> 2) & 1) << 2);
#pragma unroll
        for (int i = 0; i < 3; ++i) {
            const int n0 = (w * 3 + i) * 16 + 4 * g;
            const int arr = (n0 >= 56 ? 1 : 0) + (n0 >= 112 ? 1 : 0), rem = n0 - arr * 56, hh = rem >= 28 ? 1 : 0, dim = rem - 28 * hh;
            const int off = (arr == 1) ? t * 64 + (((dim >> 2) ^ svt) * 8) : (dim >> 3) * 256 + t * 16 + ((dim >> 2) & 1) * 8;
            edst[i] = smem + (n0 < 168 ? L_STG + (arr * 2 + hh) * 5 * 1024 + off : L_STG);
            eok[i] = n0 < 168;
            asm volatile("" : "+v"(edst[i]));
        }
    }
    const int uu = t - 4 * g, r0 = t & 1;
    const bool curA = uu - r0 >= 0, curB = uu - r0 - 2 >= 0;
    const bool noprev = (T64 == 0 && w == 0);
    const bool okA = curA || !noprev, okB = curB || !noprev;
    LAS char* aqk = smem + (L_STG + w * 1024 + lane * 16);
    LAS char* avv[2];
    unsigned odst;
    {
        const int vrow = 4 * g + ((lane & 15) >> 2);
        const int svr = ((vrow >> 1) & 1) | (((vrow >> 3) & 1) << 1) | (((vrow >> 2) & 1) << 2);
        avv[0] = smem + (L_STG + 10240 + w * 1024 + vrow * 64 + (((lane & 3) ^ svr) * 8)); avv[1] = smem + (L_STG + 10240 + w * 1024 + vrow * 64 + (((4 + (lane & 3)) ^ svr) * 8));
        const int T16 = 4 * T64 + w;
        odst = ((T16 >> 1) * 32 + (g >> 1) + 4 * mem) * 1024 + (32 * (g & 1) + 16 * (T16 & 1) + t) * 16;
        asm volatile("" : "+v"(aqk), "+v"(avv[0]), "+v"(avv[1]), "+v"(odst));
    }
    f32x4 acc1[5][3];
#pragma unroll
    for (int a = 0; a < 5; ++a)
#pragma unroll
        for (int i = 0; i < 3; ++i) acc1[a][i] = (f32x4){0.f, 0.f, 0.f, 0.f};
    {
        const int sg = (t >> 1) & 7;
        LAS char* xa[2]; LAS char* wa[2];
        xa[0] = smem + (SL1 + (t >> 3) * 1024 + (t & 7) * 128 + ((g ^ sg) * 16)); xa[1] = smem + (SL1 + (t >> 3) * 1024 + (t & 7) * 128 + (((4 + g) ^ sg) * 16));
        wa[0] = smem + (SL1 + SL1W + (8 * (g >> 1) + 4 * (g & 1) + ((lane & 15) >> 2)) * 352 + 8 * (lane & 3) + w * 96); wa[1] = wa[0] + 11264;
        auto trd = [&](LAS char* p) { return __builtin_amdgcn_ds_read_tr16_b64_v4bf16((LAS bf16x4*)p); };
#pragma unroll
        for (int a = 0; a < 5; ++a)
#pragma unroll
            for (int i = 0; i < 3; ++i) asm volatile("" : "+v"(acc1[a][i]));
        auto gemm1 = [&](auto W3) {
            constexpr bool w3 = decltype(W3)::v;
            bf16x4 wl[2][3], wh[2][3]; bf16x8 xf[2][5];
            auto rd = [&](int ks, int j) {
                if (j == 0) wl[ks][0] = trd(wa[ks]);
                else if (j == 1) wh[ks][0] = trd(wa[ks] + 5632);
                else if (j <= 6) xf[ks][j - 2] = lds_frag(xa[ks] + (j - 2) * 2048);
                else if (w3 && j >= 9) { }
                else if (j & 1) wl[ks][(j - 5) >> 1] = trd(wa[ks] + ((j - 5) >> 1) * 32);
                else wh[ks][(j - 6) >> 1] = trd(wa[ks] + 5632 + ((j - 6) >> 1) * 32);
            };
            bar();
            STAMP(1);
            if (w != 0) touch();
#pragma unroll
            for (int j = 0; j < 11; ++j) rd(0, j);
            xa[0] += SL1; wa[0] += SL1;
            sb();
#pragma unroll 1
            for (int st = 0; st < 7; ++st) {
                const int dd[2] = {((st + 3) & 3) == 0 ? -3 * SL1 : SL1, ((st + 2) & 3) == 0 ? -3 * SL1 : SL1};
#pragma unroll
                for (int ks = 0; ks < 2; ++ks) {
#pragma unroll
                    for (int n = 0; n < 15; ++n) {
                        const int i = n / 5, a = n % 5;
                        if (w3 && i == 2) mma16(acc1[a][2], xf[ks][a], xf[ks][a]);
                        else mma16(acc1[a][i], __builtin_shufflevector(wl[ks][i], wh[ks][i], 0, 1, 2, 3, 4, 5, 6, 7), xf[ks][a]);
                        if (n >= 1 && n < 12) rd(ks ^ 1, n - 1);
                        if (n == 13) { xa[ks ^ 1] += dd[ks ^ 1]; wa[ks ^ 1] += dd[ks ^ 1]; asm volatile("" : "+v"(xa[ks ^ 1]), "+v"(wa[ks ^ 1])); }
                        sb();
                    }
                }
                if (st < 6) { asm volatile("" ::: "memory"); __builtin_amdgcn_s_barrier(); asm volatile("" ::: "memory"); }
            }
        };
        struct BT { enum { v = 1 }; }; struct BF { enum { v = 0 }; };
        if (w == 3) gemm1(BT{}); else gemm1(BF{});
    }
    STAMP(2);
    LAS float* tst = (LAS float*)(smem + L_TAB1);
    if (w == 3) {
        const int r = t & 3;
        float d[5];
#pragma unroll
        for (int a = 0; a < 5; ++a) {
            const float da = (r & 1) ? acc1[a][2][1] : acc1[a][2][0], db = (r & 1) ? acc1[a][2][3] : acc1[a][2][2];
            d[a] = (r & 2) ? db : da;
        }
        LAS float* tp = tst + 2 * t;
        if (g == 2) {
#pragma unroll
            for (int a = 0; a < 5; ++a) tp[32 * a] = acc1[a][1][0];
        }
        if (g == (t >> 2)) {
#pragma unroll
            for (int a = 0; a < 5; ++a) tp[32 * a + 1] = d[a];
        }
    }
    asm volatile("" ::"v"(tch));
    bar();
    STAMP(11);
    {
        f32x2 nmu[5], rs[5];
#pragma unroll
        for (int a = 0; a < 5; ++a) {
            const f32x2 s = *(LAS f32x2*)(tst + (16 * a + t) * 2);
            const float mu = s[0] * (1.0f / D_);
            const float r = __builtin_amdgcn_rsqf(fmaxf(s[1] * (1.0f / D_) - mu * mu, 0.0f) + 1e-5f);
            nmu[a] = (f32x2){-mu, -mu}; rs[a] = (f32x2){r, r};
        }
#pragma unroll
        for (int i = 0; i < 3; ++i) {
            f32x4 cs;
#pragma unroll
            for (int r = 0; r < 4; ++r) { const float c = acc1[0][i][r]; cs[r] = u2f(__builtin_amdgcn_update_dpp(0, (int)f2u(c), 0x150, 0xf, 0xf, true)); }
            u32x2 pv[5];
#pragma unroll
            for (int a = 0; a < 5; ++a) {
                const f32x2 lo = {acc1[a][i][0], acc1[a][i][1]}, hi = {acc1[a][i][2], acc1[a][i][3]};
                const f32x2 v01 = (nmu[a] * (f32x2){cs[0], cs[1]} + lo) * rs[a], v23 = (nmu[a] * (f32x2){cs[2], cs[3]} + hi) * rs[a];
                pv[a] = (u32x2){pk(v01[0], v01[1]), pk(v23[0], v23[1])};
            }
            if (eok[i]) {
#pragma unroll
                for (int a = 0; a < 5; ++a) *(LAS u32x2*)(edst[i] + a * 1024) = pv[a];
            }
        }
    }
    STAMP(12);
    bar();
    STAMP(13);
    const rsrc_t rO = mk_rsrc(ws + O_OFF, O_BYTES);
    {
        const float scl = 0.18898223650461362f * 1.4426950408889634f;
        const f32x4 z = {0.f, 0.f, 0.f, 0.f};
        bf16x8 qf[2], kp[2], kc[2], vf[2][2];
#pragma unroll
        for (int hh = 0; hh < 2; ++hh) {
            qf[hh] = lds_frag(aqk + 20480 + 1024 + hh * 5120); kp[hh] = lds_frag(aqk + hh * 5120); kc[hh] = lds_frag(aqk + 1024 + hh * 5120);
        }
#pragma unroll
        for (int hh = 0; hh < 2; ++hh)
#pragma unroll
            for (int dt = 0; dt < 2; ++dt) vf[hh][dt] = lds_tr(avv[dt] + hh * 5120, avv[dt] + hh * 5120 + 1024);
#pragma unroll
        for (int hh = 0; hh < 2; ++hh) {
            const f32x4 sp = mfma16(kp[hh], qf[hh], z), sc = mfma16(kc[hh], qf[hh], z);
            const float ac = r0 ? sc[1] : sc[0], ap = r0 ? sp[1] : sp[0], bc = r0 ? sc[3] : sc[2], bp = r0 ? sp[3] : sp[2];
            const float za = okA ? (curA ? ac : ap) * scl : -3.0e38f, zb = okB ? (curB ? bc : bp) * scl : -3.0e38f;
            const float mx = max32(max16(fmaxf(za, zb)));
            const float ea = __builtin_amdgcn_exp2f(za - mx), eb = __builtin_amdgcn_exp2f(zb - mx);
            const float inv = __builtin_amdgcn_rcpf(sum32(sum16(ea + eb)));
            const unsigned pw = pk(ea * inv, eb * inv);
            const unsigned wa_ = (pw & 0xffffu) << (16 * r0), wb_ = (pw >> 16) << (16 * r0);
            const u32x4 pu = {curA ? 0u : wa_, curB ? 0u : wb_, curA ? wa_ : 0u, curB ? wb_ : 0u};
            const f32x4 o0 = mfma16(vf[hh][0], as_frag(pu), z), o1 = mfma16(vf[hh][1], as_frag(pu), z);
            const u32x4 ou = {pk(o0[0], o0[1]), pk(o0[2], o0[3]), pk(o1[0], o1[1]), pk(o1[2], o1[3])};
            st16<SC1>(ou, rO, odst, hh * 2048);
        }
    }
    STAMP(3);
    wait_vm<0>();
    STAMP(14);
    bar();
    STAMP(15);
    tag = rfl(tag);
    if (w == 0 && lane == 0) flag_set(ws, FL_X1 + T64 * 128 + mem * 4, tag);
    if (mem == 7) return;
    const int th = w & 1, kh = w >> 1, t32 = lane & 31, hl = lane >> 5;
    const int T32 = 2 * T64 + th;
    const unsigned colb = (64 * mem + 32 * kh) * 2;
    const unsigned rowo = (T32 * 32 + t32) * 896 + colb + hl * 8;
    u32x2 xr[4];
    {
        const rsrc_t rX = mk_rsrc(x, S_ * D_ * 2);
#pragma unroll
        for (int q = 0; q < 4; ++q) xr[q] = ld8<0>(rX, rowo, q * 16);
    }
    if (w == 0) flag_wait(ws, FL_X1 + T64 * 128, 8, tag, lane);
    bar();
    STAMP(4);
    const unsigned ird = img_rd_lane(lane);
    const rsrc_t rH = mk_rsrc(ws + H_OFF, H_BYTES), rSt = mk_rsrc(ws + ST_OFF, ST_BYTES);
    {
        constexpr int PD2 = 8;
        u32x4 of[16];
        const unsigned oso = (T32 * 32 + 16 * kh) * 1024;
        auto o_so = [&](int s) { return (unsigned)(s * 1024); };
#pragma unroll
        for (int s = 0; s < PD2; ++s) of[s] = ld16<SC1>(rO, lane * 16, oso + o_so(s));
        sb();
        f32x16 a2[2] = {zero16(), zero16()};
        const unsigned wo_hi = kh ? 114688 - 14 * 4096 : 0;
        frag_pipe<32, 4>([&](int n) { const int s = n >> 1, hp_ = s >> 1; LAS char* a = smem + (ird ^ ((n & 1) << 6)) + kh * 8 * 4096 + hp_ * 4096 + (hp_ >= 6 ? wo_hi : 0) + (s & 1) * 1024; return lds_tr(a, a + 2048); },
                         [&](int n, bf16x8 f) { const int s = n >> 1; if ((n & 1) == 0 && s + PD2 < 16) of[s + PD2] = ld16<SC1>(rO, lane * 16, oso + o_so(s + PD2)); mma32(a2[n & 1], f, as_frag(of[s])); });
        const f32x16 keep = kh ? a2[1] : a2[0], give = kh ? a2[0] : a2[1];
        LAS char* red = smem + L_RED;
#pragma unroll
        for (int q = 0; q < 4; ++q) *(LAS f32x4*)(red + (w * 4 + q) * 1024 + lane * 16) = (f32x4){give[4 * q], give[4 * q + 1], give[4 * q + 2], give[4 * q + 3]};
        bar();
        float hv[16];
        float s1 = 0.f, s2 = 0.f;
        unsigned hp[8];
#pragma unroll
        for (int q = 0; q < 4; ++q) {
            const f32x4 r = *(LAS f32x4*)(red + ((w ^ 2) * 4 + q) * 1024 + lane * 16);
            const unsigned xa = xr[q].x, xb = xr[q].y;
            hv[4 * q] = bf_lo(xa) + (keep[4 * q] + r[0]); hv[4 * q + 1] = bf_hi(xa) + (keep[4 * q + 1] + r[1]);
            hv[4 * q + 2] = bf_lo(xb) + (keep[4 * q + 2] + r[2]); hv[4 * q + 3] = bf_hi(xb) + (keep[4 * q + 3] + r[3]);
            hp[2 * q] = pk(hv[4 * q], hv[4 * q + 1]); hp[2 * q + 1] = pk(hv[4 * q + 2], hv[4 * q + 3]);
        }
        st16<SC1>((u32x4){hp[0], hp[1], hp[2], hp[3]}, rH, lane * 16, (T32 * 28 + 4 * mem + 2 * kh) * 1024);
        st16<SC1>((u32x4){hp[4], hp[5], hp[6], hp[7]}, rH, lane * 16, (T32 * 28 + 4 * mem + 2 * kh + 1) * 1024);
#pragma unroll
        for (int q = 0; q < 8; ++q) { const float lo = bf_lo(hp[q]), hi = bf_hi(hp[q]); s1 += lo + hi; s2 = fmaf(lo, lo, fmaf(hi, hi, s2)); }
        s1 = sum32(s1); s2 = sum32(s2);
        if (hl == 0) st8<SC1>((u32x2){f2u(s1), f2u(s2)}, rSt, t32 * 8, (T32 * 14 + 2 * mem + kh) * 256);
    }
    STAMP(5);
    unsigned short b1v = 0;
    if (tid < 128) { const rsrc_t rB1 = mk_rsrc(b1, F_ * 2); b1v = __builtin_amdgcn_raw_buffer_load_b16(rB1, (js * 128 + tid) * 2, 0, 0); }
    wait_vm<0>();
    STAMP(16);
    bar();
    STAMP(17);
    if (w == 0 && lane == 0) flag_set(ws, FL_X2 + PP * 128 + js * 4, tag);
    LAS float* tab = (LAS float*)(smem + L_RED);
    if (tid < 128) tab[128 + tid] = u2f((unsigned)b1v << 16);
    {
        const u32x4 onesu = {0x3F803F80u, 0x3F803F80u, 0x3F803F80u, 0x3F803F80u};
        f32x16 cs = zero16();
        LAS char* a0 = smem + (w >> 1) * 57344 + (ird ^ ((w & 1) << 6));
        frag_pipe<28, 4>([&](int s) { return lds_tr(a0 + s * 2048, a0 + s * 2048 + 1024); }, [&](int s, bf16x8 f) { mma32(cs, f, as_frag(onesu)); });
        if (t32 == 0) {
#pragma unroll
            for (int q = 0; q < 4; ++q) *(LAS f32x4*)(tab + 32 * w + 8 * q + 4 * hl) = (f32x4){cs[4 * q], cs[4 * q + 1], cs[4 * q + 2], cs[4 * q + 3]};
        }
    }
    if (w == 0) {
        flag_wait(ws, FL_X2 + PP * 128, 14, tag, lane);
        if (js == 0 && lane == 0) flag_set(ws, FL_E + PP * 128, tag);
    }
    bar();
    STAMP(6);
    const rsrc_t rG = mk_rsrc(ws + G_OFF, G_BYTES);
    const unsigned fx4 = FL_X3 + T32 * 128 + 7 * kh * 4;
    gu32* pA = (gu32*)(ws + fx4) + (lane < 7 ? lane : 0);
    unsigned fA = 0, pw3 = 0, pw4 = 0;
    const unsigned fx3 = FL_X3 + (4 * PP + w) * 128 + js * 4, fx3b = fx3 + 56;
    u32x4 gf[56];
    const unsigned gso = (T32 * 112 + 56 * kh) * 1024;
    auto g_so = [&](int s) { return (unsigned)((4 * kst(s >> 2) + (s & 3)) * 1024); };
    {
        const int TP = 4 * PP + w;
        u32x2 sv[7];
#pragma unroll
        for (int q = 0; q < 7; ++q) sv[q] = ld8<SC1>(rSt, t32 * 8 + hl * 256, (TP * 14 + 2 * q) * 256);
        constexpr int PD3 = PD3V;
        u32x4 hf[28];
        const unsigned hso = TP * 28 * 1024;
        auto h_so = [&](int s) { return (unsigned)(s * 1024); };
#pragma unroll
        for (int s = 0; s < PD3; ++s) hf[s] = ld16<SC1>(rH, lane * 16, hso + h_so(s));
        sb();
        f32x16 a3[4] = {zero16(), zero16(), zero16(), zero16()};
        LAS char* wA = smem + L_W1A; LAS char* wB = smem + L_W1B;
        frag_pipe<56, 4>([&](int n) { LAS char* a = wA + (ird ^ ((n & 1) << 6)) + (n >> 1) * 2048; return lds_tr(a, a + 1024); },
                         [&](int n, bf16x8 f) { const int s = n >> 1; if ((n & 1) == 0 && s + PD3 < 28) hf[s + PD3] = ld16<SC1>(rH, lane * 16, hso + h_so(s + PD3)); mma32(a3[n & 1], f, as_frag(hf[s])); });
        float s1 = 0.f, s2 = 0.f;
#pragma unroll
        for (int q = 0; q < 7; ++q) { const unsigned sa = sv[q].x, sb_ = sv[q].y; s1 += u2f(sa); s2 += u2f(sb_); }
        s1 = sum32(s1); s2 = sum32(s2);
        const float mu = s1 * (1.0f / D_);
        const float rstd = __builtin_amdgcn_rsqf(fmaxf(s2 * (1.0f / D_) - mu * mu, 0.0f) + 1e-5f);
        const f32x2 rs2 = {rstd, rstd}, nm2 = {-mu * rstd, -mu * rstd};
        const f32x2 c02 = {-2.0f * 0.7978845608028654f * 1.4426950408889634f, -2.0f * 0.7978845608028654f * 1.4426950408889634f};
        const f32x2 c12 = c02 * 0.044715f, one2 = {1.0f, 1.0f};
        STAMP(18);
        bar();
        f32x4 tcs_[16], tbb_[16];
        unsigned gq[8];
        LAS float* tabl = tab + 4 * hl;
        asm volatile("" : "+v"(tabl));
        auto gelu_ld = [&](int m) { const int i = m >> 2, q = m & 3; tcs_[m] = *(LAS f32x4*)(tabl + 32 * i + 8 * q); tbb_[m] = *(LAS f32x4*)(tabl + 128 + 32 * i + 8 * q); };
        f32x2 gy0[16], gy1[16], gd0[16], gd1[16];
        auto gelu_a = [&](int m) {
            const int i = m >> 2, q = m & 3;
            const f32x4 cs = tcs_[m], bb = tbb_[m];
            const f32x2 y0 = (f32x2){a3[i][4 * q], a3[i][4 * q + 1]} * rs2 + (nm2 * (f32x2){cs[0], cs[1]} + (f32x2){bb[0], bb[1]});
            const f32x2 y1 = (f32x2){a3[i][4 * q + 2], a3[i][4 * q + 3]} * rs2 + (nm2 * (f32x2){cs[2], cs[3]} + (f32x2){bb[2], bb[3]});
            gy0[m] = y0; gy1[m] = y1;
            gd0[m] = y0 * ((y0 * y0) * c12 + c02); gd1[m] = y1 * ((y1 * y1) * c12 + c02);
            asm volatile("" : "+v"(gd0[m]), "+v"(gd1[m]));
        };
        auto gelu_b = [&](int m) {
            gd0[m] = (f32x2){__builtin_amdgcn_exp2f(gd0[m][0]), __builtin_amdgcn_exp2f(gd0[m][1])} + one2;
            gd1[m] = (f32x2){__builtin_amdgcn_exp2f(gd1[m][0]), __builtin_amdgcn_exp2f(gd1[m][1])} + one2;
            asm volatile("" : "+v"(gd0[m]), "+v"(gd1[m]));
        };
        auto gelu_c = [&](int m) {
            const int i = m >> 2, q = m & 3;
            const f32x2 y0 = gy0[m] * (f32x2){__builtin_amdgcn_rcpf(gd0[m][0]), __builtin_amdgcn_rcpf(gd0[m][1])};
            const f32x2 y1 = gy1[m] * (f32x2){__builtin_amdgcn_rcpf(gd1[m][0]), __builtin_amdgcn_rcpf(gd1[m][1])};
            gq[2 * q] = pk(y0[0], y0[1]); gq[2 * q + 1] = pk(y1[0], y1[1]);
            if (q & 1) st16<SC1>((u32x4){gq[2 * q - 2], gq[2 * q - 1], gq[2 * q], gq[2 * q + 1]}, rG, lane * 16, (TP * 112 + 8 * js + 2 * i + (q >> 1)) * 1024);
            else asm volatile("" : "+v"(gq[2 * q]), "+v"(gq[2 * q + 1]));
        };
        auto gelu_do = [&](int m) { gelu_a(m); gelu_b(m); gelu_c(m); };
        frag_pipe<28, 4>([&](int s) { LAS char* a = wB + ird + s * 2048; return lds_tr(a, a + 1024); },
                         [&](int s, bf16x8 f) {
                             if (s % 3 == 0 && s / 3 < 8) gelu_ld(s / 3);
                             mma32(a3[2], f, as_frag(hf[s]));
                             if (s >= 2 && s < 26) { const int m = (s - 2) / 3, p = (s - 2) % 3; if (p == 0) gelu_a(m); else if (p == 1) gelu_b(m); else gelu_c(m); }
                         });
        frag_pipe<28, 4>([&](int s) { LAS char* a = wB + (ird ^ 64) + s * 2048; return lds_tr(a, a + 1024); },
                         [&](int s, bf16x8 f) {
                             if (s % 6 == 1 && s / 6 < 4) gelu_ld(8 + s / 6);
                             if (s >= 24) gelu_ld(12 + s - 24);
                             mma32(a3[3], f, as_frag(hf[s]));
                             if (s == AFS) { wait_vm<0>(); if (lane == 0) flag_set(ws, fx3, tag);
                             }
                             if (s == APS) fA = __hip_atomic_load(pA, __ATOMIC_RELAXED, __HIP_MEMORY_SCOPE_AGENT);
                             if (s % 6 == 3 && s / 6 < 4) gelu_a(8 + s / 6);
                             if (s % 6 == 4 && s / 6 < 4) gelu_b(8 + s / 6);
                             if (s % 6 == 5 && s / 6 < 4) gelu_c(8 + s / 6);
                         });
        bar();
        if (!__all(fA == tag)) flag_wait(ws, fx4, 7, tag, lane);
        asm volatile("" ::: "memory");
#pragma unroll
        for (int s = 0; s < 8; ++s) gf[s] = ld16<SC1>(rG, lane * 16, gso + g_so(s));
        sb();
#pragma unroll
        for (int q = 0; q < 4; ++q) gelu_do(12 + q);
        sb();
#pragma unroll
        for (int s = 8; s < PD4V; ++s) gf[s] = ld16<SC1>(rG, lane * 16, gso + g_so(s));
        STAMP(7);
    }
    STAMP(19);
    u32x4 hres[2]; u32x2 b2v[4];
    STAMP(8);
    {
        constexpr int PD = PD4V;
        gu32* pB = (gu32*)(ws + fx4 + 56) + (lane < 7 ? lane : 0);
        unsigned fB = 0;
        bar();
        sb();
        f32x16 a4[2] = {zero16(), zero16()};
        LAS char* wbp = smem + kh * 8192;
        auto run = [&](auto S0, auto NS) {
            constexpr int s0 = decltype(S0)::v, ns = decltype(NS)::v;
            frag_pipe<2 * ns, 4>(
                [&](int n) { const int s = s0 + (n >> 1); LAS char* a = wbp + (((s >> 2) + 7) % 9) * W2SLOT + (s & 3) * 2048 + (ird ^ ((n & 1) << 6)); return lds_tr(a, a + 1024); },
                [&](int n, bf16x8 f) {
                    const int s = s0 + (n >> 1);
                    if ((n & 1) == 0 && s + PD == 28 - BPD) fB = __hip_atomic_load(pB, __ATOMIC_RELAXED, __HIP_MEMORY_SCOPE_AGENT);
                    if ((n & 1) == 0 && s + PD == 28) { if (!__all(fB == tag)) flag_wait(ws, fx4 + 56, 7, tag, lane); asm volatile("" ::: "memory"); }
                    if ((n & 1) == 0 && s + PD < 56) gf[s + PD] = ld16<SC1>(rG, lane * 16, gso + g_so(s + PD));
                    if (!BPRE && (n & 1) == 0 && s == BFS) { wait_vm<PD - 8 + BFS + 1>(); if (lane == 0) flag_set(ws, fx3b, tag);
                    }
                    mma32(a4[n & 1], f, as_frag(gf[s])); });
        };
        struct I0 { enum { v = 0 }; }; struct I20 { enum { v = 20 }; }; struct I16 { enum { v = 16 }; }; struct I36 { enum { v = 36 }; }; struct I12 { enum { v = 12 }; }; struct I48 { enum { v = 48 }; }; struct I8 { enum { v = 8 }; };
        run(I0{}, I20{});
        {
            const rsrc_t rB2 = mk_rsrc(b2, D_ * 2);
            hres[0] = ld16<SC1>(rH, lane * 16, (T32 * 28 + 4 * mem + 2 * kh) * 1024);
            hres[1] = ld16<SC1>(rH, lane * 16, (T32 * 28 + 4 * mem + 2 * kh + 1) * 1024);
#pragma unroll
            for (int q = 0; q < 4; ++q) b2v[q] = ld8<0>(rB2, colb + hl * 8, q * 16);
        }
        bar();
        run(I20{}, I16{});
        bar();
        run(I36{}, I12{});
        bar();
        run(I48{}, I8{});
        STAMP(9);
        const f32x16 keep = kh ? a4[1] : a4[0], give = kh ? a4[0] : a4[1];
        LAS char* red = smem + L_RED;
#pragma unroll
        for (int q = 0; q < 4; ++q) *(LAS f32x4*)(red + (w * 4 + q) * 1024 + lane * 16) = (f32x4){give[4 * q], give[4 * q + 1], give[4 * q + 2], give[4 * q + 3]};
        bar();
        const rsrc_t rOut = mk_rsrc(out, S_ * D_ * 2);
        LAS char* ot = smem + 3 * W2SLOT;
        const int orow = 32 * th + t32;
#pragma unroll
        for (int q = 0; q < 4; ++q) {
            const f32x4 r = *(LAS f32x4*)(red + ((w ^ 2) * 4 + q) * 1024 + lane * 16);
            const unsigned ha = hres[q >> 1][2 * (q & 1)], hb = hres[q >> 1][2 * (q & 1) + 1], ba = b2v[q].x, bb = b2v[q].y;
            const u32x2 ov = {pk(bf_lo(ha) + (keep[4 * q] + r[0] + bf_lo(ba)), bf_hi(ha) + (keep[4 * q + 1] + r[1] + bf_hi(ba))),
                              pk(bf_lo(hb) + (keep[4 * q + 2] + r[2] + bf_lo(bb)), bf_hi(hb) + (keep[4 * q + 3] + r[3] + bf_hi(bb)))};
            *(LAS u32x2*)(ot + orow * 128 + (((4 * kh + q) ^ (orow & 7)) * 16) + 8 * hl) = ov;
        }
        bar();
#pragma unroll
        for (int m = 0; m < 2; ++m) {
            const int row = 16 * w + 8 * m + (lane >> 3), c = lane & 7;
            const u32x4 v = *(LAS u32x4*)(ot + row * 128 + ((c ^ (row & 7)) * 16));
            st16<OUTAUX>(v, rOut, (t0 + row) * 896 + mem * 128 + c * 16, 0);
        }
    }
    STAMP(10);
}

extern "C" void kernel_launch(void* const* d_in, const int* in_sizes, int n_in, void* d_out, int out_size, void* d_ws, size_t ws_size, hipStream_t stream) {
    (void)in_sizes; (void)n_in; (void)out_size; (void)ws_size;
    (void)hipFuncSetAttribute((const void*)k_fused, hipFuncAttributeMaxDynamicSharedMemorySize, LDS_BYTES);
    k_fused<<<256, 512, LDS_BYTES, stream>>>((const char*)d_in[0], (const char*)d_in[1], (const char*)d_in[2], (const char*)d_in[4], (const char*)d_in[5],
                                            (const char*)d_in[6], (const char*)d_in[7], (char*)d_out, (char*)d_ws);
}
```

```cpp
#include <hip/hip_runtime.h>
#include <stdint.h>

typedef __bf16 bf16x8 __attribute__((ext_vector_type(8)));
typedef __bf16 bf16x4 __attribute__((ext_vector_type(4)));
typedef __bf16 bf16x2 __attribute__((ext_vector_type(2)));
typedef float f32x16 __attribute__((ext_vector_type(16)));
typedef float f32x4 __attribute__((ext_vector_type(4)));
typedef float f32x2 __attribute__((ext_vector_type(2)));
typedef unsigned u32x4 __attribute__((ext_vector_type(4)));
typedef unsigned u32x2 __attribute__((ext_vector_type(2)));
#define LAS __attribute__((address_space(3)))
#define GAS __attribute__((address_space(1)))
#define DI __device__ __forceinline__
typedef __amdgpu_buffer_rsrc_t rsrc_t;
typedef GAS unsigned gu32;

constexpr int S_ = 2048, D_ = 448, F_ = 1792;
constexpr unsigned FL_X1 = 0, FL_X2 = 4096, FL_E = 8192, FL_X3 = 16384;
constexpr unsigned ST_OFF = 512u << 10, O_OFF = 1u << 20, H_OFF = 4u << 20, G_OFF = 6u << 20;
constexpr unsigned ST_BYTES = 64u * 14 * 256, O_BYTES = 64u * 32 * 1024, H_BYTES = 64u * 28 * 1024, G_BYTES = 64u * 112 * 1024;
constexpr int LDS_BYTES = 163840;
constexpr int SC1 = 16;
constexpr unsigned SPIN_CAP = 1u << 18;
#define POLL_SLEEP 5
#define PD3V 10
#define PD4V 10
#define LEAD1 1
#define ROT1 2
#define XBYC 0
#define TOUCH_AT 1
#define TLBW 1
#define OUTAUX 16
#define AFS 3
#define APS 21
#define BFS 4
#define RSLEEP 1
#define BPRE 0
#define BPD 2
#define TOUCH_X 0
#define WR_X1 0
#define WPACE 1
#define WPACE2 1
#define PWARM 0
#define GSPLIT 1
#define OUTLDS 1
#define PRIO 1
#define LPRIO 0

#define STAMP(i)
#define CSTAMP(i)
#define LSTAMP(i)

DI int rfl(int v) { return __builtin_amdgcn_readfirstlane(v); }
DI rsrc_t mk_rsrc(const void* p, unsigned bytes) { return __builtin_amdgcn_make_buffer_rsrc((void*)p, (short)0, (int)bytes, 0x00020000); }
DI void dma16(unsigned ldsaddr, unsigned voff, rsrc_t srd, unsigned soff) {
    asm volatile("s_mov_b32 m0, %0\n\ts_nop 0\n\tbuffer_load_dwordx4 %1, %2, %3 offen lds" ::"s"(ldsaddr), "v"(voff), "s"(srd), "s"(soff) : "memory");
}
DI void dma16_masked(unsigned ldsaddr, unsigned voff, rsrc_t srd, unsigned soff, unsigned long long off_mask) {
    unsigned long long keep;
    asm volatile("s_mov_b64 %0, exec\n\ts_andn2_b64 exec, exec, %5\n\ts_mov_b32 m0, %1\n\ts_nop 0\n\tbuffer_load_dwordx4 %2, %3, %4 offen lds\n\ts_mov_b64 exec, %0"
                 : "=&s"(keep) : "s"(ldsaddr), "v"(voff), "s"(srd), "s"(soff), "s"(off_mask) : "memory", "scc");
}
template <int N> DI void wait_vm() { asm volatile("s_waitcnt vmcnt(%0)" ::"i"(N) : "memory"); }
DI void bar() { asm volatile("s_waitcnt lgkmcnt(0)" ::: "memory"); __builtin_amdgcn_s_barrier(); asm volatile("" ::: "memory"); }
DI void sb() { __builtin_amdgcn_sched_barrier(0); }
template <int AUX> DI u32x4 ld16(rsrc_t r, unsigned voff, unsigned soff) { return __builtin_amdgcn_raw_buffer_load_b128(r, voff, soff, AUX); }
template <int AUX> DI u32x2 ld8(rsrc_t r, unsigned voff, unsigned soff) { return __builtin_amdgcn_raw_buffer_load_b64(r, voff, soff, AUX); }
template <int AUX> DI void st16(u32x4 v, rsrc_t r, unsigned voff, unsigned soff) { __builtin_amdgcn_raw_buffer_store_b128(v, r, voff + soff, 0, AUX); }
template <int AUX> DI void st8(u32x2 v, rsrc_t r, unsigned voff, unsigned soff) { __builtin_amdgcn_raw_buffer_store_b64(v, r, voff, soff, AUX); }
DI bf16x8 as_frag(u32x4 v) { return __builtin_bit_cast(bf16x8, v); }
DI bf16x8 lds_frag(LAS char* p) { return *(LAS bf16x8*)p; }
DI bf16x8 lds_tr(LAS char* p0, LAS char* p1) {
    bf16x4 a = __builtin_amdgcn_ds_read_tr16_b64_v4bf16((LAS bf16x4*)p0);
    bf16x4 b = __builtin_amdgcn_ds_read_tr16_b64_v4bf16((LAS bf16x4*)p1);
    return __builtin_shufflevector(a, b, 0, 1, 2, 3, 4, 5, 6, 7);
}
DI f32x4 mfma16(bf16x8 a, bf16x8 b, f32x4 c) { return __builtin_amdgcn_mfma_f32_16x16x32_bf16(a, b, c, 0, 0, 0); }
DI f32x16 mfma32(bf16x8 a, bf16x8 b, f32x16 c) { return __builtin_amdgcn_mfma_f32_32x32x16_bf16(a, b, c, 0, 0, 0); }
DI void mma16(f32x4& c, bf16x8 a, bf16x8 b) { c = mfma16(a, b, c); asm volatile("" ::"v"(c)); }
DI void mma32(f32x16& c, bf16x8 a, bf16x8 b) { c = mfma32(a, b, c); asm volatile("" ::"v"(c)); }
DI unsigned pk(float a, float b) { f32x2 v = {a, b}; bf16x2 r = __builtin_convertvector(v, bf16x2); return __builtin_bit_cast(unsigned, r); }
DI float bf_lo(unsigned v) { return __builtin_bit_cast(float, v << 16); }
DI float bf_hi(unsigned v) { return __builtin_bit_cast(float, v & 0xffff0000u); }
DI float dot2(unsigned a, unsigned b, float c) { return __builtin_amdgcn_fdot2_f32_bf16(__builtin_bit_cast(bf16x2, a), __builtin_bit_cast(bf16x2, b), c, false); }
DI unsigned f2u(float v) { return __builtin_bit_cast(unsigned, v); }
DI float u2f(unsigned v) { return __builtin_bit_cast(float, v); }
DI float sum16(float v) { auto a = __builtin_amdgcn_permlane16_swap(f2u(v), f2u(v), false, false); unsigned a0 = a[0], a1 = a[1]; return u2f(a0) + u2f(a1); }
DI float sum32(float v) { auto a = __builtin_amdgcn_permlane32_swap(f2u(v), f2u(v), false, false); unsigned a0 = a[0], a1 = a[1]; return u2f(a0) + u2f(a1); }
DI float max16(float v) { auto a = __builtin_amdgcn_permlane16_swap(f2u(v), f2u(v), false, false); unsigned a0 = a[0], a1 = a[1]; return fmaxf(u2f(a0), u2f(a1)); }
DI float max32(float v) { auto a = __builtin_amdgcn_permlane32_swap(f2u(v), f2u(v), false, false); unsigned a0 = a[0], a1 = a[1]; return fmaxf(u2f(a0), u2f(a1)); }
DI f32x16 zero16() { f32x16 z; for (int i = 0; i < 16; ++i) z[i] = 0.f; return z; }

template <int N, int PD, class LD, class MM> DI void frag_pipe(LD ld, MM mm) {
    bf16x8 buf[N];
#pragma unroll
    for (int i = 0; i < PD; ++i) buf[i] = ld(i);
    sb();
#pragma unroll
    for (int i = 0; i < N; ++i) {
        if (i + PD < N) buf[i + PD] = ld(i + PD);
        mm(i, buf[i]);
        sb();
    }
}

DI void flag_set(char* ws, unsigned off, unsigned tag) { __hip_atomic_store((gu32*)(ws + off), tag, __ATOMIC_RELAXED, __HIP_MEMORY_SCOPE_AGENT); }
DI void flag_wait(char* ws, unsigned off, int n, unsigned tag, int lane) {
    gu32* p = (gu32*)(ws + off) + (lane < n ? lane : 0);
    unsigned v0 = __hip_atomic_load(p, __ATOMIC_RELAXED, __HIP_MEMORY_SCOPE_AGENT);
    for (unsigned spin = 0; spin < SPIN_CAP; ++spin) {
        __builtin_amdgcn_s_sleep(POLL_SLEEP);
        const unsigned v1 = __hip_atomic_load(p, __ATOMIC_RELAXED, __HIP_MEMORY_SCOPE_AGENT);
        if (__all(v0 == tag)) break;
        __builtin_amdgcn_s_sleep(POLL_SLEEP);
        v0 = __hip_atomic_load(p, __ATOMIC_RELAXED, __HIP_MEMORY_SCOPE_AGENT);
        if (__all(v1 == tag)) break;
    }
    asm volatile("" ::: "memory");
}

DI unsigned img_src_lane(int lane) {
    const int i8 = lane >> 3, p8 = lane & 7;
    return (4 * ((p8 >> 2) ^ ((i8 >> 1) & 1)) + (p8 & 3)) * 16;
}
DI unsigned img_rd_lane(int lane) {
    const int G = lane >> 4, nh = G & 1, hh = G >> 1, q = (lane & 15) >> 2, p = lane & 3;
    return (4 * hh + q) * 128 + (q >> 1) * 64 + nh * 32 + 8 * p;
}

constexpr int SL1 = 32768, SL1W = 10240, L_TAB1 = 161792;
constexpr int L_STG = 131072;
DI constexpr int kst(int st) { return st < 7 ? 2 * st : 2 * (st - 7) + 1; }
DI constexpr int wo_pos(int m) { return m < 14 ? m * 4096 : 114688 + (m - 14) * 4096; }
constexpr int L_W1A = 0, L_W1B = 57344, L_RED = 147456;
constexpr int W2SLOT = 16384;

__global__ __launch_bounds__(512, 2) void k_fused(const char* __restrict__ x, const char* __restrict__ wqkv, const char* __restrict__ wo,
                                                  const char* __restrict__ w1, const char* __restrict__ b1, const char* __restrict__ w2,
                                                  const char* __restrict__ b2, char* __restrict__ out, char* __restrict__ ws) {
    extern __shared__ __attribute__((aligned(16))) char smem_[];
    LAS char* smem = (LAS char*)smem_;
    const unsigned lbase = (unsigned)(size_t)smem;
    const int tid = threadIdx.x, lane = tid & 63, w = rfl(tid >> 6);
    const int b = blockIdx.x, slot = b >> 3, T64 = (b & 7) * 4 + (slot >> 3), mem = slot & 7;
    const int PP = T64 >> 1, js = 7 * (T64 & 1) + mem;
    const int t0 = T64 * 64;
    const bool loader = w >= 4;
    const int rot1 = ROT1 == 0 ? 0 : rfl(ROT1 == 1 ? (2 * (T64 & 3) + mem) % 7 : (ROT1 == 2 ? (2 * (T64 & 3)) % 7 : mem % 7));
    STAMP(0);

    if (loader) {
        __builtin_amdgcn_s_setprio(LPRIO);
        const int lw = w - 4, i8 = lane >> 3;
        const rsrc_t rX = mk_rsrc(x, S_ * D_ * 2), rW = mk_rsrc(wqkv, D_ * 3 * D_ * 2);
        unsigned xv[3], xl[3];
#pragma unroll
        for (int m = 0; m < 3; ++m) {
            int rb = lw + 4 * m; rb = rb > 9 ? 9 : rb;
            const int rho = 8 * rb + i8;
            int tok = t0 - 16 + rho; tok = tok < 0 ? 0 : tok;
            xv[m] = tok * 896 + ((lane & 7) ^ ((rho >> 1) & 7)) * 16;
            xl[m] = lbase + rb * 1024;
        }
        const unsigned long long x0off = lw == 0 ? 0xFFull : 0ull;
        unsigned wv[6], wu[6]; unsigned long long woff[6];
#pragma unroll
        for (int m = 0; m < 6; ++m) {
            int u = lw + 4 * m; u = u > 21 ? 21 : u;
            const int c = 64 * u + lane, rs = c / 22, cc = c - rs * 22;
            const int q = rs & 31, bb = q >> 3, ii = q & 7;
            const int krow = (rs & 32) + (bb & 1) * 16 + (bb >> 1) * 4 + (ii & 3) + 8 * (ii >> 2);
            const int sec = cc / 7;
            wv[m] = krow * 2688 + (sec == 2 ? 0 : (sec + 1) * 896) + mem * 112 + (cc - sec * 7) * 16;
            wu[m] = lbase + SL1W + u * 1024;
            woff[m] = __ballot(cc == 21);
        }
        const bool lo2 = lw < 2;
        auto stage1 = [&](int st) {
            const unsigned sl = ((st + 1) & 3) * SL1;
            int kc = st + rot1; kc = kc >= 7 ? kc - 7 : kc;
            if (ROT1 != 0) { kc = rfl(kc); asm volatile("s_nop 4" : "+s"(kc)); }
            dma16_masked(xl[0] + sl, xv[0], rX, kc * 128, x0off);
            dma16(xl[1] + sl, xv[1], rX, kc * 128);
            if (lo2) dma16(xl[2] + sl, xv[2], rX, kc * 128);
#pragma unroll
            for (int m = 0; m < 5; ++m) dma16_masked(wu[m] + sl, wv[m], rW, kc * 64 * 2688, woff[m]);
            if (lo2) dma16_masked(wu[5] + sl, wv[5], rW, kc * 64 * 2688, woff[5]);
        };
        stage1(0); stage1(1); stage1(2);
        for (int st = 0; st < 7; ++st) {
            if (st >= 5) wait_vm<0>(); else if (lo2) wait_vm<9>(); else wait_vm<7>();
            bar();
            if (st <= 3) stage1(st + 3);
        }
        if (mem == 7) {
            bar(); bar(); bar();
            const int pi = (T64 & 3) * 4 + lw;
            unsigned acc = 0;
#pragma unroll 1
            for (int mtx = 0; mtx < 2; ++mtx) {
                const rsrc_t r = mk_rsrc(mtx ? w2 : w1, D_ * F_ * 2);
                unsigned v[13];
#pragma unroll
                for (int m = 0; m < 13; ++m) { const int l = (m * 16 + pi) * 64 + lane; v[m] = __builtin_amdgcn_raw_buffer_load_b32(r, l < 12544 ? l * 128 : 0x80000000u, 0, 0); }
#pragma unroll
                for (int m = 0; m < 13; ++m) acc ^= v[m];
            }
            asm volatile("" ::"v"(acc));
            return;
        }
        const unsigned isl = img_src_lane(lane);
        const rsrc_t rWo = mk_rsrc(wo, D_ * D_ * 2);
        const unsigned vo = (8 * lw + i8) * 896 + mem * 128 + isl;
        auto wo_unit = [&](int m) { dma16(lbase + wo_pos(m) + lw * 1024, vo, rWo, (m) * 28 * 896); };
#pragma unroll
        for (int m = 0; m < 12; ++m) wo_unit(m);
        LSTAMP(0);
        bar();
#pragma unroll
        for (int m = 12; m < 16; ++m) wo_unit(m);
        const rsrc_t rW1 = mk_rsrc(w1, D_ * F_ * 2);
        const unsigned v1o = i8 * 3584 + js * 256 + isl;
        auto w1_so = [&](int m) { return (unsigned)((lw + 4 * m) * 8 * 3584); };
#pragma unroll
        for (int m = 0; m < 14; ++m) dma16(lbase + L_W1B + (lw + 4 * m) * 1024, v1o + 128, rW1, w1_so(m));
        LSTAMP(1);
        bar();
        u32x4 w1r[14];
#pragma unroll
        for (int m = 0; m < 14; ++m) w1r[m] = ld16<0>(rW1, v1o, w1_so(m));
        LSTAMP(2);
        bar();
        const rsrc_t rW2 = mk_rsrc(w2, F_ * D_ * 2);
        const unsigned v2o = (8 * lw + i8) * 896 + mem * 128 + isl;
        auto w2_so = [&](int st, int m) { return (unsigned)(((m >> 1) * 896 + 64 * kst(st) + 32 * (m & 1)) * 896); };
        u32x4 wr[20];
        LSTAMP(3);
        wait_vm<28>();
        LSTAMP(4);
        bar();
        wait_vm<0>();
        bar();
#pragma unroll
        for (int m = 0; m < 14; ++m) *(LAS u32x4*)(smem + L_W1A + (lw + 4 * m) * 1024 + lane * 16) = w1r[m];
        LSTAMP(5);
        bar();
        auto w2_stage = [&](int st) {
#pragma unroll
            for (int m = 0; m < 4; ++m) dma16(lbase + ((st + 7) % 9) * W2SLOT + (m >> 1) * 8192 + ((m & 1) * 4 + lw) * 1024, v2o, rW2, w2_so(st, m));
        };
        w2_stage(0); w2_stage(1);
        u32x4 w5[16];
#pragma unroll
        for (int m = 0; m < 16; ++m) { w5[m] = ld16<0>(rW2, v2o, w2_so(5 + (m >> 2), m & 3)); if (WPACE2) { sb(); __builtin_amdgcn_s_sleep(WPACE2); sb(); } }
        bar();
        bar();
        w2_stage(2); w2_stage(3); w2_stage(4);
#pragma unroll
        for (int m = 0; m < 20; ++m) wr[m] = ld16<0>(rW2, v2o, w2_so(9 + (m >> 2), m & 3));
        LSTAMP(6);
        bar();
#pragma unroll
        for (int m = 0; m < 16; ++m)
            *(LAS u32x4*)(smem + ((5 + (m >> 2) + 7) % 9) * W2SLOT + ((m & 3) >> 1) * 8192 + ((m & 1) * 4 + lw) * 1024 + lane * 16) = w5[m];
        LSTAMP(7);
        wait_vm<0>();
        LSTAMP(8);
        bar();
        bar();
        auto refill = [&](int m) {
            *(LAS u32x4*)(smem + ((9 + (m >> 2) + 7) % 9) * W2SLOT + ((m & 3) >> 1) * 8192 + ((m & 1) * 4 + lw) * 1024 + lane * 16) = wr[m];
            if (RSLEEP) __builtin_amdgcn_s_sleep(RSLEEP);
        };
#pragma unroll
        for (int m = 0; m < 12; ++m) refill(m);
        bar();
#pragma unroll
        for (int m = 12; m < 20; ++m) refill(m);
        bar();
        bar();
        return;
    }

    __builtin_amdgcn_s_setprio(PRIO);
    unsigned tag = 0;
    {
        const u32x4 ones = {0x3F803F80u, 0x3F803F80u, 0x3F803F80u, 0x3F803F80u};
        if (lane < 8) *(LAS u32x4*)(smem + w * SL1 + lane * 16) = ones;
        const u32x4 e0 = {0x00003F80u, 0u, 0u, 0u};
        *(LAS u32x4*)(smem + w * SL1 + SL1W + lane * 352 + 336) = e0;
        tag = __hip_atomic_load((gu32*)(ws + FL_E + PP * 128), __ATOMIC_RELAXED, __HIP_MEMORY_SCOPE_AGENT) + 1u;
    }
    unsigned tch = 0;
    auto touch = [&]() {
        const char* tp = w == 0 ? wqkv : (w == 1 ? w1 : (w == 2 ? w2 : wo));
        const unsigned per = w == 0 ? 4704u : (w == 3 ? 1568u : 6272u);
        const rsrc_t rT = mk_rsrc(tp, per * 256);
        unsigned o = b * per + lane * 128; o = lane * 128 < per ? o : 0x80000000u;
        tch = __builtin_amdgcn_raw_buffer_load_b32(rT, o, 0, 0);
    };
    if (w == 0) touch();
    if (w == 2) {
        const rsrc_t rWs = mk_rsrc(ws, 16u << 20);
        unsigned o = 0x80000000u;
        if (lane < 2) o = O_OFF + ((2 * T64 + lane) * 32 + 4 * mem) * 1024;
        else if (mem < 7) {
            if (lane < 4) o = H_OFF + ((2 * T64 + lane - 2) * 28 + 4 * mem) * 1024;
            else if (lane < 6) o = ST_OFF + ((2 * T64 + lane - 4) * 14 + 2 * mem) * 256;
            else if (lane < 10) o = G_OFF + ((4 * PP + lane - 6) * 112 + 8 * js) * 1024;
        }
        __builtin_amdgcn_raw_buffer_store_b32(0u, rWs, o, 0, SC1);
    }
    if (w == 3 && mem < 7) {
        const rsrc_t rOutW = mk_rsrc(out, S_ * D_ * 2);
        __builtin_amdgcn_raw_buffer_store_b32(0u, rOutW, lane < 4 ? (t0 + 16 * lane) * 896 + mem * 128 : 0x80000000u, 0, 0);
    }
    unsigned tb = 0;
    if (w == 1) tb = __builtin_amdgcn_raw_buffer_load_b32(mk_rsrc(lane < 32 ? b1 : b1, F_ * 2), 0, 0, 0) ^ __builtin_amdgcn_raw_buffer_load_b32(mk_rsrc(b2, D_ * 2), 0, 0, 0);
    const int t = lane & 15, g = lane >> 4;
#pragma unroll
    for (int m = 0; m < 2; ++m) {
        const int it = tid + 256 * m, blk = it >> 4, tt = it & 15;
        if (it < 480) {
            const int sv_ = ((tt >> 1) & 1) | (((tt >> 3) & 1) << 1) | (((tt >> 2) & 1) << 2);
            const int off = (blk >= 10 && blk < 20) ? tt * 64 + ((7 ^ sv_) * 8) : 768 + tt * 16 + 8;
            *(LAS u32x2*)(smem + L_STG + blk * 1024 + off) = (u32x2){0u, 0u};
        }
    }
    LAS char* edst[3]; bool eok[3];
    {
        const int svt = ((t >> 1) & 1) | (((t >> 3) & 1) << 1) | (((t >> 2) & 1) << 2);
#pragma unroll
        for (int i = 0; i < 3; ++i) {
            const int n0 = (w * 3 + i) * 16 + 4 * g;
            const int arr = (n0 >= 56 ? 1 : 0) + (n0 >= 112 ? 1 : 0), rem = n0 - arr * 56, hh = rem >= 28 ? 1 : 0, dim = rem - 28 * hh;
            const int off = (arr == 1) ? t * 64 + (((dim >> 2) ^ svt) * 8) : (dim >> 3) * 256 + t * 16 + ((dim >> 2) & 1) * 8;
            edst[i] = smem + (n0 < 168 ? L_STG + (arr * 2 + hh) * 5 * 1024 + off : L_STG);
            eok[i] = n0 < 168;
            asm volatile("" : "+v"(edst[i]));
        }
    }
    const int uu = t - 4 * g, r0 = t & 1;
    const bool curA = uu - r0 >= 0, curB = uu - r0 - 2 >= 0;
    const bool noprev = (T64 == 0 && w == 0);
    const bool okA = curA || !noprev, okB = curB || !noprev;
    LAS char* aqk = smem + (L_STG + w * 1024 + lane * 16);
    LAS char* avv[2];
    unsigned odst;
    {
        const int vrow = 4 * g + ((lane & 15) >> 2);
        const int svr = ((vrow >> 1) & 1) | (((vrow >> 3) & 1) << 1) | (((vrow >> 2) & 1) << 2);
        avv[0] = smem + (L_STG + 10240 + w * 1024 + vrow * 64 + (((lane & 3) ^ svr) * 8)); avv[1] = smem + (L_STG + 10240 + w * 1024 + vrow * 64 + (((4 + (lane & 3)) ^ svr) * 8));
        const int T16 = 4 * T64 + w;
        odst = ((T16 >> 1) * 32 + (g >> 1) + 4 * mem) * 1024 + (32 * (g & 1) + 16 * (T16 & 1) + t) * 16;
        asm volatile("" : "+v"(aqk), "+v"(avv[0]), "+v"(avv[1]), "+v"(odst));
    }
    f32x4 acc1[5][3];
#pragma unroll
    for (int a = 0; a < 5; ++a)
#pragma unroll
        for (int i = 0; i < 3; ++i) acc1[a][i] = (f32x4){0.f, 0.f, 0.f, 0.f};
    {
        const int sg = (t >> 1) & 7;
        LAS char* xa[2]; LAS char* wa[2];
        xa[0] = smem + (SL1 + (t >> 3) * 1024 + (t & 7) * 128 + ((g ^ sg) * 16)); xa[1] = smem + (SL1 + (t >> 3) * 1024 + (t & 7) * 128 + (((4 + g) ^ sg) * 16));
        wa[0] = smem + (SL1 + SL1W + (8 * (g >> 1) + 4 * (g & 1) + ((lane & 15) >> 2)) * 352 + 8 * (lane & 3) + w * 96); wa[1] = wa[0] + 11264;
        auto trd = [&](LAS char* p) { return __builtin_amdgcn_ds_read_tr16_b64_v4bf16((LAS bf16x4*)p); };
#pragma unroll
        for (int a = 0; a < 5; ++a)
#pragma unroll
            for (int i = 0; i < 3; ++i) asm volatile("" : "+v"(acc1[a][i]));
        auto gemm1 = [&](auto W3) {
            constexpr bool w3 = decltype(W3)::v;
            bf16x4 wl[2][3], wh[2][3]; bf16x8 xf[2][5];
            auto rd = [&](int ks, int j) {
                if (j == 0) wl[ks][0] = trd(wa[ks]);
                else if (j == 1) wh[ks][0] = trd(wa[ks] + 5632);
                else if (j <= 6) xf[ks][j - 2] = lds_frag(xa[ks] + (j - 2) * 2048);
                else if (w3 && j >= 9) { }
                else if (j & 1) wl[ks][(j - 5) >> 1] = trd(wa[ks] + ((j - 5) >> 1) * 32);
                else wh[ks][(j - 6) >> 1] = trd(wa[ks] + 5632 + ((j - 6) >> 1) * 32);
            };
            bar();
            STAMP(1);
            if (w != 0) touch();
#pragma unroll
            for (int j = 0; j < 11; ++j) rd(0, j);
            xa[0] += SL1; wa[0] += SL1;
            sb();
#pragma unroll 1
            for (int st = 0; st < 7; ++st) {
                const int dd[2] = {((st + 3) & 3) == 0 ? -3 * SL1 : SL1, ((st + 2) & 3) == 0 ? -3 * SL1 : SL1};
#pragma unroll
                for (int ks = 0; ks < 2; ++ks) {
#pragma unroll
                    for (int n = 0; n < 15; ++n) {
                        const int i = n / 5, a = n % 5;
                        if (w3 && i == 2) mma16(acc1[a][2], xf[ks][a], xf[ks][a]);
                        else mma16(acc1[a][i], __builtin_shufflevector(wl[ks][i], wh[ks][i], 0, 1, 2, 3, 4, 5, 6, 7), xf[ks][a]);
                        if (n >= 1 && n < 12) rd(ks ^ 1, n - 1);
                        if (n == 13) { xa[ks ^ 1] += dd[ks ^ 1]; wa[ks ^ 1] += dd[ks ^ 1]; asm volatile("" : "+v"(xa[ks ^ 1]), "+v"(wa[ks ^ 1])); }
                        sb();
                    }
                }
                if (st < 6) { asm volatile("" ::: "memory"); __builtin_amdgcn_s_barrier(); asm volatile("" ::: "memory"); }
            }
        };
        struct BT { enum { v = 1 }; }; struct BF { enum { v = 0 }; };
        if (w == 3) gemm1(BT{}); else gemm1(BF{});
    }
    STAMP(2);
    LAS float* tst = (LAS float*)(smem + L_TAB1);
    if (w == 3) {
        const int r = t & 3;
        float d[5];
#pragma unroll
        for (int a = 0; a < 5; ++a) {
            const float da = (r & 1) ? acc1[a][2][1] : acc1[a][2][0], db = (r & 1) ? acc1[a][2][3] : acc1[a][2][2];
            d[a] = (r & 2) ? db : da;
        }
        LAS float* tp = tst + 2 * t;
        if (g == 2) {
#pragma unroll
            for (int a = 0; a < 5; ++a) tp[32 * a] = acc1[a][1][0];
        }
        if (g == (t >> 2)) {
#pragma unroll
            for (int a = 0; a < 5; ++a) tp[32 * a + 1] = d[a];
        }
    }
    asm volatile("" ::"v"(tch));
    asm volatile("" ::"v"(tb));
    wait_vm<0>();
    bar();
    STAMP(11);
    {
        f32x2 nmu[5], rs[5];
#pragma unroll
        for (int a = 0; a < 5; ++a) {
            const f32x2 s = *(LAS f32x2*)(tst + (16 * a + t) * 2);
            const float mu = s[0] * (1.0f / D_);
            const float r = __builtin_amdgcn_rsqf(fmaxf(s[1] * (1.0f / D_) - mu * mu, 0.0f) + 1e-5f);
            nmu[a] = (f32x2){-mu, -mu}; rs[a] = (f32x2){r, r};
        }
#pragma unroll
        for (int i = 0; i < 3; ++i) {
            f32x4 cs;
#pragma unroll
            for (int r = 0; r < 4; ++r) { const float c = acc1[0][i][r]; cs[r] = u2f(__builtin_amdgcn_update_dpp(0, (int)f2u(c), 0x150, 0xf, 0xf, true)); }
            u32x2 pv[5];
#pragma unroll
            for (int a = 0; a < 5; ++a) {
                const f32x2 lo = {acc1[a][i][0], acc1[a][i][1]}, hi = {acc1[a][i][2], acc1[a][i][3]};
                const f32x2 v01 = (nmu[a] * (f32x2){cs[0], cs[1]} + lo) * rs[a], v23 = (nmu[a] * (f32x2){cs[2], cs[3]} + hi) * rs[a];
                pv[a] = (u32x2){pk(v01[0], v01[1]), pk(v23[0], v23[1])};
            }
            if (eok[i]) {
#pragma unroll
                for (int a = 0; a < 5; ++a) *(LAS u32x2*)(edst[i] + a * 1024) = pv[a];
            }
        }
    }
    STAMP(12);
    bar();
    STAMP(13);
    const rsrc_t rO = mk_rsrc(ws + O_OFF, O_BYTES);
    {
        const float scl = 0.18898223650461362f * 1.4426950408889634f;
        const f32x4 z = {0.f, 0.f, 0.f, 0.f};
        bf16x8 qf[2], kp[2], kc[2], vf[2][2];
#pragma unroll
        for (int hh = 0; hh < 2; ++hh) {
            qf[hh] = lds_frag(aqk + 20480 + 1024 + hh * 5120); kp[hh] = lds_frag(aqk + hh * 5120); kc[hh] = lds_frag(aqk + 1024 + hh * 5120);
        }
#pragma unroll
        for (int hh = 0; hh < 2; ++hh)
#pragma unroll
            for (int dt = 0; dt < 2; ++dt) vf[hh][dt] = lds_tr(avv[dt] + hh * 5120, avv[dt] + hh * 5120 + 1024);
#pragma unroll
        for (int hh = 0; hh < 2; ++hh) {
            const f32x4 sp = mfma16(kp[hh], qf[hh], z), sc = mfma16(kc[hh], qf[hh], z);
            const float ac = r0 ? sc[1] : sc[0], ap = r0 ? sp[1] : sp[0], bc = r0 ? sc[3] : sc[2], bp = r0 ? sp[3] : sp[2];
            const float za = okA ? (curA ? ac : ap) * scl : -3.0e38f, zb = okB ? (curB ? bc : bp) * scl : -3.0e38f;
            const float mx = max32(max16(fmaxf(za, zb)));
            const float ea = __builtin_amdgcn_exp2f(za - mx), eb = __builtin_amdgcn_exp2f(zb - mx);
            const float inv = __builtin_amdgcn_rcpf(sum32(sum16(ea + eb)));
            const unsigned pw = pk(ea * inv, eb * inv);
            const unsigned wa_ = (pw & 0xffffu) << (16 * r0), wb_ = (pw >> 16) << (16 * r0);
            const u32x4 pu = {curA ? 0u : wa_, curB ? 0u : wb_, curA ? wa_ : 0u, curB ? wb_ : 0u};
            const f32x4 o0 = mfma16(vf[hh][0], as_frag(pu), z), o1 = mfma16(vf[hh][1], as_frag(pu), z);
            const u32x4 ou = {pk(o0[0], o0[1]), pk(o0[2], o0[3]), pk(o1[0], o1[1]), pk(o1[2], o1[3])};
            st16<SC1>(ou, rO, odst, hh * 2048);
        }
    }
    STAMP(3);
    wait_vm<0>();
    STAMP(14);
    bar();
    STAMP(15);
    tag = rfl(tag);
    if (w == 0 && lane == 0) flag_set(ws, FL_X1 + T64 * 128 + mem * 4, tag);
    if (mem == 7) return;
    const int th = w & 1, kh = w >> 1, t32 = lane & 31, hl = lane >> 5;
    const int T32 = 2 * T64 + th;
    const unsigned colb = (64 * mem + 32 * kh) * 2;
    const unsigned rowo = (T32 * 32 + t32) * 896 + colb + hl * 8;
    u32x2 xr[4];
    {
        const rsrc_t rX = mk_rsrc(x, S_ * D_ * 2);
#pragma unroll
        for (int q = 0; q < 4; ++q) xr[q] = ld8<0>(rX, rowo, q * 16);
    }
    if (w == 0) flag_wait(ws, FL_X1 + T64 * 128, 8, tag, lane);
    bar();
    STAMP(4);
    const unsigned ird = img_rd_lane(lane);
    const rsrc_t rH = mk_rsrc(ws + H_OFF, H_BYTES), rSt = mk_rsrc(ws + ST_OFF, ST_BYTES);
    {
        constexpr int PD2 = 8;
        u32x4 of[16];
        const unsigned oso = (T32 * 32 + 16 * kh) * 1024;
        auto o_so = [&](int s) { return (unsigned)(s * 1024); };
#pragma unroll
        for (int s = 0; s < PD2; ++s) of[s] = ld16<SC1>(rO, lane * 16, oso + o_so(s));
        sb();
        f32x16 a2[2] = {zero16(), zero16()};
        const unsigned wo_hi = kh ? 114688 - 14 * 4096 : 0;
        frag_pipe<32, 4>([&](int n) { const int s = n >> 1, hp_ = s >> 1; LAS char* a = smem + (ird ^ ((n & 1) << 6)) + kh * 8 * 4096 + hp_ * 4096 + (hp_ >= 6 ? wo_hi : 0) + (s & 1) * 1024; return lds_tr(a, a + 2048); },
                         [&](int n, bf16x8 f) { const int s = n >> 1; if ((n & 1) == 0 && s + PD2 < 16) of[s + PD2] = ld16<SC1>(rO, lane * 16, oso + o_so(s + PD2)); mma32(a2[n & 1], f, as_frag(of[s])); });
        const f32x16 keep = kh ? a2[1] : a2[0], give = kh ? a2[0] : a2[1];
        LAS char* red = smem + L_RED;
#pragma unroll
        for (int q = 0; q < 4; ++q) *(LAS f32x4*)(red + (w * 4 + q) * 1024 + lane * 16) = (f32x4){give[4 * q], give[4 * q + 1], give[4 * q + 2], give[4 * q + 3]};
        bar();
        float hv[16];
        float s1 = 0.f, s2 = 0.f;
        unsigned hp[8];
#pragma unroll
        for (int q = 0; q < 4; ++q) {
            const f32x4 r = *(LAS f32x4*)(red + ((w ^ 2) * 4 + q) * 1024 + lane * 16);
            const unsigned xa = xr[q].x, xb = xr[q].y;
            hv[4 * q] = bf_lo(xa) + (keep[4 * q] + r[0]); hv[4 * q + 1] = bf_hi(xa) + (keep[4 * q + 1] + r[1]);
            hv[4 * q + 2] = bf_lo(xb) + (keep[4 * q + 2] + r[2]); hv[4 * q + 3] = bf_hi(xb) + (keep[4 * q + 3] + r[3]);
            hp[2 * q] = pk(hv[4 * q], hv[4 * q + 1]); hp[2 * q + 1] = pk(hv[4 * q + 2], hv[4 * q + 3]);
        }
        st16<SC1>((u32x4){hp[0], hp[1], hp[2], hp[3]}, rH, lane * 16, (T32 * 28 + 4 * mem + 2 * kh) * 1024);
        st16<SC1>((u32x4){hp[4], hp[5], hp[6], hp[7]}, rH, lane * 16, (T32 * 28 + 4 * mem + 2 * kh + 1) * 1024);
#pragma unroll
        for (int q = 0; q < 8; ++q) { const float lo = bf_lo(hp[q]), hi = bf_hi(hp[q]); s1 += lo + hi; s2 = fmaf(lo, lo, fmaf(hi, hi, s2)); }
        s1 = sum32(s1); s2 = sum32(s2);
        if (hl == 0) st8<SC1>((u32x2){f2u(s1), f2u(s2)}, rSt, t32 * 8, (T32 * 14 + 2 * mem + kh) * 256);
    }
    STAMP(5);
    unsigned short b1v = 0;
    if (tid < 128) { const rsrc_t rB1 = mk_rsrc(b1, F_ * 2); b1v = __builtin_amdgcn_raw_buffer_load_b16(rB1, (js * 128 + tid) * 2, 0, 0); }
    wait_vm<0>();
    STAMP(16);
    bar();
    STAMP(17);
    if (w == 0 && lane == 0) flag_set(ws, FL_X2 + PP * 128 + js * 4, tag);
    LAS float* tab = (LAS float*)(smem + L_RED);
    if (tid < 128) tab[128 + tid] = u2f((unsigned)b1v << 16);
    {
        const u32x4 onesu = {0x3F803F80u, 0x3F803F80u, 0x3F803F80u, 0x3F803F80u};
        f32x16 cs = zero16();
        LAS char* a0 = smem + (w >> 1) * 57344 + (ird ^ ((w & 1) << 6));
        frag_pipe<28, 4>([&](int s) { return lds_tr(a0 + s * 2048, a0 + s * 2048 + 1024); }, [&](int s, bf16x8 f) { mma32(cs, f, as_frag(onesu)); });
        if (t32 == 0) {
#pragma unroll
            for (int q = 0; q < 4; ++q) *(LAS f32x4*)(tab + 32 * w + 8 * q + 4 * hl) = (f32x4){cs[4 * q], cs[4 * q + 1], cs[4 * q + 2], cs[4 * q + 3]};
        }
    }
    if (w == 0) {
        flag_wait(ws, FL_X2 + PP * 128, 14, tag, lane);
        if (js == 0 && lane == 0) flag_set(ws, FL_E + PP * 128, tag);
    }
    bar();
    STAMP(6);
    const rsrc_t rG = mk_rsrc(ws + G_OFF, G_BYTES);
    const unsigned fx4 = FL_X3 + T32 * 128 + 7 * kh * 4;
    gu32* pA = (gu32*)(ws + fx4) + (lane < 7 ? lane : 0);
    unsigned fA = 0, pw3 = 0, pw4 = 0;
    const unsigned fx3 = FL_X3 + (4 * PP + w) * 128 + js * 4, fx3b = fx3 + 56;
    u32x4 gf[56];
    const unsigned gso = (T32 * 112 + 56 * kh) * 1024;
    auto g_so = [&](int s) { return (unsigned)((4 * kst(s >> 2) + (s & 3)) * 1024); };
    {
        const int TP = 4 * PP + w;
        u32x2 sv[7];
#pragma unroll
        for (int q = 0; q < 7; ++q) sv[q] = ld8<SC1>(rSt, t32 * 8 + hl * 256, (TP * 14 + 2 * q) * 256);
        constexpr int PD3 = PD3V;
        u32x4 hf[28];
        const unsigned hso = TP * 28 * 1024;
        auto h_so = [&](int s) { return (unsigned)(s * 1024); };
#pragma unroll
        for (int s = 0; s < PD3; ++s) hf[s] = ld16<SC1>(rH, lane * 16, hso + h_so(s));
        sb();
        f32x16 a3[4] = {zero16(), zero16(), zero16(), zero16()};
        LAS char* wA = smem + L_W1A; LAS char* wB = smem + L_W1B;
        frag_pipe<56, 4>([&](int n) { LAS char* a = wA + (ird ^ ((n & 1) << 6)) + (n >> 1) * 2048; return lds_tr(a, a + 1024); },
                         [&](int n, bf16x8 f) { const int s = n >> 1; if ((n & 1) == 0 && s + PD3 < 28) hf[s + PD3] = ld16<SC1>(rH, lane * 16, hso + h_so(s + PD3)); mma32(a3[n & 1], f, as_frag(hf[s])); });
        float s1 = 0.f, s2 = 0.f;
#pragma unroll
        for (int q = 0; q < 7; ++q) { const unsigned sa = sv[q].x, sb_ = sv[q].y; s1 += u2f(sa); s2 += u2f(sb_); }
        s1 = sum32(s1); s2 = sum32(s2);
        const float mu = s1 * (1.0f / D_);
        const float rstd = __builtin_amdgcn_rsqf(fmaxf(s2 * (1.0f / D_) - mu * mu, 0.0f) + 1e-5f);
        const f32x2 rs2 = {rstd, rstd}, nm2 = {-mu * rstd, -mu * rstd};
        const f32x2 c02 = {-2.0f * 0.7978845608028654f * 1.4426950408889634f, -2.0f * 0.7978845608028654f * 1.4426950408889634f};
        const f32x2 c12 = c02 * 0.044715f, one2 = {1.0f, 1.0f};
        STAMP(18);
        bar();
        f32x4 tcs_[16], tbb_[16];
        unsigned gq[8];
        LAS float* tabl = tab + 4 * hl;
        asm volatile("" : "+v"(tabl));
        auto gelu_ld = [&](int m) { const int i = m >> 2, q = m & 3; tcs_[m] = *(LAS f32x4*)(tabl + 32 * i + 8 * q); tbb_[m] = *(LAS f32x4*)(tabl + 128 + 32 * i + 8 * q); };
        f32x2 gy0[16], gy1[16], gd0[16], gd1[16];
        auto gelu_a = [&](int m) {
            const int i = m >> 2, q = m & 3;
            const f32x4 cs = tcs_[m], bb = tbb_[m];
            const f32x2 y0 = (f32x2){a3[i][4 * q], a3[i][4 * q + 1]} * rs2 + (nm2 * (f32x2){cs[0], cs[1]} + (f32x2){bb[0], bb[1]});
            const f32x2 y1 = (f32x2){a3[i][4 * q + 2], a3[i][4 * q + 3]} * rs2 + (nm2 * (f32x2){cs[2], cs[3]} + (f32x2){bb[2], bb[3]});
            gy0[m] = y0; gy1[m] = y1;
            gd0[m] = y0 * ((y0 * y0) * c12 + c02); gd1[m] = y1 * ((y1 * y1) * c12 + c02);
            asm volatile("" : "+v"(gd0[m]), "+v"(gd1[m]));
        };
        auto gelu_b = [&](int m) {
            gd0[m] = (f32x2){__builtin_amdgcn_exp2f(gd0[m][0]), __builtin_amdgcn_exp2f(gd0[m][1])} + one2;
            gd1[m] = (f32x2){__builtin_amdgcn_exp2f(gd1[m][0]), __builtin_amdgcn_exp2f(gd1[m][1])} + one2;
            asm volatile("" : "+v"(gd0[m]), "+v"(gd1[m]));
        };
        auto gelu_c = [&](int m) {
            const int i = m >> 2, q = m & 3;
            const f32x2 y0 = gy0[m] * (f32x2){__builtin_amdgcn_rcpf(gd0[m][0]), __builtin_amdgcn_rcpf(gd0[m][1])};
            const f32x2 y1 = gy1[m] * (f32x2){__builtin_amdgcn_rcpf(gd1[m][0]), __builtin_amdgcn_rcpf(gd1[m][1])};
            gq[2 * q] = pk(y0[0], y0[1]); gq[2 * q + 1] = pk(y1[0], y1[1]);
            if (q & 1) st16<SC1>((u32x4){gq[2 * q - 2], gq[2 * q - 1], gq[2 * q], gq[2 * q + 1]}, rG, lane * 16, (TP * 112 + 8 * js + 2 * i + (q >> 1)) * 1024);
            else asm volatile("" : "+v"(gq[2 * q]), "+v"(gq[2 * q + 1]));
        };
        auto gelu_do = [&](int m) { gelu_a(m); gelu_b(m); gelu_c(m); };
        frag_pipe<28, 4>([&](int s) { LAS char* a = wB + ird + s * 2048; return lds_tr(a, a + 1024); },
                         [&](int s, bf16x8 f) {
                             if (s % 3 == 0 && s / 3 < 8) gelu_ld(s / 3);
                             mma32(a3[2], f, as_frag(hf[s]));
                             if (s >= 2 && s < 26) { const int m = (s - 2) / 3, p = (s - 2) % 3; if (p == 0) gelu_a(m); else if (p == 1) gelu_b(m); else gelu_c(m); }
                         });
        frag_pipe<28, 4>([&](int s) { LAS char* a = wB + (ird ^ 64) + s * 2048; return lds_tr(a, a + 1024); },
                         [&](int s, bf16x8 f) {
                             if (s % 6 == 1 && s / 6 < 4) gelu_ld(8 + s / 6);
                             if (s >= 24) gelu_ld(12 + s - 24);
                             mma32(a3[3], f, as_frag(hf[s]));
                             if (s == AFS) { wait_vm<0>(); if (lane == 0) flag_set(ws, fx3, tag);
                             }
                             if (s == APS) fA = __hip_atomic_load(pA, __ATOMIC_RELAXED, __HIP_MEMORY_SCOPE_AGENT);
                             if (s % 6 == 3 && s / 6 < 4) gelu_a(8 + s / 6);
                             if (s % 6 == 4 && s / 6 < 4) gelu_b(8 + s / 6);
                             if (s % 6 == 5 && s / 6 < 4) gelu_c(8 + s / 6);
                         });
        bar();
        if (!__all(fA == tag)) flag_wait(ws, fx4, 7, tag, lane);
        asm volatile("" ::: "memory");
#pragma unroll
        for (int s = 0; s < 8; ++s) gf[s] = ld16<SC1>(rG, lane * 16, gso + g_so(s));
        sb();
#pragma unroll
        for (int q = 0; q < 4; ++q) gelu_do(12 + q);
        sb();
#pragma unroll
        for (int s = 8; s < PD4V; ++s) gf[s] = ld16<SC1>(rG, lane * 16, gso + g_so(s));
        STAMP(7);
    }
    STAMP(19);
    u32x4 hres[2]; u32x2 b2v[4];
    STAMP(8);
    {
        constexpr int PD = PD4V;
        gu32* pB = (gu32*)(ws + fx4 + 56) + (lane < 7 ? lane : 0);
        unsigned fB = 0;
        bar();
        sb();
        f32x16 a4[2] = {zero16(), zero16()};
        LAS char* wbp = smem + kh * 8192;
        auto run = [&](auto S0, auto NS) {
            constexpr int s0 = decltype(S0)::v, ns = decltype(NS)::v;
            frag_pipe<2 * ns, 4>(
                [&](int n) { const int s = s0 + (n >> 1); LAS char* a = wbp + (((s >> 2) + 7) % 9) * W2SLOT + (s & 3) * 2048 + (ird ^ ((n & 1) << 6)); return lds_tr(a, a + 1024); },
                [&](int n, bf16x8 f) {
                    const int s = s0 + (n >> 1);
                    if ((n & 1) == 0 && s + PD == 28 - BPD) fB = __hip_atomic_load(pB, __ATOMIC_RELAXED, __HIP_MEMORY_SCOPE_AGENT);
                    if ((n & 1) == 0 && s + PD == 28) { if (!__all(fB == tag)) flag_wait(ws, fx4 + 56, 7, tag, lane); asm volatile("" ::: "memory"); }
                    if ((n & 1) == 0 && s + PD < 56) gf[s + PD] = ld16<SC1>(rG, lane * 16, gso + g_so(s + PD));
                    if (!BPRE && (n & 1) == 0 && s == BFS) { wait_vm<PD - 8 + BFS + 1>(); if (lane == 0) flag_set(ws, fx3b, tag);
                    }
                    mma32(a4[n & 1], f, as_frag(gf[s])); });
        };
        struct I0 { enum { v = 0 }; }; struct I20 { enum { v = 20 }; }; struct I16 { enum { v = 16 }; }; struct I36 { enum { v = 36 }; }; struct I12 { enum { v = 12 }; }; struct I48 { enum { v = 48 }; }; struct I8 { enum { v = 8 }; };
        run(I0{}, I20{});
        {
            const rsrc_t rB2 = mk_rsrc(b2, D_ * 2);
            hres[0] = ld16<SC1>(rH, lane * 16, (T32 * 28 + 4 * mem + 2 * kh) * 1024);
            hres[1] = ld16<SC1>(rH, lane * 16, (T32 * 28 + 4 * mem + 2 * kh + 1) * 1024);
#pragma unroll
            for (int q = 0; q < 4; ++q) b2v[q] = ld8<0>(rB2, colb + hl * 8, q * 16);
        }
        bar();
        run(I20{}, I16{});
        bar();
        run(I36{}, I12{});
        bar();
        run(I48{}, I8{});
        STAMP(9);
        const f32x16 keep = kh ? a4[1] : a4[0], give = kh ? a4[0] : a4[1];
        LAS char* red = smem + L_RED;
#pragma unroll
        for (int q = 0; q < 4; ++q) *(LAS f32x4*)(red + (w * 4 + q) * 1024 + lane * 16) = (f32x4){give[4 * q], give[4 * q + 1], give[4 * q + 2], give[4 * q + 3]};
        bar();
        const rsrc_t rOut = mk_rsrc(out, S_ * D_ * 2);
        LAS char* ot = smem + 3 * W2SLOT;
        const int orow = 32 * th + t32;
#pragma unroll
        for (int q = 0; q < 4; ++q) {
            const f32x4 r = *(LAS f32x4*)(red + ((w ^ 2) * 4 + q) * 1024 + lane * 16);
            const unsigned ha = hres[q >> 1][2 * (q & 1)], hb = hres[q >> 1][2 * (q & 1) + 1], ba = b2v[q].x, bb = b2v[q].y;
            const u32x2 ov = {pk(bf_lo(ha) + (keep[4 * q] + r[0] + bf_lo(ba)), bf_hi(ha) + (keep[4 * q + 1] + r[1] + bf_hi(ba))),
                              pk(bf_lo(hb) + (keep[4 * q + 2] + r[2] + bf_lo(bb)), bf_hi(hb) + (keep[4 * q + 3] + r[3] + bf_hi(bb)))};
            *(LAS u32x2*)(ot + orow * 128 + (((4 * kh + q) ^ (orow & 7)) * 16) + 8 * hl) = ov;
        }
        bar();
#pragma unroll
        for (int m = 0; m < 2; ++m) {
            const int row = 16 * w + 8 * m + (lane >> 3), c = lane & 7;
            const u32x4 v = *(LAS u32x4*)(ot + row * 128 + ((c ^ (row & 7)) * 16));
            st16<OUTAUX>(v, rOut, (t0 + row) * 896 + mem * 128 + c * 16, 0);
        }
    }
    STAMP(10);
}

extern "C" void kernel_launch(void* const* d_in, const int* in_sizes, int n_in, void* d_out, int out_size, void* d_ws, size_t ws_size, hipStream_t stream) {
    (void)in_sizes; (void)n_in; (void)out_size; (void)ws_size;
    (void)hipFuncSetAttribute((const void*)k_fused, hipFuncAttributeMaxDynamicSharedMemorySize, LDS_BYTES);
    k_fused<<<256, 512, LDS_BYTES, stream>>>((const char*)d_in[0], (const char*)d_in[1], (const char*)d_in[2], (const char*)d_in[4], (const char*)d_in[5],
                                            (const char*)d_in[6], (const char*)d_in[7], (char*)d_out, (char*)d_ws);
}
```

```cpp
#include <hip/hip_runtime.h>
#include <stdint.h>

typedef __bf16 bf16x8 __attribute__((ext_vector_type(8)));
typedef __bf16 bf16x4 __attribute__((ext_vector_type(4)));
typedef __bf16 bf16x2 __attribute__((ext_vector_type(2)));
typedef float f32x16 __attribute__((ext_vector_type(16)));
typedef float f32x4 __attribute__((ext_vector_type(4)));
typedef float f32x2 __attribute__((ext_vector_type(2)));
typedef unsigned u32x4 __attribute__((ext_vector_type(4)));
typedef unsigned u32x2 __attribute__((ext_vector_type(2)));
#define LAS __attribute__((address_space(3)))
#define GAS __attribute__((address_space(1)))
#define DI __device__ __forceinline__
typedef __amdgpu_buffer_rsrc_t rsrc_t;
typedef GAS unsigned gu32;

constexpr int S_ = 2048, D_ = 448, F_ = 1792;
constexpr unsigned FL_X1 = 0, FL_X2 = 4096, FL_E = 8192, FL_X3 = 16384;
constexpr unsigned ST_OFF = 512u << 10, O_OFF = 1u << 20, H_OFF = 4u << 20, G_OFF = 6u << 20;
constexpr unsigned ST_BYTES = 64u * 14 * 256, O_BYTES = 64u * 32 * 1024, H_BYTES = 64u * 28 * 1024, G_BYTES = 64u * 112 * 1024;
constexpr int LDS_BYTES = 163840;
constexpr int SC1 = 16;
constexpr unsigned SPIN_CAP = 1u << 18;

DI int rfl(int v) { return __builtin_amdgcn_readfirstlane(v); }
DI rsrc_t mk_rsrc(const void* p, unsigned bytes) { return __builtin_amdgcn_make_buffer_rsrc((void*)p, (short)0, (int)bytes, 0x00020000); }
DI void dma16(unsigned ldsaddr, unsigned voff, rsrc_t srd, unsigned soff) {
    asm volatile("s_mov_b32 m0, %0\n\ts_nop 0\n\tbuffer_load_dwordx4 %1, %2, %3 offen lds" ::"s"(ldsaddr), "v"(voff), "s"(srd), "s"(soff) : "memory");
}
DI void dma16_masked(unsigned ldsaddr, unsigned voff, rsrc_t srd, unsigned soff, unsigned long long off_mask) {
    unsigned long long keep;
    asm volatile("s_mov_b64 %0, exec\n\ts_andn2_b64 exec, exec, %5\n\ts_mov_b32 m0, %1\n\ts_nop 0\n\tbuffer_load_dwordx4 %2, %3, %4 offen lds\n\ts_mov_b64 exec, %0"
                 : "=&s"(keep) : "s"(ldsaddr), "v"(voff), "s"(srd), "s"(soff), "s"(off_mask) : "memory", "scc");
}
template <int N> DI void wait_vm() { asm volatile("s_waitcnt vmcnt(%0)" ::"i"(N) : "memory"); }
DI void bar() { asm volatile("s_waitcnt lgkmcnt(0)" ::: "memory"); __builtin_amdgcn_s_barrier(); asm volatile("" ::: "memory"); }
DI void sb() { __builtin_amdgcn_sched_barrier(0); }
template <int AUX> DI u32x4 ld16(rsrc_t r, unsigned voff, unsigned soff) { return __builtin_amdgcn_raw_buffer_load_b128(r, voff, soff, AUX); }
template <int AUX> DI u32x2 ld8(rsrc_t r, unsigned voff, unsigned soff) { return __builtin_amdgcn_raw_buffer_load_b64(r, voff, soff, AUX); }
template <int AUX> DI void st16(u32x4 v, rsrc_t r, unsigned voff, unsigned soff) { __builtin_amdgcn_raw_buffer_store_b128(v, r, voff + soff, 0, AUX); }
template <int AUX> DI void st8(u32x2 v, rsrc_t r, unsigned voff, unsigned soff) { __builtin_amdgcn_raw_buffer_store_b64(v, r, voff, soff, AUX); }
DI bf16x8 as_frag(u32x4 v) { return __builtin_bit_cast(bf16x8, v); }
DI bf16x8 lds_frag(LAS char* p) { return *(LAS bf16x8*)p; }
DI bf16x8 lds_tr(LAS char* p0, LAS char* p1) {
    bf16x4 a = __builtin_amdgcn_ds_read_tr16_b64_v4bf16((LAS bf16x4*)p0);
    bf16x4 b = __builtin_amdgcn_ds_read_tr16_b64_v4bf16((LAS bf16x4*)p1);
    return __builtin_shufflevector(a, b, 0, 1, 2, 3, 4, 5, 6, 7);
}
DI f32x4 mfma16(bf16x8 a, bf16x8 b, f32x4 c) { return __builtin_amdgcn_mfma_f32_16x16x32_bf16(a, b, c, 0, 0, 0); }
DI f32x16 mfma32(bf16x8 a, bf16x8 b, f32x16 c) { return __builtin_amdgcn_mfma_f32_32x32x16_bf16(a, b, c, 0, 0, 0); }
DI void mma16(f32x4& c, bf16x8 a, bf16x8 b) { c = mfma16(a, b, c); asm volatile("" ::"v"(c)); }
DI void mma32(f32x16& c, bf16x8 a, bf16x8 b) { c = mfma32(a, b, c); asm volatile("" ::"v"(c)); }
DI unsigned pk(float a, float b) { f32x2 v = {a, b}; bf16x2 r = __builtin_convertvector(v, bf16x2); return __builtin_bit_cast(unsigned, r); }
DI float bf_lo(unsigned v) { return __builtin_bit_cast(float, v << 16); }
DI float bf_hi(unsigned v) { return __builtin_bit_cast(float, v & 0xffff0000u); }
DI unsigned f2u(float v) { return __builtin_bit_cast(unsigned, v); }
DI float u2f(unsigned v) { return __builtin_bit_cast(float, v); }
DI float sum16(float v) { auto a = __builtin_amdgcn_permlane16_swap(f2u(v), f2u(v), false, false); unsigned a0 = a[0], a1 = a[1]; return u2f(a0) + u2f(a1); }
DI float sum32(float v) { auto a = __builtin_amdgcn_permlane32_swap(f2u(v), f2u(v), false, false); unsigned a0 = a[0], a1 = a[1]; return u2f(a0) + u2f(a1); }
DI float max16(float v) { auto a = __builtin_amdgcn_permlane16_swap(f2u(v), f2u(v), false, false); unsigned a0 = a[0], a1 = a[1]; return fmaxf(u2f(a0), u2f(a1)); }
DI float max32(float v) { auto a = __builtin_amdgcn_permlane32_swap(f2u(v), f2u(v), false, false); unsigned a0 = a[0], a1 = a[1]; return fmaxf(u2f(a0), u2f(a1)); }
DI f32x16 zero16() { f32x16 z; for (int i = 0; i < 16; ++i) z[i] = 0.f; return z; }

template <int N, int PD, class LD, class MM> DI void frag_pipe(LD ld, MM mm) {
    bf16x8 buf[N];
#pragma unroll
    for (int i = 0; i < PD; ++i) buf[i] = ld(i);
    sb();
#pragma unroll
    for (int i = 0; i < N; ++i) {
        if (i + PD < N) buf[i + PD] = ld(i + PD);
        mm(i, buf[i]);
        sb();
    }
}

DI void flag_set(char* ws, unsigned off, unsigned tag) { __hip_atomic_store((gu32*)(ws + off), tag, __ATOMIC_RELAXED, __HIP_MEMORY_SCOPE_AGENT); }
DI void flag_wait(char* ws, unsigned off, int n, unsigned tag, int lane) {
    gu32* p = (gu32*)(ws + off) + (lane < n ? lane : 0);
    unsigned v0 = __hip_atomic_load(p, __ATOMIC_RELAXED, __HIP_MEMORY_SCOPE_AGENT);
    for (unsigned spin = 0; spin < SPIN_CAP; ++spin) {
        __builtin_amdgcn_s_sleep(5);
        const unsigned v1 = __hip_atomic_load(p, __ATOMIC_RELAXED, __HIP_MEMORY_SCOPE_AGENT);
        if (__all(v0 == tag)) break;
        __builtin_amdgcn_s_sleep(5);
        v0 = __hip_atomic_load(p, __ATOMIC_RELAXED, __HIP_MEMORY_SCOPE_AGENT);
        if (__all(v1 == tag)) break;
    }
    asm volatile("" ::: "memory");
}

DI unsigned img_src_lane(int lane) {
    const int i8 = lane >> 3, p8 = lane & 7;
    return (4 * ((p8 >> 2) ^ ((i8 >> 1) & 1)) + (p8 & 3)) * 16;
}
DI unsigned img_rd_lane(int lane) {
    const int G = lane >> 4, nh = G & 1, hh = G >> 1, q = (lane & 15) >> 2, p = lane & 3;
    return (4 * hh + q) * 128 + (q >> 1) * 64 + nh * 32 + 8 * p;
}

constexpr int SL1 = 32768, SL1W = 10240, L_TAB1 = 161792;
constexpr int L_STG = 131072;
DI constexpr int kst(int st) { return st < 7 ? 2 * st : 2 * (st - 7) + 1; }
DI constexpr int wo_pos(int m) { return m < 14 ? m * 4096 : 114688 + (m - 14) * 4096; }
constexpr int L_W1A = 0, L_W1B = 57344, L_RED = 147456;
constexpr int W2SLOT = 16384;

__global__ __launch_bounds__(512, 2) void k_fused(const char* __restrict__ x, const char* __restrict__ wqkv, const char* __restrict__ wo,
                                                  const char* __restrict__ w1, const char* __restrict__ b1, const char* __restrict__ w2,
                                                  const char* __restrict__ b2, char* __restrict__ out, char* __restrict__ ws) {
    extern __shared__ __attribute__((aligned(16))) char smem_[];
    LAS char* smem = (LAS char*)smem_;
    const unsigned lbase = (unsigned)(size_t)smem;
    const int tid = threadIdx.x, lane = tid & 63, w = rfl(tid >> 6);
    const int b = blockIdx.x, slot = b >> 3, T64 = (b & 7) * 4 + (slot >> 3), mem = slot & 7;
    const int PP = T64 >> 1, js = 7 * (T64 & 1) + mem;
    const int t0 = T64 * 64;
    const bool loader = w >= 4;

    if (__builtin_expect(loader, 1)) {
        __builtin_amdgcn_s_setprio(0);
        const int lw = w - 4, i8 = lane >> 3;
        const rsrc_t rX = mk_rsrc(x, S_ * D_ * 2), rW = mk_rsrc(wqkv, D_ * 3 * D_ * 2);
        unsigned xv[3], xl[3];
#pragma unroll
        for (int m = 0; m < 3; ++m) {
            int rb = lw + 4 * m; rb = rb > 9 ? 9 : rb;
            const int rho = 8 * rb + i8;
            int tok = t0 - 16 + rho; tok = tok < 0 ? 0 : tok;
            xv[m] = tok * 896 + ((lane & 7) ^ ((rho >> 1) & 7)) * 16;
            xl[m] = lbase + rb * 1024;
        }
        const unsigned long long x0off = lw == 0 ? 0xFFull : 0ull;
        unsigned wv[6], wu[6]; unsigned long long woff[6];
#pragma unroll
        for (int m = 0; m < 6; ++m) {
            int u = lw + 4 * m; u = u > 21 ? 21 : u;
            const int c = 64 * u + lane, rs = c / 22, cc = c - rs * 22;
            const int q = rs & 31, bb = q >> 3, ii = q & 7;
            const int krow = (rs & 32) + (bb & 1) * 16 + (bb >> 1) * 4 + (ii & 3) + 8 * (ii >> 2);
            const int sec = cc / 7;
            wv[m] = krow * 2688 + (sec == 2 ? 0 : (sec + 1) * 896) + mem * 112 + (cc - sec * 7) * 16;
            wu[m] = lbase + SL1W + u * 1024;
            woff[m] = __ballot(cc == 21);
        }
        const bool lo2 = lw < 2;
        auto stage1 = [&](int st) {
            const unsigned sl = ((st + 1) & 3) * SL1;
            dma16_masked(xl[0] + sl, xv[0], rX, st * 128, x0off);
            dma16(xl[1] + sl, xv[1], rX, st * 128);
            if (lo2) dma16(xl[2] + sl, xv[2], rX, st * 128);
#pragma unroll
            for (int m = 0; m < 5; ++m) dma16_masked(wu[m] + sl, wv[m], rW, st * 64 * 2688, woff[m]);
            if (lo2) dma16_masked(wu[5] + sl, wv[5], rW, st * 64 * 2688, woff[5]);
        };
        stage1(0); stage1(1); stage1(2);
        for (int st = 0; st < 7; ++st) {
            if (st >= 5) wait_vm<0>(); else if (lo2) wait_vm<9>(); else wait_vm<7>();
            bar();
            if (st <= 3) stage1(st + 3);
        }
        if (mem == 7) {
            bar(); bar(); bar();
            const int pi = (T64 & 3) * 4 + lw;
            unsigned acc = 0;
#pragma unroll 1
            for (int mtx = 0; mtx < 2; ++mtx) {
                const rsrc_t r = mk_rsrc(mtx ? w2 : w1, D_ * F_ * 2);
                unsigned v[13];
#pragma unroll
                for (int m = 0; m < 13; ++m) { const int l = (m * 16 + pi) * 64 + lane; v[m] = __builtin_amdgcn_raw_buffer_load_b32(r, l < 12544 ? l * 128 : 0x80000000u, 0, 0); }
#pragma unroll
                for (int m = 0; m < 13; ++m) acc ^= v[m];
            }
            asm volatile("" ::"v"(acc));
            return;
        }
        const unsigned isl = img_src_lane(lane);
        const rsrc_t rWo = mk_rsrc(wo, D_ * D_ * 2);
        const unsigned vo = (8 * lw + i8) * 896 + mem * 128 + isl;
        auto wo_unit = [&](int m) { dma16(lbase + wo_pos(m) + lw * 1024, vo, rWo, m * 28 * 896); };
#pragma unroll
        for (int m = 0; m < 10; ++m) wo_unit(m);
        bar();
#pragma unroll
        for (int m = 10; m < 16; ++m) wo_unit(m);
        const rsrc_t rW1 = mk_rsrc(w1, D_ * F_ * 2);
        const unsigned v1o = i8 * 3584 + js * 256 + isl;
        auto w1_so = [&](int m) { return (unsigned)((lw + 4 * m) * 8 * 3584); };
#pragma unroll
        for (int m = 0; m < 14; ++m) dma16(lbase + L_W1B + (lw + 4 * m) * 1024, v1o + 128, rW1, w1_so(m));
        bar();
        u32x4 w1r[14];
#pragma unroll
        for (int m = 0; m < 14; ++m) w1r[m] = ld16<0>(rW1, v1o, w1_so(m));
        bar();
        const rsrc_t rW2 = mk_rsrc(w2, F_ * D_ * 2);
        const unsigned v2o = (8 * lw + i8) * 896 + mem * 128 + isl;
        auto w2_so = [&](int st, int m) { return (unsigned)(((m >> 1) * 896 + 64 * kst(st) + 32 * (m & 1)) * 896); };
        wait_vm<28>();
        bar();
        wait_vm<0>();
        bar();
#pragma unroll
        for (int m = 0; m < 14; ++m) *(LAS u32x4*)(smem + L_W1A + (lw + 4 * m) * 1024 + lane * 16) = w1r[m];
        bar();
        auto w2_stage = [&](int st) {
#pragma unroll
            for (int m = 0; m < 4; ++m) dma16(lbase + ((st + 7) % 9) * W2SLOT + (m >> 1) * 8192 + ((m & 1) * 4 + lw) * 1024, v2o, rW2, w2_so(st, m));
        };
        w2_stage(0); w2_stage(1);
        u32x4 w5[16];
#pragma unroll
        for (int m = 0; m < 16; ++m) { w5[m] = ld16<0>(rW2, v2o, w2_so(5 + (m >> 2), m & 3)); sb(); __builtin_amdgcn_s_sleep(1); sb(); }
        bar();
        bar();
        w2_stage(2); w2_stage(3); w2_stage(4);
        u32x4 wr[20];
#pragma unroll
        for (int m = 0; m < 20; ++m) wr[m] = ld16<0>(rW2, v2o, w2_so(9 + (m >> 2), m & 3));
        bar();
#pragma unroll
        for (int m = 0; m < 16; ++m)
            *(LAS u32x4*)(smem + ((5 + (m >> 2) + 7) % 9) * W2SLOT + ((m & 3) >> 1) * 8192 + ((m & 1) * 4 + lw) * 1024 + lane * 16) = w5[m];
        wait_vm<0>();
        bar();
        bar();
        auto refill = [&](int m) {
            *(LAS u32x4*)(smem + ((9 + (m >> 2) + 7) % 9) * W2SLOT + ((m & 3) >> 1) * 8192 + ((m & 1) * 4 + lw) * 1024 + lane * 16) = wr[m];
            __builtin_amdgcn_s_sleep(1);
        };
#pragma unroll
        for (int m = 0; m < 12; ++m) refill(m);
        bar();
#pragma unroll
        for (int m = 12; m < 20; ++m) refill(m);
        bar();
        bar();
        return;
    }

    __builtin_amdgcn_s_setprio(1);
    unsigned tag = 0;
    {
        const u32x4 ones = {0x3F803F80u, 0x3F803F80u, 0x3F803F80u, 0x3F803F80u};
        if (lane < 8) *(LAS u32x4*)(smem + w * SL1 + lane * 16) = ones;
        const u32x4 e0 = {0x00003F80u, 0u, 0u, 0u};
        *(LAS u32x4*)(smem + w * SL1 + SL1W + lane * 352 + 336) = e0;
        tag = __hip_atomic_load((gu32*)(ws + FL_E + PP * 128), __ATOMIC_RELAXED, __HIP_MEMORY_SCOPE_AGENT) + 1u;
    }
    unsigned tch = 0;
    auto touch = [&]() {
        const char* tp = w == 0 ? wqkv : (w == 1 ? w1 : (w == 2 ? w2 : wo));
        const unsigned per = w == 0 ? 4704u : (w == 3 ? 1568u : 6272u);
        const rsrc_t rT = mk_rsrc(tp, per * 256);
        unsigned o = b * per + lane * 128; o = lane * 128 < per ? o : 0x80000000u;
        tch = __builtin_amdgcn_raw_buffer_load_b32(rT, o, 0, 0);
    };
    if (w == 0) touch();
    if (w == 2) {
        const rsrc_t rWs = mk_rsrc(ws, 16u << 20);
        unsigned o = 0x80000000u;
        if (lane < 2) o = O_OFF + ((2 * T64 + lane) * 32 + 4 * mem) * 1024;
        else if (mem < 7) {
            if (lane < 4) o = H_OFF + ((2 * T64 + lane - 2) * 28 + 4 * mem) * 1024;
            else if (lane < 6) o = ST_OFF + ((2 * T64 + lane - 4) * 14 + 2 * mem) * 256;
            else if (lane < 10) o = G_OFF + ((4 * PP + lane - 6) * 112 + 8 * js) * 1024;
        }
        __builtin_amdgcn_raw_buffer_store_b32(0u, rWs, o, 0, SC1);
    }
    if (w == 3 && mem < 7) {
        const rsrc_t rOutW = mk_rsrc(out, S_ * D_ * 2);
        __builtin_amdgcn_raw_buffer_store_b32(0u, rOutW, lane < 4 ? (t0 + 16 * lane) * 896 + mem * 128 : 0x80000000u, 0, SC1);
    }
    unsigned tb = 0;
    if (w == 1) tb = __builtin_amdgcn_raw_buffer_load_b32(mk_rsrc(b1, F_ * 2), 0, 0, 0) ^ __builtin_amdgcn_raw_buffer_load_b32(mk_rsrc(b2, D_ * 2), 0, 0, 0);
    const int t = lane & 15, g = lane >> 4;
#pragma unroll
    for (int m = 0; m < 2; ++m) {
        const int it = tid + 256 * m, blk = it >> 4, tt = it & 15;
        if (it < 480) {
            const int sv_ = ((tt >> 1) & 1) | (((tt >> 3) & 1) << 1) | (((tt >> 2) & 1) << 2);
            const int off = (blk >= 10 && blk < 20) ? tt * 64 + ((7 ^ sv_) * 8) : 768 + tt * 16 + 8;
            *(LAS u32x2*)(smem + L_STG + blk * 1024 + off) = (u32x2){0u, 0u};
        }
    }
    LAS char* edst[3]; bool eok[3];
    {
        const int svt = ((t >> 1) & 1) | (((t >> 3) & 1) << 1) | (((t >> 2) & 1) << 2);
#pragma unroll
        for (int i = 0; i < 3; ++i) {
            const int n0 = (w * 3 + i) * 16 + 4 * g;
            const int arr = (n0 >= 56 ? 1 : 0) + (n0 >= 112 ? 1 : 0), rem = n0 - arr * 56, hh = rem >= 28 ? 1 : 0, dim = rem - 28 * hh;
            const int off = (arr == 1) ? t * 64 + (((dim >> 2) ^ svt) * 8) : (dim >> 3) * 256 + t * 16 + ((dim >> 2) & 1) * 8;
            edst[i] = smem + (n0 < 168 ? L_STG + (arr * 2 + hh) * 5 * 1024 + off : L_STG);
            eok[i] = n0 < 168;
            asm volatile("" : "+v"(edst[i]));
        }
    }
    const int uu = t - 4 * g, r0 = t & 1;
    const bool curA = uu - r0 >= 0, curB = uu - r0 - 2 >= 0;
    const bool noprev = (T64 == 0 && w == 0);
    const bool okA = curA || !noprev, okB = curB || !noprev;
    LAS char* aqk = smem + (L_STG + w * 1024 + lane * 16);
    LAS char* avv[2];
    unsigned odst;
    {
        const int vrow = 4 * g + ((lane & 15) >> 2);
        const int svr = ((vrow >> 1) & 1) | (((vrow >> 3) & 1) << 1) | (((vrow >> 2) & 1) << 2);
        avv[0] = smem + (L_STG + 10240 + w * 1024 + vrow * 64 + (((lane & 3) ^ svr) * 8)); avv[1] = smem + (L_STG + 10240 + w * 1024 + vrow * 64 + (((4 + (lane & 3)) ^ svr) * 8));
        const int T16 = 4 * T64 + w;
        odst = ((T16 >> 1) * 32 + (g >> 1) + 4 * mem) * 1024 + (32 * (g & 1) + 16 * (T16 & 1) + t) * 16;
        asm volatile("" : "+v"(aqk), "+v"(avv[0]), "+v"(avv[1]), "+v"(odst));
    }
    f32x4 acc1[5][3];
#pragma unroll
    for (int a = 0; a < 5; ++a)
#pragma unroll
        for (int i = 0; i < 3; ++i) acc1[a][i] = (f32x4){0.f, 0.f, 0.f, 0.f};
    {
        const int sg = (t >> 1) & 7;
        LAS char* xa[2]; LAS char* wa[2];
        xa[0] = smem + (SL1 + (t >> 3) * 1024 + (t & 7) * 128 + ((g ^ sg) * 16)); xa[1] = smem + (SL1 + (t >> 3) * 1024 + (t & 7) * 128 + (((4 + g) ^ sg) * 16));
        wa[0] = smem + (SL1 + SL1W + (8 * (g >> 1) + 4 * (g & 1) + ((lane & 15) >> 2)) * 352 + 8 * (lane & 3) + w * 96); wa[1] = wa[0] + 11264;
        auto trd = [&](LAS char* p) { return __builtin_amdgcn_ds_read_tr16_b64_v4bf16((LAS bf16x4*)p); };
#pragma unroll
        for (int a = 0; a < 5; ++a)
#pragma unroll
            for (int i = 0; i < 3; ++i) asm volatile("" : "+v"(acc1[a][i]));
        auto gemm1 = [&](auto W3) {
            constexpr bool w3 = decltype(W3)::v;
            bf16x4 wl[2][3], wh[2][3]; bf16x8 xf[2][5];
            auto rd = [&](int ks, int j) {
                if (j == 0) wl[ks][0] = trd(wa[ks]);
                else if (j == 1) wh[ks][0] = trd(wa[ks] + 5632);
                else if (j <= 6) xf[ks][j - 2] = lds_frag(xa[ks] + (j - 2) * 2048);
                else if (w3 && j >= 9) { }
                else if (j & 1) wl[ks][(j - 5) >> 1] = trd(wa[ks] + ((j - 5) >> 1) * 32);
                else wh[ks][(j - 6) >> 1] = trd(wa[ks] + 5632 + ((j - 6) >> 1) * 32);
            };
            bar();
            if (w != 0) touch();
#pragma unroll
            for (int j = 0; j < 11; ++j) rd(0, j);
            xa[0] += SL1; wa[0] += SL1;
            sb();
#pragma unroll 1
            for (int st = 0; st < 7; ++st) {
                const int dd[2] = {((st + 3) & 3) == 0 ? -3 * SL1 : SL1, ((st + 2) & 3) == 0 ? -3 * SL1 : SL1};
#pragma unroll
                for (int ks = 0; ks < 2; ++ks) {
#pragma unroll
                    for (int n = 0; n < 15; ++n) {
                        const int i = n / 5, a = n % 5;
                        if (w3 && i == 2) mma16(acc1[a][2], xf[ks][a], xf[ks][a]);
                        else mma16(acc1[a][i], __builtin_shufflevector(wl[ks][i], wh[ks][i], 0, 1, 2, 3, 4, 5, 6, 7), xf[ks][a]);
                        if (n >= 1 && n < 12) rd(ks ^ 1, n - 1);
                        if (n == 13) { xa[ks ^ 1] += dd[ks ^ 1]; wa[ks ^ 1] += dd[ks ^ 1]; asm volatile("" : "+v"(xa[ks ^ 1]), "+v"(wa[ks ^ 1])); }
                        sb();
                    }
                }
                if (st < 6) { asm volatile("" ::: "memory"); __builtin_amdgcn_s_barrier(); asm volatile("" ::: "memory"); }
            }
        };
        struct BT { enum { v = 1 }; }; struct BF { enum { v = 0 }; };
        if (w == 3) gemm1(BT{}); else gemm1(BF{});
    }
    LAS float* tst = (LAS float*)(smem + L_TAB1);
    if (w == 3) {
        const int r = t & 3;
        float d[5];
#pragma unroll
        for (int a = 0; a < 5; ++a) {
            const float da = (r & 1) ? acc1[a][2][1] : acc1[a][2][0], db = (r & 1) ? acc1[a][2][3] : acc1[a][2][2];
            d[a] = (r & 2) ? db : da;
        }
        LAS float* tp = tst + 2 * t;
        if (g == 2) {
#pragma unroll
            for (int a = 0; a < 5; ++a) tp[32 * a] = acc1[a][1][0];
        }
        if (g == (t >> 2)) {
#pragma unroll
            for (int a = 0; a < 5; ++a) tp[32 * a + 1] = d[a];
        }
    }
    asm volatile("" ::"v"(tch), "v"(tb));
    wait_vm<0>();
    bar();
    {
        f32x2 nmu[5], rs[5];
#pragma unroll
        for (int a = 0; a < 5; ++a) {
            const f32x2 s = *(LAS f32x2*)(tst + (16 * a + t) * 2);
            const float mu = s[0] * (1.0f / D_);
            const float r = __builtin_amdgcn_rsqf(fmaxf(s[1] * (1.0f / D_) - mu * mu, 0.0f) + 1e-5f);
            nmu[a] = (f32x2){-mu, -mu}; rs[a] = (f32x2){r, r};
        }
#pragma unroll
        for (int i = 0; i < 3; ++i) {
            f32x4 cs;
#pragma unroll
            for (int r = 0; r < 4; ++r) { const float c = acc1[0][i][r]; cs[r] = u2f(__builtin_amdgcn_update_dpp(0, (int)f2u(c), 0x150, 0xf, 0xf, true)); }
            u32x2 pv[5];
#pragma unroll
            for (int a = 0; a < 5; ++a) {
                const f32x2 lo = {acc1[a][i][0], acc1[a][i][1]}, hi = {acc1[a][i][2], acc1[a][i][3]};
                const f32x2 v01 = (nmu[a] * (f32x2){cs[0], cs[1]} + lo) * rs[a], v23 = (nmu[a] * (f32x2){cs[2], cs[3]} + hi) * rs[a];
                pv[a] = (u32x2){pk(v01[0], v01[1]), pk(v23[0], v23[1])};
            }
            if (eok[i]) {
#pragma unroll
                for (int a = 0; a < 5; ++a) *(LAS u32x2*)(edst[i] + a * 1024) = pv[a];
            }
        }
    }
    bar();
    const rsrc_t rO = mk_rsrc(ws + O_OFF, O_BYTES);
    {
        const float scl = 0.18898223650461362f * 1.4426950408889634f;
        const f32x4 z = {0.f, 0.f, 0.f, 0.f};
        bf16x8 qf[2], kp[2], kc[2], vf[2][2];
#pragma unroll
        for (int hh = 0; hh < 2; ++hh) {
            qf[hh] = lds_frag(aqk + 20480 + 1024 + hh * 5120); kp[hh] = lds_frag(aqk + hh * 5120); kc[hh] = lds_frag(aqk + 1024 + hh * 5120);
        }
#pragma unroll
        for (int hh = 0; hh < 2; ++hh)
#pragma unroll
            for (int dt = 0; dt < 2; ++dt) vf[hh][dt] = lds_tr(avv[dt] + hh * 5120, avv[dt] + hh * 5120 + 1024);
#pragma unroll
        for (int hh = 0; hh < 2; ++hh) {
            const f32x4 sp = mfma16(kp[hh], qf[hh], z), sc = mfma16(kc[hh], qf[hh], z);
            const float ac = r0 ? sc[1] : sc[0], ap = r0 ? sp[1] : sp[0], bc = r0 ? sc[3] : sc[2], bp = r0 ? sp[3] : sp[2];
            const float za = okA ? (curA ? ac : ap) * scl : -3.0e38f, zb = okB ? (curB ? bc : bp) * scl : -3.0e38f;
            const float mx = max32(max16(fmaxf(za, zb)));
            const float ea = __builtin_amdgcn_exp2f(za - mx), eb = __builtin_amdgcn_exp2f(zb - mx);
            const float inv = __builtin_amdgcn_rcpf(sum32(sum16(ea + eb)));
            const unsigned pw = pk(ea * inv, eb * inv);
            const unsigned wa_ = (pw & 0xffffu) << (16 * r0), wb_ = (pw >> 16) << (16 * r0);
            const u32x4 pu = {curA ? 0u : wa_, curB ? 0u : wb_, curA ? wa_ : 0u, curB ? wb_ : 0u};
            const f32x4 o0 = mfma16(vf[hh][0], as_frag(pu), z), o1 = mfma16(vf[hh][1], as_frag(pu), z);
            const u32x4 ou = {pk(o0[0], o0[1]), pk(o0[2], o0[3]), pk(o1[0], o1[1]), pk(o1[2], o1[3])};
            st16<SC1>(ou, rO, odst, hh * 2048);
        }
    }
    wait_vm<0>();
    bar();
    tag = rfl(tag);
    if (w == 0 && lane == 0) flag_set(ws, FL_X1 + T64 * 128 + mem * 4, tag);
    if (mem == 7) return;
    const int th = w & 1, kh = w >> 1, t32 = lane & 31, hl = lane >> 5;
    const int T32 = 2 * T64 + th;
    const unsigned colb = (64 * mem + 32 * kh) * 2;
    const unsigned rowo = (T32 * 32 + t32) * 896 + colb + hl * 8;
    u32x2 xr[4];
    {
        const rsrc_t rX = mk_rsrc(x, S_ * D_ * 2);
#pragma unroll
        for (int q = 0; q < 4; ++q) xr[q] = ld8<0>(rX, rowo, q * 16);
    }
    if (w == 0) flag_wait(ws, FL_X1 + T64 * 128, 8, tag, lane);
    bar();
    const unsigned ird = img_rd_lane(lane);
    const rsrc_t rH = mk_rsrc(ws + H_OFF, H_BYTES), rSt = mk_rsrc(ws + ST_OFF, ST_BYTES);
    {
        constexpr int PD2 = 10;
        u32x4 of[16];
        const unsigned oso = (T32 * 32 + 16 * kh) * 1024;
#pragma unroll
        for (int s = 0; s < PD2; ++s) of[s] = ld16<SC1>(rO, lane * 16, oso + s * 1024);
        sb();
        f32x16 a2[2] = {zero16(), zero16()};
        const unsigned wo_hi = kh ? 114688 - 14 * 4096 : 0;
        frag_pipe<32, 4>([&](int n) { const int s = n >> 1, hp_ = s >> 1; LAS char* a = smem + (ird ^ ((n & 1) << 6)) + kh * 8 * 4096 + hp_ * 4096 + (hp_ >= 6 ? wo_hi : 0) + (s & 1) * 1024; return lds_tr(a, a + 2048); },
                         [&](int n, bf16x8 f) { const int s = n >> 1; if ((n & 1) == 0 && s + PD2 < 16) of[s + PD2] = ld16<SC1>(rO, lane * 16, oso + (s + PD2) * 1024); mma32(a2[n & 1], f, as_frag(of[s])); });
        const f32x16 keep = kh ? a2[1] : a2[0], give = kh ? a2[0] : a2[1];
        LAS char* red = smem + L_RED;
#pragma unroll
        for (int q = 0; q < 4; ++q) *(LAS f32x4*)(red + (w * 4 + q) * 1024 + lane * 16) = (f32x4){give[4 * q], give[4 * q + 1], give[4 * q + 2], give[4 * q + 3]};
        bar();
        float hv[16], s1 = 0.f, s2 = 0.f;
        unsigned hp[8];
#pragma unroll
        for (int q = 0; q < 4; ++q) {
            const f32x4 r = *(LAS f32x4*)(red + ((w ^ 2) * 4 + q) * 1024 + lane * 16);
            const unsigned xa = xr[q].x, xb = xr[q].y;
            hv[4 * q] = bf_lo(xa) + (keep[4 * q] + r[0]); hv[4 * q + 1] = bf_hi(xa) + (keep[4 * q + 1] + r[1]);
            hv[4 * q + 2] = bf_lo(xb) + (keep[4 * q + 2] + r[2]); hv[4 * q + 3] = bf_hi(xb) + (keep[4 * q + 3] + r[3]);
            hp[2 * q] = pk(hv[4 * q], hv[4 * q + 1]); hp[2 * q + 1] = pk(hv[4 * q + 2], hv[4 * q + 3]);
        }
        st16<SC1>((u32x4){hp[0], hp[1], hp[2], hp[3]}, rH, lane * 16, (T32 * 28 + 4 * mem + 2 * kh) * 1024);
        st16<SC1>((u32x4){hp[4], hp[5], hp[6], hp[7]}, rH, lane * 16, (T32 * 28 + 4 * mem + 2 * kh + 1) * 1024);
#pragma unroll
        for (int q = 0; q < 8; ++q) { const float lo = bf_lo(hp[q]), hi = bf_hi(hp[q]); s1 += lo + hi; s2 = fmaf(lo, lo, fmaf(hi, hi, s2)); }
        s1 = sum32(s1); s2 = sum32(s2);
        if (hl == 0) st8<SC1>((u32x2){f2u(s1), f2u(s2)}, rSt, t32 * 8, (T32 * 14 + 2 * mem + kh) * 256);
    }
    unsigned short b1v = 0;
    if (tid < 128) { const rsrc_t rB1 = mk_rsrc(b1, F_ * 2); b1v = __builtin_amdgcn_raw_buffer_load_b16(rB1, (js * 128 + tid) * 2, 0, 0); }
    wait_vm<0>();
    bar();
    if (w == 0 && lane == 0) flag_set(ws, FL_X2 + PP * 128 + js * 4, tag);
    LAS float* tab = (LAS float*)(smem + L_RED);
    if (tid < 128) tab[128 + tid] = u2f((unsigned)b1v << 16);
    {
        const u32x4 onesu = {0x3F803F80u, 0x3F803F80u, 0x3F803F80u, 0x3F803F80u};
        f32x16 cs = zero16();
        LAS char* a0 = smem + (w >> 1) * 57344 + (ird ^ ((w & 1) << 6));
        frag_pipe<28, 4>([&](int s) { return lds_tr(a0 + s * 2048, a0 + s * 2048 + 1024); }, [&](int s, bf16x8 f) { mma32(cs, f, as_frag(onesu)); });
        if (t32 == 0) {
#pragma unroll
            for (int q = 0; q < 4; ++q) *(LAS f32x4*)(tab + 32 * w + 8 * q + 4 * hl) = (f32x4){cs[4 * q], cs[4 * q + 1], cs[4 * q + 2], cs[4 * q + 3]};
        }
    }
    if (w == 0) {
        flag_wait(ws, FL_X2 + PP * 128, 14, tag, lane);
        if (js == 0 && lane == 0) flag_set(ws, FL_E + PP * 128, tag);
    }
    bar();
    const rsrc_t rG = mk_rsrc(ws + G_OFF, G_BYTES);
    const unsigned fx4 = FL_X3 + T32 * 128 + 7 * kh * 4;
    gu32* pA = (gu32*)(ws + fx4) + (lane < 7 ? lane : 0);
    unsigned fA = 0;
    const unsigned fx3 = FL_X3 + (4 * PP + w) * 128 + js * 4, fx3b = fx3 + 56;
    constexpr int PD4 = 10;
    u32x4 gf[56];
    const unsigned gso = (T32 * 112 + 56 * kh) * 1024;
    auto g_so = [&](int s) { return (unsigned)((4 * kst(s >> 2) + (s & 3)) * 1024); };
    {
        const int TP = 4 * PP + w;
        u32x2 sv[7];
#pragma unroll
        for (int q = 0; q < 7; ++q) sv[q] = ld8<SC1>(rSt, t32 * 8 + hl * 256, (TP * 14 + 2 * q) * 256);
        constexpr int PD3 = 10;
        u32x4 hf[28];
        const unsigned hso = TP * 28 * 1024;
#pragma unroll
        for (int s = 0; s < PD3; ++s) hf[s] = ld16<SC1>(rH, lane * 16, hso + s * 1024);
        sb();
        f32x16 a3[4] = {zero16(), zero16(), zero16(), zero16()};
        LAS char* wA = smem + L_W1A; LAS char* wB = smem + L_W1B;
        frag_pipe<56, 4>([&](int n) { LAS char* a = wA + (ird ^ ((n & 1) << 6)) + (n >> 1) * 2048; return lds_tr(a, a + 1024); },
                         [&](int n, bf16x8 f) { const int s = n >> 1; if ((n & 1) == 0 && s + PD3 < 28) hf[s + PD3] = ld16<SC1>(rH, lane * 16, hso + (s + PD3) * 1024); mma32(a3[n & 1], f, as_frag(hf[s])); });
        float s1 = 0.f, s2 = 0.f;
#pragma unroll
        for (int q = 0; q < 7; ++q) { const unsigned sa = sv[q].x, sb_ = sv[q].y; s1 += u2f(sa); s2 += u2f(sb_); }
        s1 = sum32(s1); s2 = sum32(s2);
        const float mu = s1 * (1.0f / D_);
        const float rstd = __builtin_amdgcn_rsqf(fmaxf(s2 * (1.0f / D_) - mu * mu, 0.0f) + 1e-5f);
        const f32x2 rs2 = {rstd, rstd}, nm2 = {-mu * rstd, -mu * rstd};
        const f32x2 c02 = {-2.0f * 0.7978845608028654f * 1.4426950408889634f, -2.0f * 0.7978845608028654f * 1.4426950408889634f};
        const f32x2 c12 = c02 * 0.044715f, one2 = {1.0f, 1.0f};
        bar();
        f32x4 tcs_[16], tbb_[16];
        unsigned gq[8];
        LAS float* tabl = tab + 4 * hl;
        asm volatile("" : "+v"(tabl));
        auto gelu_ld = [&](int m) { const int i = m >> 2, q = m & 3; tcs_[m] = *(LAS f32x4*)(tabl + 32 * i + 8 * q); tbb_[m] = *(LAS f32x4*)(tabl + 128 + 32 * i + 8 * q); };
        f32x2 gy0[16], gy1[16], gd0[16], gd1[16];
        auto gelu_a = [&](int m) {
            const int i = m >> 2, q = m & 3;
            const f32x4 cs = tcs_[m], bb = tbb_[m];
            const f32x2 y0 = (f32x2){a3[i][4 * q], a3[i][4 * q + 1]} * rs2 + (nm2 * (f32x2){cs[0], cs[1]} + (f32x2){bb[0], bb[1]});
            const f32x2 y1 = (f32x2){a3[i][4 * q + 2], a3[i][4 * q + 3]} * rs2 + (nm2 * (f32x2){cs[2], cs[3]} + (f32x2){bb[2], bb[3]});
            gy0[m] = y0; gy1[m] = y1;
            gd0[m] = y0 * ((y0 * y0) * c12 + c02); gd1[m] = y1 * ((y1 * y1) * c12 + c02);
            asm volatile("" : "+v"(gd0[m]), "+v"(gd1[m]));
        };
        auto gelu_b = [&](int m) {
            gd0[m] = (f32x2){__builtin_amdgcn_exp2f(gd0[m][0]), __builtin_amdgcn_exp2f(gd0[m][1])} + one2;
            gd1[m] = (f32x2){__builtin_amdgcn_exp2f(gd1[m][0]), __builtin_amdgcn_exp2f(gd1[m][1])} + one2;
            asm volatile("" : "+v"(gd0[m]), "+v"(gd1[m]));
        };
        auto gelu_c = [&](int m) {
            const int i = m >> 2, q = m & 3;
            const f32x2 y0 = gy0[m] * (f32x2){__builtin_amdgcn_rcpf(gd0[m][0]), __builtin_amdgcn_rcpf(gd0[m][1])};
            const f32x2 y1 = gy1[m] * (f32x2){__builtin_amdgcn_rcpf(gd1[m][0]), __builtin_amdgcn_rcpf(gd1[m][1])};
            gq[2 * q] = pk(y0[0], y0[1]); gq[2 * q + 1] = pk(y1[0], y1[1]);
            if (q & 1) st16<SC1>((u32x4){gq[2 * q - 2], gq[2 * q - 1], gq[2 * q], gq[2 * q + 1]}, rG, lane * 16, (TP * 112 + 8 * js + 2 * i + (q >> 1)) * 1024);
            else asm volatile("" : "+v"(gq[2 * q]), "+v"(gq[2 * q + 1]));
        };
        auto gelu_do = [&](int m) { gelu_a(m); gelu_b(m); gelu_c(m); };
        frag_pipe<28, 4>([&](int s) { LAS char* a = wB + ird + s * 2048; return lds_tr(a, a + 1024); },
                         [&](int s, bf16x8 f) {
                             if (s % 3 == 0 && s / 3 < 8) gelu_ld(s / 3);
                             mma32(a3[2], f, as_frag(hf[s]));
                             if (s >= 2 && s < 26) { const int m = (s - 2) / 3, p = (s - 2) % 3; if (p == 0) gelu_a(m); else if (p == 1) gelu_b(m); else gelu_c(m); }
                         });
        frag_pipe<28, 4>([&](int s) { LAS char* a = wB + (ird ^ 64) + s * 2048; return lds_tr(a, a + 1024); },
                         [&](int s, bf16x8 f) {
                             if (s % 6 == 1 && s / 6 < 4) gelu_ld(8 + s / 6);
                             if (s >= 24) gelu_ld(12 + s - 24);
                             mma32(a3[3], f, as_frag(hf[s]));
                             if (s == 3) { wait_vm<0>(); if (lane == 0) flag_set(ws, fx3, tag); }
                             if (s == 21) fA = __hip_atomic_load(pA, __ATOMIC_RELAXED, __HIP_MEMORY_SCOPE_AGENT);
                             if (s % 6 == 3 && s / 6 < 4) gelu_a(8 + s / 6);
                             if (s % 6 == 4 && s / 6 < 4) gelu_b(8 + s / 6);
                             if (s % 6 == 5 && s / 6 < 4) gelu_c(8 + s / 6);
                         });
        bar();
#pragma unroll
        for (int q = 0; q < 4; ++q) gelu_do(12 + q);
        sb();
        if (!__all(fA == tag)) flag_wait(ws, fx4, 7, tag, lane);
        asm volatile("" ::: "memory");
#pragma unroll
        for (int s = 0; s < PD4; ++s) gf[s] = ld16<SC1>(rG, lane * 16, gso + g_so(s));
        wait_vm<PD4>();
        if (lane == 0) flag_set(ws, fx3b, tag);
    }
    u32x4 hres[2]; u32x2 b2v[4];
    {
        constexpr int PD = PD4;
        gu32* pB = (gu32*)(ws + fx4 + 56) + (lane < 7 ? lane : 0);
        unsigned fB = 0;
        bar();
        sb();
        f32x16 a4[2] = {zero16(), zero16()};
        LAS char* wbp = smem + kh * 8192;
        auto run = [&](auto S0, auto NS) {
            constexpr int s0 = decltype(S0)::v, ns = decltype(NS)::v;
            frag_pipe<2 * ns, 4>(
                [&](int n) { const int s = s0 + (n >> 1); LAS char* a = wbp + (((s >> 2) + 7) % 9) * W2SLOT + (s & 3) * 2048 + (ird ^ ((n & 1) << 6)); return lds_tr(a, a + 1024); },
                [&](int n, bf16x8 f) {
                    const int s = s0 + (n >> 1);
                    if ((n & 1) == 0 && s + PD == 26) fB = __hip_atomic_load(pB, __ATOMIC_RELAXED, __HIP_MEMORY_SCOPE_AGENT);
                    if ((n & 1) == 0 && s + PD == 28) { if (!__all(fB == tag)) flag_wait(ws, fx4 + 56, 7, tag, lane); asm volatile("" ::: "memory"); }
                    if ((n & 1) == 0 && s + PD < 56) gf[s + PD] = ld16<SC1>(rG, lane * 16, gso + g_so(s + PD));
                    mma32(a4[n & 1], f, as_frag(gf[s])); });
        };
        struct I0 { enum { v = 0 }; }; struct I8 { enum { v = 8 }; }; struct I12 { enum { v = 12 }; }; struct I16 { enum { v = 16 }; };
        struct I20 { enum { v = 20 }; }; struct I36 { enum { v = 36 }; }; struct I48 { enum { v = 48 }; };
        run(I0{}, I20{});
        {
            const rsrc_t rB2 = mk_rsrc(b2, D_ * 2);
            hres[0] = ld16<SC1>(rH, lane * 16, (T32 * 28 + 4 * mem + 2 * kh) * 1024);
            hres[1] = ld16<SC1>(rH, lane * 16, (T32 * 28 + 4 * mem + 2 * kh + 1) * 1024);
#pragma unroll
            for (int q = 0; q < 4; ++q) b2v[q] = ld8<0>(rB2, colb + hl * 8, q * 16);
        }
        bar();
        run(I20{}, I16{});
        bar();
        run(I36{}, I12{});
        bar();
        run(I48{}, I8{});
        const f32x16 keep = kh ? a4[1] : a4[0], give = kh ? a4[0] : a4[1];
        LAS char* red = smem + L_RED;
#pragma unroll
        for (int q = 0; q < 4; ++q) *(LAS f32x4*)(red + (w * 4 + q) * 1024 + lane * 16) = (f32x4){give[4 * q], give[4 * q + 1], give[4 * q + 2], give[4 * q + 3]};
        bar();
        const rsrc_t rOut = mk_rsrc(out, S_ * D_ * 2);
        LAS char* ot = smem + 3 * W2SLOT;
        const int orow = 32 * th + t32;
#pragma unroll
        for (int q = 0; q < 4; ++q) {
            const f32x4 r = *(LAS f32x4*)(red + ((w ^ 2) * 4 + q) * 1024 + lane * 16);
            const unsigned ha = hres[q >> 1][2 * (q & 1)], hb = hres[q >> 1][2 * (q & 1) + 1], ba = b2v[q].x, bb = b2v[q].y;
            const u32x2 ov = {pk(bf_lo(ha) + (keep[4 * q] + r[0] + bf_lo(ba)), bf_hi(ha) + (keep[4 * q + 1] + r[1] + bf_hi(ba))),
                              pk(bf_lo(hb) + (keep[4 * q + 2] + r[2] + bf_lo(bb)), bf_hi(hb) + (keep[4 * q + 3] + r[3] + bf_hi(bb)))};
            *(LAS u32x2*)(ot + orow * 128 + (((4 * kh + q) ^ (orow & 7)) * 16) + 8 * hl) = ov;
        }
        bar();
#pragma unroll
        for (int m = 0; m < 2; ++m) {
            const int row = 16 * w + 8 * m + (lane >> 3), c = lane & 7;
            const u32x4 v = *(LAS u32x4*)(ot + row * 128 + ((c ^ (row & 7)) * 16));
            st16<SC1>(v, rOut, (t0 + row) * 896 + mem * 128 + c * 16, 0);
        }
    }
}

extern "C" void kernel_launch(void* const* d_in, const int* in_sizes, int n_in, void* d_out, int out_size, void* d_ws, size_t ws_size, hipStream_t stream) {
    (void)in_sizes; (void)n_in; (void)out_size; (void)ws_size;
    (void)hipFuncSetAttribute((const void*)k_fused, hipFuncAttributeMaxDynamicSharedMemorySize, LDS_BYTES);
    k_fused<<<256, 512, LDS_BYTES, stream>>>((const char*)d_in[0], (const char*)d_in[1], (const char*)d_in[2], (const char*)d_in[4], (const char*)d_in[5],
                                            (const char*)d_in[6], (const char*)d_in[7], (char*)d_out, (char*)d_ws);
}
```
